```python
import torch

def _softmax(x, axis=-1):
    return torch.softmax(x, dim=axis)
def _gelu(x):
    return torch.nn.functional.gelu(x, approximate="tanh")
def _silu(x):
    return torch.nn.functional.silu(x)
def _sigmoid(x):
    return torch.sigmoid(x)
def _layernorm(x, axis=-1, eps=1e-5):
    m = x.mean(dim=axis, keepdim=True)
    v = x.var(dim=axis, keepdim=True, unbiased=False)
    return (x - m) / torch.sqrt(v + eps)
def _scatter_add(tgt, idx, src, axis=0):
    return tgt.clone().index_add_(axis, idx, src)
def _segment_sum(src, seg, n):
    out = torch.zeros((n,) + src.shape[1:], dtype=src.dtype)
    return out.index_add_(0, seg, src)
def _pad(x, pw):
    flat = tuple(v for pair in reversed(pw) for v in pair)
    return torch.nn.functional.pad(x, flat)

kernel_name = 'gated_mix__sliding_local_dilated__sliding_local_dilated__prompt'

def setup_inputs():

    _w = (lambda a: (lambda t: t.to(torch.bfloat16) if t.is_floating_point() else t)(torch.as_tensor(a)))
    def _A_setup():
        B, S, D = 1, 2048, 576
        return {
            "x": _w((lambda *s: torch.randn(*s, dtype=torch.bfloat16) * 0.05)(B, S, D)),
            "wqkv": _w((lambda *s: torch.randn(*s, dtype=torch.bfloat16) * 0.8)(D, 3 * D)),
            "wo": _w((lambda *s: torch.randn(*s, dtype=torch.bfloat16) * 0.05)(D, D)),
        }
    _A_in = _A_setup()

    def _B_setup():
        B, S, D = 1, 2048, 576
        return {
            "x": _w((lambda *s: torch.randn(*s, dtype=torch.bfloat16) * 0.05)(B, S, D)),
            "wqkv": _w((lambda *s: torch.randn(*s, dtype=torch.bfloat16) * 0.8)(D, 3 * D)),
            "wo": _w((lambda *s: torch.randn(*s, dtype=torch.bfloat16) * 0.05)(D, D)),
        }
    _B_in = _B_setup()

    return {
        "A_x": _A_in["x"],
        "A_wqkv": _A_in["wqkv"],
        "A_wo": _A_in["wo"],
        "B_x": _B_in["x"],
        "B_wqkv": _B_in["wqkv"],
        "B_wo": _B_in["wo"],
        "gate_w": _w((lambda *s: torch.randn(*s, dtype=torch.bfloat16) * 0.05)(576) * 16.0),
    }

def reference(A_x, A_wqkv, A_wo, B_x, B_wqkv, B_wo, gate_w):
    def _A_ref(x, wqkv, wo):
        B, S, D = x.shape
        H = 32; Dh = D // H
        W, dil = 8, 2
        qkv = (lambda x, *d: x.permute(*d))((x @ wqkv).reshape(B, S, 3, H, Dh), 2, 0, 3, 1, 4)
        q, k, v = qkv[0], qkv[1], qkv[2]
        scale = Dh ** -0.5
        pos = torch.arange(S)
        off = torch.arange(W) * dil
        idx = pos[:, None] - off[None, :]
        mask = idx >= 0
        idx_c = torch.where(mask, idx, 0).reshape(-1)
        kg = k[:, :, idx_c, :].reshape(B, H, S, W, Dh)
        vg = v[:, :, idx_c, :].reshape(B, H, S, W, Dh)
        logits = torch.einsum("bhsd,bhswd->bhsw", q, kg) * scale
        logits = torch.where(mask[None, None, :, :], logits, -1e9)
        attn = _softmax(logits, axis=-1)
        o = torch.einsum("bhsw,bhswd->bhsd", attn, vg)
        return (lambda x, *d: x.permute(*d))(o, 0, 2, 1, 3).reshape(B, S, D) @ wo

    def _B_ref(x, wqkv, wo):
        B, S, D = x.shape
        H = 32; Dh = D // H
        W, dil = 8, 2
        qkv = (lambda x, *d: x.permute(*d))((x @ wqkv).reshape(B, S, 3, H, Dh), 2, 0, 3, 1, 4)
        q, k, v = qkv[0], qkv[1], qkv[2]
        scale = Dh ** -0.5
        pos = torch.arange(S)
        off = torch.arange(W) * dil
        idx = pos[:, None] - off[None, :]
        mask = idx >= 0
        idx_c = torch.where(mask, idx, 0).reshape(-1)
        kg = k[:, :, idx_c, :].reshape(B, H, S, W, Dh)
        vg = v[:, :, idx_c, :].reshape(B, H, S, W, Dh)
        logits = torch.einsum("bhsd,bhswd->bhsw", q, kg) * scale
        logits = torch.where(mask[None, None, :, :], logits, -1e9)
        attn = _softmax(logits, axis=-1)
        o = torch.einsum("bhsw,bhswd->bhsd", attn, vg)
        return (lambda x, *d: x.permute(*d))(o, 0, 2, 1, 3).reshape(B, S, D) @ wo

    x = A_x
    g = _sigmoid((x * gate_w).sum(-1, keepdims=True))
    a = _A_ref(x, A_wqkv, A_wo)
    b = _B_ref(x, B_wqkv, B_wo)
    return g * a + (1 - g) * b
```

```cpp
#include <hip/hip_runtime.h>
#include <hip/hip_bf16.h>
typedef unsigned short u16;
__device__ __forceinline__ float bf2f(u16 v){ return __uint_as_float(((unsigned)v)<<16); }
__device__ __forceinline__ u16 f2bf(float f){ __hip_bfloat16 b = __float2bfloat16(f); return *reinterpret_cast<u16*>(&b); }
constexpr int S=2048, D=576, H=32, DH=18;
__global__ void k_qkv(const u16* x, const u16* wa, const u16* wb, u16* qkv){
  int n = blockIdx.x*256+threadIdx.x;
  int s = blockIdx.y;
  if(n>=3456) return;
  const u16* w = n<1728? wa: wb; int nn = n%1728;
  float acc=0;
  for(int k=0;k<D;k++) acc += bf2f(x[s*D+k])*bf2f(w[k*1728+nn]);
  qkv[s*3456+n]=f2bf(acc);
}
__global__ void k_attn(const u16* x, const u16* gw, const u16* qkv, u16* o){
  int s = blockIdx.x; int t = threadIdx.x;
  int br = t>>5, h = t&31;
  float g=0; for(int k=0;k<D;k++) g += bf2f(x[s*D+k])*bf2f(gw[k]);
  g = 1.f/(1.f+__expf(-g));
  float gs = br? 1.f-g : g;
  const u16* q = qkv + s*3456 + br*1728 + h*DH;
  float lg[8]; float m=-1e30f;
  for(int w=0;w<8;w++){ int p=s-2*w; float a=-1e9f; if(p>=0){ const u16* k=qkv+p*3456+br*1728+576+h*DH; a=0; for(int d=0;d<DH;d++) a+=bf2f(q[d])*bf2f(k[d]); a*=0.23570226039551584f;} lg[w]=a; m=fmaxf(m,a);}
  float sum=0; for(int w=0;w<8;w++){ lg[w]=__expf(lg[w]-m); sum+=lg[w]; }
  for(int d=0;d<DH;d++){ float a=0; for(int w=0;w<8;w++){ int p=s-2*w; if(p<0) p=0; a+=lg[w]*bf2f(qkv[p*3456+br*1728+1152+h*DH+d]); } o[s*1152+br*576+h*DH+d]=f2bf(a/sum*gs); }
}
__global__ void k_out(const u16* o, const u16* wa, const u16* wb, u16* out){
  int n = blockIdx.x*64+threadIdx.x; int s = blockIdx.y;
  float acc=0;
  for(int k=0;k<576;k++) acc += bf2f(o[s*1152+k])*bf2f(wa[k*576+n]);
  for(int k=0;k<576;k++) acc += bf2f(o[s*1152+576+k])*bf2f(wb[k*576+n]);
  out[s*576+n]=f2bf(acc);
}
extern "C" void kernel_launch(void* const* d_in, const int* in_sizes, int n_in, void* d_out, int out_size, void* d_ws, size_t ws_size, hipStream_t stream){
  const u16* x=(const u16*)d_in[0]; const u16* wqa=(const u16*)d_in[1]; const u16* woa=(const u16*)d_in[2];
  const u16* wqb=(const u16*)d_in[4]; const u16* wob=(const u16*)d_in[5]; const u16* gw=(const u16*)d_in[6];
  u16* qkv=(u16*)d_ws; u16* o=qkv+S*3456;
  k_qkv<<<dim3(14,S),256,0,stream>>>(x,wqa,wqb,qkv);
  k_attn<<<S,64,0,stream>>>(x,gw,qkv,o);
  k_out<<<dim3(9,S),64,0,stream>>>(o,woa,wob,(u16*)d_out);
}
```

```cpp
#include <hip/hip_runtime.h>
typedef unsigned short u16; typedef unsigned int u32; typedef unsigned long long u64;
typedef __attribute__((ext_vector_type(8))) __bf16 bf16x8;
typedef __attribute__((ext_vector_type(2))) __bf16 bf16x2;
typedef __attribute__((ext_vector_type(4))) float f32x4;
typedef __attribute__((ext_vector_type(2))) float f32x2;
typedef __attribute__((ext_vector_type(4))) short s16x4;
typedef __attribute__((ext_vector_type(4))) u32 u32x4;
typedef __attribute__((ext_vector_type(2))) u32 u32x2;
typedef __attribute__((ext_vector_type(10))) u32 u32x10;
#define LAS __attribute__((address_space(3)))
#define DI __device__ __forceinline__
#define SBAR() __builtin_amdgcn_sched_barrier(0)
#define PIN(x) asm volatile("" : "+v"(x))
template <int V> struct IC { static constexpr int value = V; };
template <int A, int B, class F> __device__ __forceinline__ void sfor(F&& f) { if constexpr (A < B) { f(IC<A>{}); sfor<A + 1, B>(f); } }
#define ROT 1
#define ROT2 1
#define PD 6
#define PRE 4

constexpr int WROW = 144, WPART = 9232, XSTG = 136 * 128, STG = 45184;
constexpr int S0 = 0, S1 = 73472, S2 = S1 + STG;
constexpr int LDS_BYTES = 163840;
static_assert(S2 + STG == LDS_BYTES, "lds map");
constexpr int KQB = 4608;
constexpr int XKQ = 73728, XV = XKQ + 9 * KQB, MRED = 147456;
static_assert(XV + 5 * 4608 <= S2 + XSTG + 4608, "exchange area");
constexpr u32 OP_BYTES = 128u * 36u * 1024u;
constexpr u32 GATE_OFF = OP_BYTES;
constexpr u32 NON_OFF = GATE_OFF + 8192;
constexpr u32 INB_OFF = NON_OFF + 1024;
constexpr u32 WS_BYTES = INB_OFF + 256 * 512;
constexpr float CEXP = 0.23570226039551584f * 1.4426950408889634f;
constexpr int AUX_SC1 = 16;

DI u32 pk(float a, float b) { f32x2 v = {a, b}; bf16x2 r = __builtin_convertvector(v, bf16x2); return __builtin_bit_cast(u32, r); }
DI f32x4 mfma(u32x4 a, u32x4 b, f32x4 c) { return __builtin_amdgcn_mfma_f32_16x16x32_bf16(__builtin_bit_cast(bf16x8, a), __builtin_bit_cast(bf16x8, b), c, 0, 0, 0); }
template <bool BI> DI void mfma_acc(f32x4& c, u32x4 a, u32x4 b) {
  if constexpr (BI) c = mfma(a, b, c);
  else asm volatile("v_mfma_f32_16x16x32_bf16 %0, %1, %2, %0" : "+v"(c) : "v"(a), "v"(b));
}
DI u32x2 trd(u32 a) { return __builtin_bit_cast(u32x2, __builtin_amdgcn_ds_read_tr16_b64_v4i16((LAS s16x4*)(uintptr_t)a)); }
DI u32x4 lrd128(u32 a) { return *(LAS u32x4*)(uintptr_t)a; }
DI u32x2 lrd64(u32 a) { return *(LAS u32x2*)(uintptr_t)a; }
DI void lwr64(u32 a, f32x4 v) { *(LAS u32x2*)(uintptr_t)a = (u32x2){pk(v[0], v[1]), pk(v[2], v[3])}; }
DI void lwr128(u32 a, f32x4 v) { *(LAS f32x4*)(uintptr_t)a = v; }
DI void dma16(__amdgpu_buffer_rsrc_t rs, u32 voff, u32 soff, u32 ldsdst) {
  asm volatile("s_mov_b32 m0, %0\n\ts_nop 0\n\tbuffer_load_dwordx4 %1, %2, %3 offen lds" :: "s"(ldsdst), "v"(voff), "s"(rs), "s"(soff) : "memory");
}
template <int N> DI void wait_vm() { asm volatile("s_waitcnt vmcnt(%0)" :: "i"(N) : "memory"); }
DI void bar() { asm volatile("s_barrier" ::: "memory"); }
DI void lgkm0() { asm volatile("s_waitcnt lgkmcnt(0)" ::: "memory"); }
#define STAMP(i)
#define RSTAMP(i)
#define STAMP_DECL
#define STAMP_OUT()
DI float dot2(u32 a, u32 b, float c) { return __builtin_amdgcn_fdot2_f32_bf16(__builtin_bit_cast(bf16x2, a), __builtin_bit_cast(bf16x2, b), c, false); }
DI float fmx(float a, float b) { return __builtin_elementwise_maximum(a, b); }
DI void sw32(float& a, float& b) { auto r = __builtin_amdgcn_permlane32_swap(__float_as_uint(a), __float_as_uint(b), false, false); a = __uint_as_float(r[0]); b = __uint_as_float(r[1]); }
DI void sw16(float& a, float& b) { auto r = __builtin_amdgcn_permlane16_swap(__float_as_uint(a), __float_as_uint(b), false, false); a = __uint_as_float(r[0]); b = __uint_as_float(r[1]); }
DI int nxt9(int s) { return s == 8 ? 0 : s + 1; }
DI int nxtslot(int s) { return s == S0 ? S1 : (s == S1 ? S2 : S0); }
DI __amdgpu_buffer_rsrc_t mkrs(const void* p, u32 n) { return __builtin_amdgcn_make_buffer_rsrc((void*)p, 0, n, 0x00020000); }

DI void wo_block(const u16* woa, const u16* wob, u32 voR, u32 voT, u32 lds0, int hf, int idx, int rot) {
  const bool br = idx >= 9;
  int ks = 9 * hf + (br ? idx - 9 : idx) + rot; ks = ks >= 18 ? ks - 18 : ks;
  const int kq = ks >= 9, kk = kq ? ks - 9 : ks;
  const bool tl = kk == 8;
  const u32 so = (u32)((kq ? 288 : 0) + (tl ? 64 : 72 * (kk >> 1) + 32 * (kk & 1))) * 1152u;
  const u32 dst = lds0 + hf * 73728 + idx * 4096;
  const __amdgpu_buffer_rsrc_t rs = mkrs(br ? wob : woa, 576 * 1152);
  const u32 vo = tl ? voT : voR;
  dma16(rs, vo, so, dst);
  dma16(rs, vo, so + (tl ? 144u : 8u) * 1152u, dst + 1024);
  dma16(rs, vo, so + (tl ? 4u : 16u) * 1152u, dst + 2048);
  dma16(rs, vo, so + (tl ? 148u : 24u) * 1152u, dst + 3072);
}

__global__ __launch_bounds__(512, 2) void fused(const u16* __restrict__ x, const u16* __restrict__ wqa, const u16* __restrict__ woa,
                                                const u16* __restrict__ wqb, const u16* __restrict__ wob, const u16* __restrict__ gw,
                                                u16* __restrict__ out, char* __restrict__ ws) {
  extern __shared__ __attribute__((aligned(16))) char lds[];
  asm volatile("" :: "s"(x), "s"(wqa), "s"(woa), "s"(wqb), "s"(wob), "s"(gw), "s"(out), "s"(ws));
  const int tid = threadIdx.x, lane = tid & 63;
  const int wid = __builtin_amdgcn_readfirstlane(tid >> 6);
  STAMP_DECL; RSTAMP(0); STAMP(1);
  const int b = blockIdx.x, xcd = b & 7, bi = b >> 3;
  const int cp = 8 * (xcd & 1) + (bi & 7), hg = 4 * (xcd >> 1) + (bi >> 3);
  const int par = cp & 1, chunk = cp >> 1;
  const int hq = hg & 7;
  int s0 = ROT ? (bi & 7) + 2 * (bi >> 3) : 0; s0 = s0 >= 9 ? s0 - 9 : s0;
  const int sg = cp >> 2, mi = 16 * (hg >> 2) + 4 * (cp & 3) + (hg & 3);
  const int rg = (mi * 57) >> 9, ct = mi - 9 * rg;
  const int rot2 = ROT2 ? 2 * ct : 0;
  const int c = lane & 15, g = lane >> 4, q = (lane >> 2) & 3, p = lane & 3;
  const int r8 = lane >> 3, sp8 = lane & 7;
  const u32 lds0 = (u32)(uintptr_t)(LAS char*)lds;
  const __amdgpu_buffer_rsrc_t rsS = mkrs(ws, WS_BYTES);
  const f32x4 z4 = {0.f, 0.f, 0.f, 0.f};
  u32 nonce = 0, vn = 0;
  f32x4 bias; u32 mq[5][2], mP; u32 voR, voT;
  auto setup = [&]() {
    const float NINF = -__builtin_inff();
    const bool first = (chunk == 0 && wid == 0);
#pragma unroll
    for (int r = 0; r < 4; ++r) {
      const int i = 4 * g + r;
      const bool ok = (i <= c && i >= c - 7) || (i >= c + 9 && !first);
      bias[r] = ok ? 0.f : NINF;
    }
    const u32 lo = c < 8 ? ~0u : 0u;
    const u32 pr[5] = {~0u, g == 0 ? ~0u : 0u, g >= 1 ? ~0u : 0u, g <= 1 ? ~0u : 0u, g >= 2 ? ~0u : 0u};
#pragma unroll
    for (int i = 0; i < 5; ++i) { mq[i][0] = pr[i] & lo; mq[i][1] = pr[i] & ~lo; PIN(mq[i][0]); PIN(mq[i][1]); }
    mP = ~mq[4][0]; PIN(mP);
    PIN(bias);
    const u32 woc = (u32)(ct * 128 + ((sp8 ^ (2 * ((r8 >> 1) & 3))) * 16));
    voR = (u32)r8 * 1152u + woc; voT = (u32)(72 * (r8 >> 2) + (r8 & 3)) * 1152u + woc; PIN(voR); PIN(voT);
  };

  if (wid >= 4) {
    if (wid < 7) {
      const int a = wid - 4;
      const __amdgpu_buffer_rsrc_t rsX = mkrs(x, 2048 * 1152);
      const __amdgpu_buffer_rsrc_t rsW = mkrs((hg >> 3) ? wqb : wqa, 576 * 3456);
      const u32 vA = ((u32)(2 * (128 * chunk + r8) + par) * 1152u + (u32)(sp8 ^ (r8 >> 1)) * 16u) ^ (u32)((a & 1) * 64), vB = vA ^ 64u;
      int jh = 128 * chunk - 8 + r8; jh = jh < 0 ? 0 : jh;
      const u32 v5 = a == 0 ? vB + 15u * 18432u : (u32)(2 * jh + par) * 1152u + (u32)(sp8 ^ (r8 & 6)) * 16u;
      const u32 xso = a * 18432, xd = lds0 + a * 1024, xd5 = lds0 + (a == 0 ? 15 : 16) * 1024;
      auto xlo = [&](int st, int sl) {
        dma16(rsX, vA, st * 128 + xso, xd + sl); dma16(rsX, vB, st * 128 + xso + 3 * 18432, xd + sl + 3072); dma16(rsX, vA, st * 128 + xso + 6 * 18432, xd + sl + 6144);
      };
      auto xhi = [&](int st, int sl) {
        dma16(rsX, vB, st * 128 + xso + 9 * 18432, xd + sl + 9216); dma16(rsX, vA, st * 128 + xso + 12 * 18432, xd + sl + 12288); dma16(rsX, v5, st * 128, xd5 + sl);
      };
      u32 vw[9];
      const u32 wso = a * 1152 + hq * 144, wd = lds0 + XSTG + a * WPART;
      xlo(s0, S0); xhi(s0, S0);
      SBAR();
#pragma unroll
      for (int i = 0; i < 9; ++i) {
        const u32 ci = (u32)(64 * i) + lane;
        const u32 pos = __umul24(ci, 7282u) >> 16, ch = ci - __umul24(pos, 9u);
        const u32 row = (pos & ~7u) | ((pos & 1u) << 2) | ((pos & 7u) >> 1);
        vw[i] = __umul24(row, 3456u) + ch * 16u;
        dma16(rsW, vw[i], s0 * (64 * 3456) + wso, wd + i * 1024);
        SBAR();
      }
      int st = nxt9(s0), sl = S1;
      xlo(st, sl);
      setup();
#pragma unroll 1
      for (int it = 0; it < 8; ++it) {
        wait_vm<7>(); bar();
#pragma unroll
        for (int i = 0; i < 5; ++i) dma16(rsW, vw[i], st * (64 * 3456) + wso, wd + sl + i * 1024);
        xhi(st, sl);
        wait_vm<11>(); bar();
#pragma unroll
        for (int i = 5; i < 9; ++i) dma16(rsW, vw[i], st * (64 * 3456) + wso, wd + sl + i * 1024);
        st = nxt9(st); sl = nxtslot(sl);
        if (it < 7) xlo(st, sl);
      }
      wait_vm<4>();
    } else {
      const u32x2 inb0 = __builtin_amdgcn_raw_buffer_load_b64(rsS, INB_OFF + (u32)(sg * 64 + mi) * 512u + lane * 8u, 0, AUX_SC1);
      const u32 non0 = __builtin_amdgcn_raw_buffer_load_b32(rsS, NON_OFF + (u32)(sg * 64 + mi) * 4u, 0, AUX_SC1);
      setup();
      bar(); bar();
      nonce = __builtin_amdgcn_readfirstlane(non0) + 1u;
      while (__ballot(inb0[0] == nonce) != 0ull) ++nonce;
      if (lane == 0) __builtin_amdgcn_raw_buffer_store_b32(nonce, rsS, NON_OFF + (u32)(sg * 64 + mi) * 4u, 0, AUX_SC1);
      bar();
      const int jl = 8 * hg + r8;
      const u16* xr = x + (size_t)(2 * (128 * chunk + jl) + par) * 576;
      u32x4 xv[9], gv[9];
#pragma unroll
      for (int i = 0; i < 9; ++i) { xv[i] = *(const u32x4*)(xr + (i * 8 + sp8) * 8); gv[i] = *(const u32x4*)(gw + (i * 8 + sp8) * 8); }
      bar(); bar(); bar(); bar();
      float a0 = 0.f, a1 = 0.f;
#pragma unroll
      for (int i = 0; i < 9; ++i) {
        a0 = dot2(xv[i][0], gv[i][0], a0); a1 = dot2(xv[i][1], gv[i][1], a1);
        a0 = dot2(xv[i][2], gv[i][2], a0); a1 = dot2(xv[i][3], gv[i][3], a1);
      }
      float a = a0 + a1; PIN(a);
      bar();
      a += __shfl_xor(a, 1); a += __shfl_xor(a, 2); a += __shfl_xor(a, 4);
      const float gt = __builtin_amdgcn_rcpf(1.f + __builtin_amdgcn_exp2f(-1.4426950408889634f * a));
      if (sp8 == 0) __builtin_amdgcn_raw_buffer_store_b32(__float_as_uint(gt), rsS, GATE_OFF + (u32)(cp * 128 + jl) * 4u, 0, AUX_SC1);
      for (int i = 0; i < 7; ++i) bar();
      vn = __builtin_amdgcn_raw_buffer_load_b32(rsS, NON_OFF + (u32)(sg * 64 + lane) * 4u, 0, AUX_SC1);
      bar();
    }
    bar();
#pragma unroll 1
    for (int idx = wid - 4; idx < 18; idx += 4) {
      wo_block(woa, wob, voR, voT, lds0, 0, idx, rot2);
      if (idx >= 4 && idx < 8) { wait_vm<8>(); bar(); }
    }
    bar();
  } else {
    const int w = wid;
    const u32 rb = lds0 + XSTG + (8 * g + 2 * q) * WROW + 8 * p;
    u32 B[4], BM, BV, XB[2], HB[2];
#pragma unroll
    for (int i = 0; i < 4; ++i) B[i] = rb + 32 * (i ^ w);
    BM = rb + (p >= 2 ? WPART + 112 : 128); BV = rb + (p >= 2 ? WPART + 112 : 2 * WPART + 128);
    XB[0] = lds0 + (32 * w + c) * 128 + ((g ^ (c >> 1)) * 16); XB[1] = XB[0] ^ 64;
    HB[0] = lds0 + (128 + (c & 7)) * 128 + ((g ^ (c & 6)) * 16); HB[1] = HB[0] ^ 64;
    u32 EK[4], EV[4], EM, EH;
#pragma unroll
    for (int i = 0; i < 4; ++i) {
      const int t = i ^ w;
      EK[i] = lds0 + XKQ + (2 * w + 1) * KQB + (t >> 1) * 1024 + lane * 16 + (t & 1) * 8;
      EV[i] = lds0 + XV + t * 4608 + (2 * w + 1) * 512 + lane * 8;
      PIN(EK[i]); PIN(EV[i]);
    }
    EM = lds0 + XKQ + (2 * w + 1) * KQB + 4096 + (lane & 31) * 16 + (g < 2 ? 8 : 0); PIN(EM);
    EH = lds0 + XKQ + 4096 + (4 * g + w + 16 * ((c & 7) >> 2)) * 16 + (c & 3) * 2; PIN(EH);
    setup();
    f32x4 aQ[2][4], aK[2][4], aM[2], aV[2][5], hK, hV, hV4;
#pragma unroll
    for (int t = 0; t < 2; ++t) { aM[t] = z4; aV[t][4] = z4; PIN(aM[t]); PIN(aV[t][4]);
#pragma unroll
      for (int i = 0; i < 4; ++i) { aQ[t][i] = z4; aK[t][i] = z4; aV[t][i] = z4; PIN(aQ[t][i]); PIN(aK[t][i]); PIN(aV[t][i]); } }
    hK = z4; hV = z4; hV4 = z4; PIN(hK); PIN(hV); PIN(hV4);
    PIN(BM); PIN(BV); PIN(XB[0]); PIN(XB[1]); PIN(HB[0]); PIN(HB[1]);
    __builtin_amdgcn_s_waitcnt(0xC07F);
    u32x2 wlo[7], whi[7]; u32x4 xf[2][3];
    auto faddr = [&](auto NC) -> u32 {
      constexpr int n = decltype(NC)::value; constexpr int f = n % 14, ko = (n / 14) * 32 * WROW;
      if constexpr (f < 4) return B[f] + ko;
      else if constexpr (f < 8) return B[f - 4] + ko + WPART;
      else if constexpr (f == 8) return BM + ko;
      else if constexpr (f < 13) return B[f - 9] + ko + 2 * WPART;
      else return BV + ko;
    };
    auto xw = [&](auto FC, auto TC) {
      constexpr int f = decltype(FC)::value, t = decltype(TC)::value;
      if constexpr (t < 2) {
        if constexpr (f < 4) lwr64(EK[f] + t * KQB + 2048, aQ[t][f]);
        else if constexpr (f < 8) lwr64(EK[f - 4] + t * KQB, aK[t][f - 4]);
        else if constexpr (f == 8) lwr64(EM + t * KQB, aM[t]);
        else if constexpr (f < 13) lwr64(EV[f - 9] + t * 512, aV[t][f - 9]);
        else lwr64(lds0 + XV + 4 * 4608 + lane * 8 + (2 * w + 1 + t) * 512, aV[t][4]);
      } else {
        if constexpr (f == 4) lwr64(EK[0] - (2 * w + 1) * KQB, hK);
        if constexpr (f == 9) lwr64(EV[0] - (2 * w + 1) * 512, hV);
      }
    };
    STAMP(2);
    bar();
    STAMP(3);
    xf[0][0] = lrd128(XB[0]); xf[0][1] = lrd128(XB[0] + 2048);
    sfor<0, PD>([&](auto NC) { constexpr int n = decltype(NC)::value; const u32 a = faddr(NC); wlo[n % 7] = trd(a); whi[n % 7] = trd(a + WROW); if constexpr (n == 1) xf[0][2] = lrd128(HB[0]); SBAR(); });
    auto stage = [&](auto LC, int d) {
      constexpr bool LAST = decltype(LC)::value;
      sfor<0, 28>([&](auto NC) {
        constexpr int n = decltype(NC)::value;
        constexpr int f = n % 14, k2 = n / 14, nn = (n + PD) % 28, kn = nn / 14;
        constexpr bool RD = !(LAST && n + PD >= 28), BI = LAST && k2 == 1;
        constexpr int fx = f - 2;
        if constexpr (nn == 0 || nn == 14) { SBAR(); bar(); SBAR(); }
        const u32x4 wv = {wlo[n % 7][0], wlo[n % 7][1], whi[n % 7][0], whi[n % 7][1]};
        const u32x4 x0 = xf[k2][0], x1 = xf[k2][1], xh = xf[k2][2];
        const u32 a = faddr(IC<nn>{});
        if constexpr (f < 4) mfma_acc<BI>(aQ[0][f], wv, x0); else if constexpr (f < 8) mfma_acc<BI>(aK[0][f - 4], wv, x0);
        else if constexpr (f == 8) mfma_acc<BI>(aM[0], wv, x0); else mfma_acc<BI>(aV[0][f - 9], x0, wv);
        SBAR();
        if constexpr (RD) wlo[nn % 7] = trd(a);
        if constexpr (BI && fx >= 0) xw(IC<(fx >= 0 ? fx : 0)>{}, IC<0>{});
        SBAR();
        if constexpr (f < 4) mfma_acc<BI>(aQ[1][f], wv, x1); else if constexpr (f < 8) mfma_acc<BI>(aK[1][f - 4], wv, x1);
        else if constexpr (f == 8) mfma_acc<BI>(aM[1], wv, x1); else mfma_acc<BI>(aV[1][f - 9], x1, wv);
        SBAR();
        if constexpr (RD) whi[nn % 7] = trd(a + WROW);
        if constexpr (BI && fx >= 0) { xw(IC<(fx >= 0 ? fx : 0)>{}, IC<1>{}); xw(IC<(fx >= 0 ? fx : 0)>{}, IC<2>{}); }
        if constexpr (!LAST) {
          if constexpr (nn >= 23 && nn <= 26) { B[nn - 23] += d; PIN(B[nn - 23]); }
          if constexpr (nn == 22) { BM += d; PIN(BM); }
          if constexpr (nn == 27) { BV += d; PIN(BV); }
          if constexpr (nn == 7) { XB[0] += d; PIN(XB[0]); }
          if constexpr (nn == 8) { HB[0] += d; PIN(HB[0]); }
          if constexpr (nn == 20) { XB[1] += d; PIN(XB[1]); }
          if constexpr (nn == 21) { HB[1] += d; PIN(HB[1]); }
        }
        SBAR();
        if constexpr (f == 4) mfma_acc<BI>(hK, wv, xh);
        if constexpr (f == 9) mfma_acc<BI>(hV, xh, wv);
        if constexpr (f == 13) mfma_acc<BI>(hV4, xh, wv);
        SBAR();
        if constexpr (RD) {
          if constexpr (nn % 14 == 1) xf[kn][0] = lrd128(XB[kn]);
          if constexpr (nn % 14 == 2) xf[kn][1] = lrd128(XB[kn] + 2048);
          if constexpr (nn % 14 == 5) xf[kn][2] = lrd128(HB[kn]);
        }
        SBAR();
      });
    };
    int sl = S0;
#pragma unroll 1
    for (int it = 0; it < 8; ++it) { const int sn = nxtslot(sl), d = sn - sl; sl = sn; stage(IC<0>{}, d); }
    STAMP(4);
    stage(IC<1>{}, 0);
    xw(IC<12>{}, IC<0>{}); xw(IC<12>{}, IC<1>{}); xw(IC<13>{}, IC<0>{}); xw(IC<13>{}, IC<1>{});
    lwr64(lds0 + XV + 4 * 4608 + lane * 8, hV4);
    {
      const float hv = (w & 2) ? ((w & 1) ? hV4[3] : hV4[2]) : ((w & 1) ? hV4[1] : hV4[0]);
      if (c >= 8) *(LAS u16*)(uintptr_t)EH = (u16)(pk(hv, 0.f) & 0xffffu);
    }
    lgkm0();
  }
  STAMP(5);
  bar();
  STAMP(6);
  {
    const u32 kq = lds0 + XKQ + (wid + 1) * KQB + lane * 16;
    const u32 ks = kq - (c >= 8 ? KQB : 0);
    const u32 fix = g >= 2 ? 512 : 0;
    const u32 va = lds0 + XV + wid * 512 + lane * 8;
    u32x10 Ks, Kc; u32 Q[10];
    { const u32x4 a = lrd128(kq + 2048), b0 = lrd128(ks), c0 = lrd128(kq), a2 = lrd128(kq + 3072), b1 = lrd128(ks + 1024), c1 = lrd128(kq + 1024);
      const u32x4 tq = lrd128(kq - fix + 4096); const u32x2 tp = lrd64(ks - fix + 4096);
#pragma unroll
      for (int i = 0; i < 4; ++i) { Q[i] = a[i]; Q[4 + i] = a2[i]; Ks[i] = b0[i]; Ks[4 + i] = b1[i]; Kc[i] = c0[i]; Kc[4 + i] = c1[i]; }
      Kc[8] = tq[0]; Kc[9] = tq[1]; Q[8] = tq[2]; Q[9] = tq[3]; Ks[8] = tp[0]; Ks[9] = tp[1]; }
    u32x4 Vo[5];
#pragma unroll
    for (int t = 0; t < 5; ++t) { const u32x2 a = lrd64(va + t * 4608), bb = lrd64(va + t * 4608 + 512); Vo[t] = (u32x4){a[0], a[1], bb[0], bb[1]}; }
    PIN(Ks); PIN(Kc);
    f32x4 sc[4]; float mx[4];
    sfor<0, 4>([&](auto HC) {
      constexpr int h = decltype(HC)::value;
      constexpr int ia0 = h == 0 ? 0 : (h == 3 ? 4 : 2), ia1 = h <= 1 ? 0 : 2, ib0 = h <= 1 ? 1 : 3, ib1 = h == 0 ? -1 : (h == 3 ? 3 : 1);
      u32x4 qo[2];
#pragma unroll
      for (int s = 0; s < 2; ++s) {
        qo[s][0] = Q[2 * h] & mq[ia0][s]; qo[s][1] = Q[2 * h + 1] & mq[ia1][s]; qo[s][2] = Q[2 * h + 2] & mq[ib0][s];
        if constexpr (ib1 >= 0) qo[s][3] = Q[2 * h + 3] & mq[ib1 >= 0 ? ib1 : 0][s]; else qo[s][3] = 0u;
      }
      const u32x4 k0 = __builtin_shufflevector(Ks, Ks, 2 * h, 2 * h + 1, 2 * h + 2, 2 * h + 3);
      const u32x4 k1 = __builtin_shufflevector(Kc, Kc, 2 * h, 2 * h + 1, 2 * h + 2, 2 * h + 3);
      sc[h] = mfma(k1, qo[1], mfma(k0, qo[0], bias));
    });
#pragma unroll
    for (int h = 0; h < 4; ++h) mx[h] = fmx(fmx(fmx(sc[h][0], sc[h][1]), sc[h][2]), sc[h][3]);
    { sw32(mx[0], mx[1]); float m01 = fmx(mx[0], mx[1]); sw32(mx[2], mx[3]); float m23 = fmx(mx[2], mx[3]);
      sw16(m01, m23); float R = fmx(m01, m23) * CEXP;
      float a = R, bb = R; sw16(a, bb); mx[0] = a; mx[1] = a; sw32(mx[0], mx[1]); mx[2] = bb; mx[3] = bb; sw32(mx[2], mx[3]); }
    f32x4 olo[4], ohi[4]; float inv[4];
    const u32x4 ones = {0x3f803f80u, 0x3f803f80u, 0x3f803f80u, 0x3f803f80u};
    sfor<0, 4>([&](auto HC) {
      constexpr int h = decltype(HC)::value;
      float e[4];
#pragma unroll
      for (int r = 0; r < 4; ++r) e[r] = __builtin_amdgcn_exp2f(__builtin_fmaf(sc[h][r], CEXP, -mx[h]));
      const u32 p0 = pk(e[0], e[1]), p1 = pk(e[2], e[3]);
      const u32x4 po = {p0 & mq[4][0], p1 & mq[4][0], p0 & mP, p1 & mP};
      const f32x4 sm = mfma(ones, po, z4);
      olo[h] = mfma(Vo[h], po, z4); ohi[h] = mfma(Vo[h + 1], po, z4);
      inv[h] = __builtin_amdgcn_rcpf(sm[0]);
    });
    u32 op[10];
#pragma unroll
    for (int i = 0; i < 5; ++i)
#pragma unroll
      for (int e = 0; e < 2; ++e) {
        float v0, v1, iv;
        if (i == 0) { v0 = olo[0][2 * e]; v1 = olo[0][2 * e + 1]; iv = inv[0]; }
        else if (i == 4) { v0 = ohi[3][2 * e]; v1 = ohi[3][2 * e + 1]; iv = inv[3]; }
        else { const bool s = 2 * g + e < i; v0 = s ? ohi[i - 1][2 * e] : olo[i][2 * e]; v1 = s ? ohi[i - 1][2 * e + 1] : olo[i][2 * e + 1]; iv = s ? inv[i - 1] : inv[i]; }
        op[2 * i + e] = pk(v0 * iv, v1 * iv);
      }
    const u32 ob = (u32)((cp * 8 + wid) * 36 + 9 * (hg >> 2)) * 1024u;
    __builtin_amdgcn_raw_buffer_store_b128((u32x4){op[0], op[1], op[2], op[3]}, rsS, lane * 16u, ob + (hg & 3) * 2048u, AUX_SC1);
    __builtin_amdgcn_raw_buffer_store_b128((u32x4){op[4], op[5], op[6], op[7]}, rsS, lane * 16u + 1024u, ob + (hg & 3) * 2048u, AUX_SC1);
    if (g < 2) __builtin_amdgcn_raw_buffer_store_b64((u32x2){op[8], op[9]}, rsS, c * 16u + g * 8u, ob + 8192u + (hg & 3) * 256u, AUX_SC1);
  }
  const int br = wid >> 2, ti = wid & 3, ei = (rg < 4) ? (ti ^ (br << 1)) : -1;
  u32 WB[4];
  { const int pos = 4 * g + q, s = (pos >> 1) & 3;
#pragma unroll
    for (int i = 0; i < 4; ++i) { WB[i] = lds0 + br * 36864 + pos * 128 + (((i ^ (2 * br)) ^ s) * 32) + p * 8; PIN(WB[i]); } }
  u32 myin = INB_OFF + (u32)(sg * 64 + mi) * 512u + lane * 8u, dst = INB_OFF + (u32)(sg * 64 + lane) * 512u + mi * 8u;
  PIN(myin); PIN(dst);
  STAMP(7);
  wait_vm<0>();
  STAMP(8);
  bar();
  STAMP(9);
  u32x2 flo[PRE + 2][4], fhi[PRE + 2][4];
  if (wid == 7) {
    __builtin_amdgcn_raw_buffer_store_b64((u32x2){vn, nonce}, rsS, dst, 0, AUX_SC1);
  } else if (wid < 6) {
#pragma unroll 1
    for (int idx = wid; idx < 18; idx += 6) wo_block(woa, wob, voR, voT, lds0, 1, idx, rot2);
  }
  sfor<0, PRE + 1>([&](auto KC) { constexpr int k = decltype(KC)::value;
#pragma unroll
    for (int i = 0; i < 4; ++i) { flo[k][i] = trd(WB[i] + k * 4096); fhi[k][i] = trd(WB[i] + k * 4096 + 2048); } });
  SBAR();
  if (wid == 7) {
#pragma unroll 1
    for (int spin = 0; spin < (1 << 17); ++spin) {
      const u32x2 v = __builtin_amdgcn_raw_buffer_load_b64(rsS, myin, 0, AUX_SC1);
      const bool ok = v[0] == nonce;
      if (ok && v[1] != vn) { vn = v[1]; __builtin_amdgcn_raw_buffer_store_b64((u32x2){vn, nonce}, rsS, dst, 0, AUX_SC1); }
      if (__ballot(ok) == ~0ull) break;
      __builtin_amdgcn_s_sleep(1);
    }
  }
  asm volatile("s_waitcnt vmcnt(0) lgkmcnt(0)" ::: "memory");
  STAMP(10);
  bar();
  STAMP(11);
  if (rg < 7) {
    const int TT = 32 * sg + 4 * rg + ti, TX = 32 * sg + 28 + rg;
    u32x4 of[18], ef[5];
    const u32 ob = (u32)(TT * 36 + 18 * br) * 1024u;
    const int es = ei == 0 ? 0 : (ei == 1 ? 5 : (ei == 2 ? 10 : 14));
    const u32 eb = ei >= 0 ? (u32)(TX * 36 + 18 * br) * 1024u : 0xf0000000u;
    const u32 l16 = lane * 16u;
    sfor<0, 18>([&](auto KC) { constexpr int k = decltype(KC)::value;
      { int ks = k + rot2; ks = ks >= 18 ? ks - 18 : ks;
        of[k] = __builtin_amdgcn_raw_buffer_load_b128(rsS, l16, ob + ks * 1024u, AUX_SC1); }
      if constexpr (k < 5) { int ks = es + k + rot2; ks = ks >= 18 ? ks - 18 : ks;
        ef[k] = __builtin_amdgcn_raw_buffer_load_b128(rsS, l16, (k == 4 && ei >= 2) ? 0xf0000000u : eb + ks * 1024u, AUX_SC1); }
      SBAR(); });
    const float gm = __uint_as_float(__builtin_amdgcn_raw_buffer_load_b32(rsS, GATE_OFF + (u32)(TT * 16 + c) * 4u, 0, AUX_SC1));
    const float gx = __uint_as_float(__builtin_amdgcn_raw_buffer_load_b32(rsS, GATE_OFF + (u32)((TX & 127) * 16 + c) * 4u, 0, AUX_SC1));
    f32x4 am[4], ax[4];
#pragma unroll
    for (int i = 0; i < 4; ++i) { am[i] = z4; ax[i] = z4; }
    constexpr int NW = PRE + 2;
    sfor<0, 18>([&](auto KC) {
      constexpr int k = decltype(KC)::value, kn = k + PRE + 1;
      constexpr int eg = k < 5 ? 0 : (k < 10 ? 1 : (k < 14 ? 2 : 3)), ej = k - (eg == 0 ? 0 : (eg == 1 ? 5 : (eg == 2 ? 10 : 14)));
      if constexpr (kn == 9) {
#pragma unroll
        for (int i = 0; i < 4; ++i) { WB[i] += 73728; PIN(WB[i]); }
      }
      u32x4 wv[4];
#pragma unroll
      for (int i = 0; i < 4; ++i) wv[i] = (u32x4){flo[k % NW][i][0], flo[k % NW][i][1], fhi[k % NW][i][0], fhi[k % NW][i][1]};
#pragma unroll
      for (int i = 0; i < 4; ++i) {
        am[i] = mfma(wv[i], of[k], am[i]);
        SBAR();
        if constexpr (kn < 18) { flo[kn % NW][i] = trd(WB[i] + (kn % 9) * 4096); fhi[kn % NW][i] = trd(WB[i] + (kn % 9) * 4096 + 2048); }
        SBAR();
      }
      if (ei == eg) {
#pragma unroll
        for (int i = 0; i < 4; ++i) ax[i] = mfma(wv[i], ef[ej], ax[i]);
      }
      SBAR();
    });
    STAMP(12);
    const float fm = br ? 1.f - gm : gm, fx = br ? 1.f - gx : gx;
    lwr128(lds0 + MRED + wid * 2048 + lane * 16, am[2] * fm);
    lwr128(lds0 + MRED + wid * 2048 + 1024 + lane * 16, am[3] * fm);
    if (rg < 4) {
#pragma unroll
      for (int i = 0; i < 4; ++i) lwr128(lds0 + br * 36864 + ti * 4096 + ((i ^ (2 * br)) * 1024) + lane * 16, ax[i] * fx);
    }
    lgkm0();
    bar();
    STAMP(13);
    {
      const f32x4 r0 = *(LAS f32x4*)(uintptr_t)(lds0 + MRED + (wid ^ 4) * 2048 + lane * 16);
      const f32x4 r1 = *(LAS f32x4*)(uintptr_t)(lds0 + MRED + (wid ^ 4) * 2048 + 1024 + lane * 16);
      const f32x4 v0 = am[0] * fm + r0, v1 = am[1] * fm + r1;
      auto s0_ = __builtin_amdgcn_permlane16_swap(pk(v0[0], v0[1]), pk(v1[0], v1[1]), false, false);
      auto s1_ = __builtin_amdgcn_permlane16_swap(pk(v0[2], v0[3]), pk(v1[2], v1[3]), false, false);
      const int row = 2 * (128 * (TT >> 4) + 16 * (TT & 7) + c) + ((TT >> 3) & 1);
      *(u32x4*)(out + (size_t)row * 576 + ct * 64 + br * 32 + (g & 1) * 16 + (g >> 1) * 8) = (u32x4){s0_[0], s1_[0], s0_[1], s1_[1]};
    }
    if (rg < 4) {
      f32x2 s = {0.f, 0.f};
#pragma unroll
      for (int j = 0; j < 8; ++j) s += *(LAS f32x2*)(uintptr_t)(lds0 + (j >> 2) * 36864 + (j & 3) * 4096 + wid * 512 + lane * 8);
      const int L = 32 * (wid & 1) + (lane >> 1);
      const int row = 2 * (128 * (TX >> 4) + 16 * (TX & 7) + (L & 15)) + ((TX >> 3) & 1);
      *(u32*)(out + (size_t)row * 576 + ct * 64 + (wid >> 1) * 16 + (L >> 4) * 4 + (lane & 1) * 2) = pk(s[0], s[1]);
    }
  }
  STAMP(14);
  STAMP(15); RSTAMP(16);
  STAMP_OUT();
}

extern "C" void kernel_launch(void* const* d_in, const int* in_sizes, int n_in, void* d_out, int out_size, void* d_ws, size_t ws_size, hipStream_t stream) {
  const u16* x = (const u16*)d_in[0]; const u16* wqa = (const u16*)d_in[1]; const u16* woa = (const u16*)d_in[2];
  const u16* wqb = (const u16*)d_in[4]; const u16* wob = (const u16*)d_in[5]; const u16* gw = (const u16*)d_in[6];
  (void)hipFuncSetAttribute((const void*)fused, hipFuncAttributeMaxDynamicSharedMemorySize, LDS_BYTES);
  fused<<<256, 512, LDS_BYTES, stream>>>(x, wqa, woa, wqb, wob, gw, (u16*)d_out, (char*)d_ws);
}
```

```cpp
#define TOUCH 2
#include <hip/hip_runtime.h>
typedef unsigned short u16; typedef unsigned int u32; typedef unsigned long long u64;
typedef __attribute__((ext_vector_type(8))) __bf16 bf16x8;
typedef __attribute__((ext_vector_type(2))) __bf16 bf16x2;
typedef __attribute__((ext_vector_type(4))) float f32x4;
typedef __attribute__((ext_vector_type(2))) float f32x2;
typedef __attribute__((ext_vector_type(4))) short s16x4;
typedef __attribute__((ext_vector_type(4))) u32 u32x4;
typedef __attribute__((ext_vector_type(2))) u32 u32x2;
typedef __attribute__((ext_vector_type(10))) u32 u32x10;
#define LAS __attribute__((address_space(3)))
#define DI __device__ __forceinline__
#define SBAR() __builtin_amdgcn_sched_barrier(0)
#define PIN(x) asm volatile("" : "+v"(x))
template <int V> struct IC { static constexpr int value = V; };
template <int A, int B, class F> __device__ __forceinline__ void sfor(F&& f) { if constexpr (A < B) { f(IC<A>{}); sfor<A + 1, B>(f); } }
#define ROT 1
#define ROT2 1
#define PD 6
#define PRE 4

constexpr int WROW = 144, WPART = 9232, XSTG = 136 * 128, STG = 45184;
constexpr int S0 = 0, S1 = 73472, S2 = S1 + STG;
constexpr int LDS_BYTES = 163840;
static_assert(S2 + STG == LDS_BYTES, "lds map");
constexpr int KQB = 4608;
constexpr int XKQ = 73728, XV = XKQ + 9 * KQB, MRED = 147456;
static_assert(XV + 5 * 4608 <= S2 + XSTG + 4608, "exchange area");
constexpr u32 OP_BYTES = 128u * 36u * 1024u;
constexpr u32 GATE_OFF = OP_BYTES;
constexpr u32 NON_OFF = GATE_OFF + 8192;
constexpr u32 INB_OFF = NON_OFF + 1024;
constexpr u32 WS_BYTES = INB_OFF + 256 * 512;
constexpr float CEXP = 0.23570226039551584f * 1.4426950408889634f;
constexpr int AUX_SC1 = 16;

DI u32 pk(float a, float b) { f32x2 v = {a, b}; bf16x2 r = __builtin_convertvector(v, bf16x2); return __builtin_bit_cast(u32, r); }
DI f32x4 mfma(u32x4 a, u32x4 b, f32x4 c) { return __builtin_amdgcn_mfma_f32_16x16x32_bf16(__builtin_bit_cast(bf16x8, a), __builtin_bit_cast(bf16x8, b), c, 0, 0, 0); }
template <bool BI> DI void mfma_acc(f32x4& c, u32x4 a, u32x4 b) {
  if constexpr (BI) c = mfma(a, b, c);
  else asm volatile("v_mfma_f32_16x16x32_bf16 %0, %1, %2, %0" : "+v"(c) : "v"(a), "v"(b));
}
DI u32x2 trd(u32 a) { return __builtin_bit_cast(u32x2, __builtin_amdgcn_ds_read_tr16_b64_v4i16((LAS s16x4*)(uintptr_t)a)); }
DI u32x4 lrd128(u32 a) { return *(LAS u32x4*)(uintptr_t)a; }
DI u32x2 lrd64(u32 a) { return *(LAS u32x2*)(uintptr_t)a; }
DI void lwr64(u32 a, f32x4 v) { *(LAS u32x2*)(uintptr_t)a = (u32x2){pk(v[0], v[1]), pk(v[2], v[3])}; }
DI void lwr128(u32 a, f32x4 v) { *(LAS f32x4*)(uintptr_t)a = v; }
DI void dma16(__amdgpu_buffer_rsrc_t rs, u32 voff, u32 soff, u32 ldsdst) {
  asm volatile("s_mov_b32 m0, %0\n\ts_nop 0\n\tbuffer_load_dwordx4 %1, %2, %3 offen lds" :: "s"(ldsdst), "v"(voff), "s"(rs), "s"(soff) : "memory");
}
template <int N> DI void wait_vm() { asm volatile("s_waitcnt vmcnt(%0)" :: "i"(N) : "memory"); }
DI void bar() { asm volatile("s_barrier" ::: "memory"); }
DI void lgkm0() { asm volatile("s_waitcnt lgkmcnt(0)" ::: "memory"); }
#define STAMP(i)
#define RSTAMP(i)
#define STAMP_DECL
#define STAMP_OUT()
DI float dot2(u32 a, u32 b, float c) { return __builtin_amdgcn_fdot2_f32_bf16(__builtin_bit_cast(bf16x2, a), __builtin_bit_cast(bf16x2, b), c, false); }
DI float fmx(float a, float b) { return __builtin_elementwise_maximum(a, b); }
DI void sw32(float& a, float& b) { auto r = __builtin_amdgcn_permlane32_swap(__float_as_uint(a), __float_as_uint(b), false, false); a = __uint_as_float(r[0]); b = __uint_as_float(r[1]); }
DI void sw16(float& a, float& b) { auto r = __builtin_amdgcn_permlane16_swap(__float_as_uint(a), __float_as_uint(b), false, false); a = __uint_as_float(r[0]); b = __uint_as_float(r[1]); }
DI int nxt9(int s) { return s == 8 ? 0 : s + 1; }
DI int nxtslot(int s) { return s == S0 ? S1 : (s == S1 ? S2 : S0); }
DI __amdgpu_buffer_rsrc_t mkrs(const void* p, u32 n) { return __builtin_amdgcn_make_buffer_rsrc((void*)p, 0, n, 0x00020000); }

DI void wo_block(const u16* woa, const u16* wob, u32 voR, u32 voT, u32 lds0, int hf, int idx, int rot) {
  const bool br = idx >= 9;
  int ks = 9 * hf + (br ? idx - 9 : idx) + rot; ks = ks >= 18 ? ks - 18 : ks;
  const int kq = ks >= 9, kk = kq ? ks - 9 : ks;
  const bool tl = kk == 8;
  const u32 so = (u32)((kq ? 288 : 0) + (tl ? 64 : 72 * (kk >> 1) + 32 * (kk & 1))) * 1152u;
  const u32 dst = lds0 + hf * 73728 + idx * 4096;
  const __amdgpu_buffer_rsrc_t rs = mkrs(br ? wob : woa, 576 * 1152);
  const u32 vo = tl ? voT : voR;
  dma16(rs, vo, so, dst);
  dma16(rs, vo, so + (tl ? 144u : 8u) * 1152u, dst + 1024);
  dma16(rs, vo, so + (tl ? 4u : 16u) * 1152u, dst + 2048);
  dma16(rs, vo, so + (tl ? 148u : 24u) * 1152u, dst + 3072);
}

__global__ __launch_bounds__(512, 2) void fused(const u16* __restrict__ x, const u16* __restrict__ wqa, const u16* __restrict__ woa,
                                                const u16* __restrict__ wqb, const u16* __restrict__ wob, const u16* __restrict__ gw,
                                                u16* __restrict__ out, char* __restrict__ ws) {
  extern __shared__ __attribute__((aligned(16))) char lds[];
  unsigned long long pc0; asm volatile("s_getpc_b64 %0" : "=s"(pc0)); pc0 &= ~255ull;
  asm volatile("" :: "s"(x), "s"(wqa), "s"(woa), "s"(wqb), "s"(wob), "s"(gw), "s"(out), "s"(ws));
  const int tid = threadIdx.x, lane = tid & 63;
  const int wid = __builtin_amdgcn_readfirstlane(tid >> 6);
  STAMP_DECL; RSTAMP(0); STAMP(1);
  const int b = blockIdx.x, xcd = b & 7, bi = b >> 3;
  const int cp = 8 * (xcd & 1) + (bi & 7), hg = 4 * (xcd >> 1) + (bi >> 3);
  const int par = cp & 1, chunk = cp >> 1;
  const int hq = hg & 7;
  int s0 = ROT ? (bi & 7) + 2 * (bi >> 3) : 0; s0 = s0 >= 9 ? s0 - 9 : s0;
  const int sg = cp >> 2, mi = 16 * (hg >> 2) + 4 * (cp & 3) + (hg & 3);
  const int rg = (mi * 57) >> 9, ct = mi - 9 * rg;
  const int rot2 = ROT2 ? 2 * ct : 0;
  const int c = lane & 15, g = lane >> 4, q = (lane >> 2) & 3, p = lane & 3;
  const int r8 = lane >> 3, sp8 = lane & 7;
  const u32 lds0 = (u32)(uintptr_t)(LAS char*)lds;
  const __amdgpu_buffer_rsrc_t rsS = mkrs(ws, WS_BYTES);
  const f32x4 z4 = {0.f, 0.f, 0.f, 0.f};
  u32 nonce = 0, vn = 0;
  f32x4 bias; u32 mq[5][2], mP; u32 voR, voT;
  auto setup = [&]() {
    const float NINF = -__builtin_inff();
    const bool first = (chunk == 0 && wid == 0);
#pragma unroll
    for (int r = 0; r < 4; ++r) {
      const int i = 4 * g + r;
      const bool ok = (i <= c && i >= c - 7) || (i >= c + 9 && !first);
      bias[r] = ok ? 0.f : NINF;
    }
    const u32 lo = c < 8 ? ~0u : 0u;
    const u32 pr[5] = {~0u, g == 0 ? ~0u : 0u, g >= 1 ? ~0u : 0u, g <= 1 ? ~0u : 0u, g >= 2 ? ~0u : 0u};
#pragma unroll
    for (int i = 0; i < 5; ++i) { mq[i][0] = pr[i] & lo; mq[i][1] = pr[i] & ~lo; PIN(mq[i][0]); PIN(mq[i][1]); }
    mP = ~mq[4][0]; PIN(mP);
    PIN(bias);
    const u32 woc = (u32)(ct * 128 + ((sp8 ^ (2 * ((r8 >> 1) & 3))) * 16));
    voR = (u32)r8 * 1152u + woc; voT = (u32)(72 * (r8 >> 2) + (r8 & 3)) * 1152u + woc; PIN(voR); PIN(voT);
  };

  if (wid >= 4) {
    if (wid < 7) {
      const int a = wid - 4;
      const __amdgpu_buffer_rsrc_t rsX = mkrs(x, 2048 * 1152);
      const __amdgpu_buffer_rsrc_t rsW = mkrs((hg >> 3) ? wqb : wqa, 576 * 3456);
      const u32 vA = ((u32)(2 * (128 * chunk + r8) + par) * 1152u + (u32)(sp8 ^ (r8 >> 1)) * 16u) ^ (u32)((a & 1) * 64), vB = vA ^ 64u;
      int jh = 128 * chunk - 8 + r8; jh = jh < 0 ? 0 : jh;
      const u32 v5 = a == 0 ? vB + 15u * 18432u : (u32)(2 * jh + par) * 1152u + (u32)(sp8 ^ (r8 & 6)) * 16u;
      const u32 xso = a * 18432, xd = lds0 + a * 1024, xd5 = lds0 + (a == 0 ? 15 : 16) * 1024;
      auto xlo = [&](int st, int sl) {
        dma16(rsX, vA, st * 128 + xso, xd + sl); dma16(rsX, vB, st * 128 + xso + 3 * 18432, xd + sl + 3072); dma16(rsX, vA, st * 128 + xso + 6 * 18432, xd + sl + 6144);
      };
      auto xhi = [&](int st, int sl) {
        dma16(rsX, vB, st * 128 + xso + 9 * 18432, xd + sl + 9216); dma16(rsX, vA, st * 128 + xso + 12 * 18432, xd + sl + 12288); dma16(rsX, v5, st * 128, xd5 + sl);
      };
      u32 vw[9];
      const u32 wso = a * 1152 + hq * 144, wd = lds0 + XSTG + a * WPART;
      xlo(s0, S0); xhi(s0, S0);
      SBAR();
#pragma unroll
      for (int i = 0; i < 9; ++i) {
        const u32 ci = (u32)(64 * i) + lane;
        const u32 pos = __umul24(ci, 7282u) >> 16, ch = ci - __umul24(pos, 9u);
        const u32 row = (pos & ~7u) | ((pos & 1u) << 2) | ((pos & 7u) >> 1);
        vw[i] = __umul24(row, 3456u) + ch * 16u;
        dma16(rsW, vw[i], s0 * (64 * 3456) + wso, wd + i * 1024);
        SBAR();
      }
      int st = nxt9(s0), sl = S1;
      xlo(st, sl);
      setup();
#pragma unroll 1
      for (int it = 0; it < 8; ++it) {
        wait_vm<7>(); bar();
#pragma unroll
        for (int i = 0; i < 5; ++i) dma16(rsW, vw[i], st * (64 * 3456) + wso, wd + sl + i * 1024);
        xhi(st, sl);
        wait_vm<11>(); bar();
#pragma unroll
        for (int i = 5; i < 9; ++i) dma16(rsW, vw[i], st * (64 * 3456) + wso, wd + sl + i * 1024);
        st = nxt9(st); sl = nxtslot(sl);
        if (it < 7) xlo(st, sl);
      }
      wait_vm<4>();
    } else {
      const u32x2 inb0 = __builtin_amdgcn_raw_buffer_load_b64(rsS, INB_OFF + (u32)(sg * 64 + mi) * 512u + lane * 8u, 0, AUX_SC1);
      const u32 non0 = __builtin_amdgcn_raw_buffer_load_b32(rsS, NON_OFF + (u32)(sg * 64 + mi) * 4u, 0, AUX_SC1);
      u32 tt[6] = {0, 0, 0, 0, 0, 0};
      { const __attribute__((address_space(1))) char* cpn = (const __attribute__((address_space(1))) char*)pc0 + lane * 128;
        typedef const __attribute__((address_space(1))) u32* gp;
        tt[0] = *(gp)(cpn); tt[1] = *(gp)(cpn + 8192); tt[2] = *(gp)(cpn + (lane < 12 ? 16384 : 0)); }
      setup();
      bar(); bar();
      nonce = __builtin_amdgcn_readfirstlane(non0) + 1u;
      while (__ballot(inb0[0] == nonce) != 0ull) ++nonce;
      if (lane == 0) __builtin_amdgcn_raw_buffer_store_b32(nonce, rsS, NON_OFF + (u32)(sg * 64 + mi) * 4u, 0, AUX_SC1);
      bar();
      const int jl = 8 * hg + r8;
      const u16* xr = x + (size_t)(2 * (128 * chunk + jl) + par) * 576;
      u32x4 xv[9], gv[9];
#pragma unroll
      for (int i = 0; i < 9; ++i) { xv[i] = *(const u32x4*)(xr + (i * 8 + sp8) * 8); gv[i] = *(const u32x4*)(gw + (i * 8 + sp8) * 8); }
      bar(); bar(); bar(); bar();
      asm volatile("" :: "v"(tt[0]), "v"(tt[1]), "v"(tt[2]), "v"(tt[3]), "v"(tt[4]), "v"(tt[5]));
      float a0 = 0.f, a1 = 0.f;
#pragma unroll
      for (int i = 0; i < 9; ++i) {
        a0 = dot2(xv[i][0], gv[i][0], a0); a1 = dot2(xv[i][1], gv[i][1], a1);
        a0 = dot2(xv[i][2], gv[i][2], a0); a1 = dot2(xv[i][3], gv[i][3], a1);
      }
      float a = a0 + a1; PIN(a);
      bar();
      a += __shfl_xor(a, 1); a += __shfl_xor(a, 2); a += __shfl_xor(a, 4);
      const float gt = __builtin_amdgcn_rcpf(1.f + __builtin_amdgcn_exp2f(-1.4426950408889634f * a));
      if (sp8 == 0) __builtin_amdgcn_raw_buffer_store_b32(__float_as_uint(gt), rsS, GATE_OFF + (u32)(cp * 128 + jl) * 4u, 0, AUX_SC1);
      for (int i = 0; i < 7; ++i) bar();
      vn = __builtin_amdgcn_raw_buffer_load_b32(rsS, NON_OFF + (u32)(sg * 64 + lane) * 4u, 0, AUX_SC1);
      bar();
    }
    bar();
#pragma unroll 1
    for (int idx = wid - 4; idx < 18; idx += 4) {
      wo_block(woa, wob, voR, voT, lds0, 0, idx, rot2);
      if (idx >= 4 && idx < 8) { wait_vm<8>(); bar(); }
    }
    bar();
  } else {
    const int w = wid;
    const u32 rb = lds0 + XSTG + (8 * g + 2 * q) * WROW + 8 * p;
    u32 B[4], BM, BV, XB[2], HB[2];
#pragma unroll
    for (int i = 0; i < 4; ++i) B[i] = rb + 32 * (i ^ w);
    BM = rb + (p >= 2 ? WPART + 112 : 128); BV = rb + (p >= 2 ? WPART + 112 : 2 * WPART + 128);
    XB[0] = lds0 + (32 * w + c) * 128 + ((g ^ (c >> 1)) * 16); XB[1] = XB[0] ^ 64;
    HB[0] = lds0 + (128 + (c & 7)) * 128 + ((g ^ (c & 6)) * 16); HB[1] = HB[0] ^ 64;
    u32 EK[4], EV[4], EM, EH;
#pragma unroll
    for (int i = 0; i < 4; ++i) {
      const int t = i ^ w;
      EK[i] = lds0 + XKQ + (2 * w + 1) * KQB + (t >> 1) * 1024 + lane * 16 + (t & 1) * 8;
      EV[i] = lds0 + XV + t * 4608 + (2 * w + 1) * 512 + lane * 8;
      PIN(EK[i]); PIN(EV[i]);
    }
    EM = lds0 + XKQ + (2 * w + 1) * KQB + 4096 + (lane & 31) * 16 + (g < 2 ? 8 : 0); PIN(EM);
    EH = lds0 + XKQ + 4096 + (4 * g + w + 16 * ((c & 7) >> 2)) * 16 + (c & 3) * 2; PIN(EH);
    setup();
    f32x4 aQ[2][4], aK[2][4], aM[2], aV[2][5], hK, hV, hV4;
#pragma unroll
    for (int t = 0; t < 2; ++t) { aM[t] = z4; aV[t][4] = z4; PIN(aM[t]); PIN(aV[t][4]);
#pragma unroll
      for (int i = 0; i < 4; ++i) { aQ[t][i] = z4; aK[t][i] = z4; aV[t][i] = z4; PIN(aQ[t][i]); PIN(aK[t][i]); PIN(aV[t][i]); } }
    hK = z4; hV = z4; hV4 = z4; PIN(hK); PIN(hV); PIN(hV4);
    PIN(BM); PIN(BV); PIN(XB[0]); PIN(XB[1]); PIN(HB[0]); PIN(HB[1]);
    __builtin_amdgcn_s_waitcnt(0xC07F);
    u32x2 wlo[7], whi[7]; u32x4 xf[2][3];
    auto faddr = [&](auto NC) -> u32 {
      constexpr int n = decltype(NC)::value; constexpr int f = n % 14, ko = (n / 14) * 32 * WROW;
      if constexpr (f < 4) return B[f] + ko;
      else if constexpr (f < 8) return B[f - 4] + ko + WPART;
      else if constexpr (f == 8) return BM + ko;
      else if constexpr (f < 13) return B[f - 9] + ko + 2 * WPART;
      else return BV + ko;
    };
    auto xw = [&](auto FC, auto TC) {
      constexpr int f = decltype(FC)::value, t = decltype(TC)::value;
      if constexpr (t < 2) {
        if constexpr (f < 4) lwr64(EK[f] + t * KQB + 2048, aQ[t][f]);
        else if constexpr (f < 8) lwr64(EK[f - 4] + t * KQB, aK[t][f - 4]);
        else if constexpr (f == 8) lwr64(EM + t * KQB, aM[t]);
        else if constexpr (f < 13) lwr64(EV[f - 9] + t * 512, aV[t][f - 9]);
        else lwr64(lds0 + XV + 4 * 4608 + lane * 8 + (2 * w + 1 + t) * 512, aV[t][4]);
      } else {
        if constexpr (f == 4) lwr64(EK[0] - (2 * w + 1) * KQB, hK);
        if constexpr (f == 9) lwr64(EV[0] - (2 * w + 1) * 512, hV);
      }
    };
    STAMP(2);
    bar();
    STAMP(3);
    xf[0][0] = lrd128(XB[0]); xf[0][1] = lrd128(XB[0] + 2048);
    sfor<0, PD>([&](auto NC) { constexpr int n = decltype(NC)::value; const u32 a = faddr(NC); wlo[n % 7] = trd(a); whi[n % 7] = trd(a + WROW); if constexpr (n == 1) xf[0][2] = lrd128(HB[0]); SBAR(); });
    auto stage = [&](auto LC, int d) {
      constexpr bool LAST = decltype(LC)::value;
      sfor<0, 28>([&](auto NC) {
        constexpr int n = decltype(NC)::value;
        constexpr int f = n % 14, k2 = n / 14, nn = (n + PD) % 28, kn = nn / 14;
        constexpr bool RD = !(LAST && n + PD >= 28), BI = LAST && k2 == 1;
        constexpr int fx = f - 2;
        if constexpr (nn == 0 || nn == 14) { SBAR(); bar(); SBAR(); }
        const u32x4 wv = {wlo[n % 7][0], wlo[n % 7][1], whi[n % 7][0], whi[n % 7][1]};
        const u32x4 x0 = xf[k2][0], x1 = xf[k2][1], xh = xf[k2][2];
        const u32 a = faddr(IC<nn>{});
        if constexpr (f < 4) mfma_acc<BI>(aQ[0][f], wv, x0); else if constexpr (f < 8) mfma_acc<BI>(aK[0][f - 4], wv, x0);
        else if constexpr (f == 8) mfma_acc<BI>(aM[0], wv, x0); else mfma_acc<BI>(aV[0][f - 9], x0, wv);
        SBAR();
        if constexpr (RD) wlo[nn % 7] = trd(a);
        if constexpr (BI && fx >= 0) xw(IC<(fx >= 0 ? fx : 0)>{}, IC<0>{});
        SBAR();
        if constexpr (f < 4) mfma_acc<BI>(aQ[1][f], wv, x1); else if constexpr (f < 8) mfma_acc<BI>(aK[1][f - 4], wv, x1);
        else if constexpr (f == 8) mfma_acc<BI>(aM[1], wv, x1); else mfma_acc<BI>(aV[1][f - 9], x1, wv);
        SBAR();
        if constexpr (RD) whi[nn % 7] = trd(a + WROW);
        if constexpr (BI && fx >= 0) { xw(IC<(fx >= 0 ? fx : 0)>{}, IC<1>{}); xw(IC<(fx >= 0 ? fx : 0)>{}, IC<2>{}); }
        if constexpr (!LAST) {
          if constexpr (nn >= 23 && nn <= 26) { B[nn - 23] += d; PIN(B[nn - 23]); }
          if constexpr (nn == 22) { BM += d; PIN(BM); }
          if constexpr (nn == 27) { BV += d; PIN(BV); }
          if constexpr (nn == 7) { XB[0] += d; PIN(XB[0]); }
          if constexpr (nn == 8) { HB[0] += d; PIN(HB[0]); }
          if constexpr (nn == 20) { XB[1] += d; PIN(XB[1]); }
          if constexpr (nn == 21) { HB[1] += d; PIN(HB[1]); }
        }
        SBAR();
        if constexpr (f == 4) mfma_acc<BI>(hK, wv, xh);
        if constexpr (f == 9) mfma_acc<BI>(hV, xh, wv);
        if constexpr (f == 13) mfma_acc<BI>(hV4, xh, wv);
        SBAR();
        if constexpr (RD) {
          if constexpr (nn % 14 == 1) xf[kn][0] = lrd128(XB[kn]);
          if constexpr (nn % 14 == 2) xf[kn][1] = lrd128(XB[kn] + 2048);
          if constexpr (nn % 14 == 5) xf[kn][2] = lrd128(HB[kn]);
        }
        SBAR();
      });
    };
    int sl = S0;
#pragma unroll 1
    for (int it = 0; it < 8; ++it) { const int sn = nxtslot(sl), d = sn - sl; sl = sn; stage(IC<0>{}, d); }
    STAMP(4);
    stage(IC<1>{}, 0);
    xw(IC<12>{}, IC<0>{}); xw(IC<12>{}, IC<1>{}); xw(IC<13>{}, IC<0>{}); xw(IC<13>{}, IC<1>{});
    lwr64(lds0 + XV + 4 * 4608 + lane * 8, hV4);
    {
      const float hv = (w & 2) ? ((w & 1) ? hV4[3] : hV4[2]) : ((w & 1) ? hV4[1] : hV4[0]);
      if (c >= 8) *(LAS u16*)(uintptr_t)EH = (u16)(pk(hv, 0.f) & 0xffffu);
    }
    lgkm0();
  }
  STAMP(5);
  bar();
  STAMP(6);
  {
    const u32 kq = lds0 + XKQ + (wid + 1) * KQB + lane * 16;
    const u32 ks = kq - (c >= 8 ? KQB : 0);
    const u32 fix = g >= 2 ? 512 : 0;
    const u32 va = lds0 + XV + wid * 512 + lane * 8;
    u32x10 Ks, Kc; u32 Q[10];
    { const u32x4 a = lrd128(kq + 2048), b0 = lrd128(ks), c0 = lrd128(kq), a2 = lrd128(kq + 3072), b1 = lrd128(ks + 1024), c1 = lrd128(kq + 1024);
      const u32x4 tq = lrd128(kq - fix + 4096); const u32x2 tp = lrd64(ks - fix + 4096);
#pragma unroll
      for (int i = 0; i < 4; ++i) { Q[i] = a[i]; Q[4 + i] = a2[i]; Ks[i] = b0[i]; Ks[4 + i] = b1[i]; Kc[i] = c0[i]; Kc[4 + i] = c1[i]; }
      Kc[8] = tq[0]; Kc[9] = tq[1]; Q[8] = tq[2]; Q[9] = tq[3]; Ks[8] = tp[0]; Ks[9] = tp[1]; }
    u32x4 Vo[5];
#pragma unroll
    for (int t = 0; t < 5; ++t) { const u32x2 a = lrd64(va + t * 4608), bb = lrd64(va + t * 4608 + 512); Vo[t] = (u32x4){a[0], a[1], bb[0], bb[1]}; }
    PIN(Ks); PIN(Kc);
    f32x4 sc[4]; float mx[4];
    sfor<0, 4>([&](auto HC) {
      constexpr int h = decltype(HC)::value;
      constexpr int ia0 = h == 0 ? 0 : (h == 3 ? 4 : 2), ia1 = h <= 1 ? 0 : 2, ib0 = h <= 1 ? 1 : 3, ib1 = h == 0 ? -1 : (h == 3 ? 3 : 1);
      u32x4 qo[2];
#pragma unroll
      for (int s = 0; s < 2; ++s) {
        qo[s][0] = Q[2 * h] & mq[ia0][s]; qo[s][1] = Q[2 * h + 1] & mq[ia1][s]; qo[s][2] = Q[2 * h + 2] & mq[ib0][s];
        if constexpr (ib1 >= 0) qo[s][3] = Q[2 * h + 3] & mq[ib1 >= 0 ? ib1 : 0][s]; else qo[s][3] = 0u;
      }
      const u32x4 k0 = __builtin_shufflevector(Ks, Ks, 2 * h, 2 * h + 1, 2 * h + 2, 2 * h + 3);
      const u32x4 k1 = __builtin_shufflevector(Kc, Kc, 2 * h, 2 * h + 1, 2 * h + 2, 2 * h + 3);
      sc[h] = mfma(k1, qo[1], mfma(k0, qo[0], bias));
    });
#pragma unroll
    for (int h = 0; h < 4; ++h) mx[h] = fmx(fmx(fmx(sc[h][0], sc[h][1]), sc[h][2]), sc[h][3]);
    { sw32(mx[0], mx[1]); float m01 = fmx(mx[0], mx[1]); sw32(mx[2], mx[3]); float m23 = fmx(mx[2], mx[3]);
      sw16(m01, m23); float R = fmx(m01, m23) * CEXP;
      float a = R, bb = R; sw16(a, bb); mx[0] = a; mx[1] = a; sw32(mx[0], mx[1]); mx[2] = bb; mx[3] = bb; sw32(mx[2], mx[3]); }
    f32x4 olo[4], ohi[4]; float inv[4];
    const u32x4 ones = {0x3f803f80u, 0x3f803f80u, 0x3f803f80u, 0x3f803f80u};
    sfor<0, 4>([&](auto HC) {
      constexpr int h = decltype(HC)::value;
      float e[4];
#pragma unroll
      for (int r = 0; r < 4; ++r) e[r] = __builtin_amdgcn_exp2f(__builtin_fmaf(sc[h][r], CEXP, -mx[h]));
      const u32 p0 = pk(e[0], e[1]), p1 = pk(e[2], e[3]);
      const u32x4 po = {p0 & mq[4][0], p1 & mq[4][0], p0 & mP, p1 & mP};
      const f32x4 sm = mfma(ones, po, z4);
      olo[h] = mfma(Vo[h], po, z4); ohi[h] = mfma(Vo[h + 1], po, z4);
      inv[h] = __builtin_amdgcn_rcpf(sm[0]);
    });
    u32 op[10];
#pragma unroll
    for (int i = 0; i < 5; ++i)
#pragma unroll
      for (int e = 0; e < 2; ++e) {
        float v0, v1, iv;
        if (i == 0) { v0 = olo[0][2 * e]; v1 = olo[0][2 * e + 1]; iv = inv[0]; }
        else if (i == 4) { v0 = ohi[3][2 * e]; v1 = ohi[3][2 * e + 1]; iv = inv[3]; }
        else { const bool s = 2 * g + e < i; v0 = s ? ohi[i - 1][2 * e] : olo[i][2 * e]; v1 = s ? ohi[i - 1][2 * e + 1] : olo[i][2 * e + 1]; iv = s ? inv[i - 1] : inv[i]; }
        op[2 * i + e] = pk(v0 * iv, v1 * iv);
      }
    const u32 ob = (u32)((cp * 8 + wid) * 36 + 9 * (hg >> 2)) * 1024u;
    __builtin_amdgcn_raw_buffer_store_b128((u32x4){op[0], op[1], op[2], op[3]}, rsS, lane * 16u, ob + (hg & 3) * 2048u, AUX_SC1);
    __builtin_amdgcn_raw_buffer_store_b128((u32x4){op[4], op[5], op[6], op[7]}, rsS, lane * 16u + 1024u, ob + (hg & 3) * 2048u, AUX_SC1);
    if (g < 2) __builtin_amdgcn_raw_buffer_store_b64((u32x2){op[8], op[9]}, rsS, c * 16u + g * 8u, ob + 8192u + (hg & 3) * 256u, AUX_SC1);
  }
  const int br = wid >> 2, ti = wid & 3, ei = (rg < 4) ? (ti ^ (br << 1)) : -1;
  u32 WB[4];
  { const int pos = 4 * g + q, s = (pos >> 1) & 3;
#pragma unroll
    for (int i = 0; i < 4; ++i) { WB[i] = lds0 + br * 36864 + pos * 128 + (((i ^ (2 * br)) ^ s) * 32) + p * 8; PIN(WB[i]); } }
  u32 myin = INB_OFF + (u32)(sg * 64 + mi) * 512u + lane * 8u, dst = INB_OFF + (u32)(sg * 64 + lane) * 512u + mi * 8u;
  PIN(myin); PIN(dst);
  STAMP(7);
  wait_vm<0>();
  STAMP(8);
  bar();
  STAMP(9);
  u32x2 flo[PRE + 2][4], fhi[PRE + 2][4];
  if (wid == 7) {
    __builtin_amdgcn_raw_buffer_store_b64((u32x2){vn, nonce}, rsS, dst, 0, AUX_SC1);
  } else if (wid < 6) {
#pragma unroll 1
    for (int idx = wid; idx < 18; idx += 6) wo_block(woa, wob, voR, voT, lds0, 1, idx, rot2);
  }
  sfor<0, PRE + 1>([&](auto KC) { constexpr int k = decltype(KC)::value;
#pragma unroll
    for (int i = 0; i < 4; ++i) { flo[k][i] = trd(WB[i] + k * 4096); fhi[k][i] = trd(WB[i] + k * 4096 + 2048); } });
  SBAR();
  if (wid == 7) {
#pragma unroll 1
    for (int spin = 0; spin < (1 << 17); ++spin) {
      const u32x2 v = __builtin_amdgcn_raw_buffer_load_b64(rsS, myin, 0, AUX_SC1);
      const bool ok = v[0] == nonce;
      if (ok && v[1] != vn) { vn = v[1]; __builtin_amdgcn_raw_buffer_store_b64((u32x2){vn, nonce}, rsS, dst, 0, AUX_SC1); }
      if (__ballot(ok) == ~0ull) break;
      __builtin_amdgcn_s_sleep(1);
    }
  }
  asm volatile("s_waitcnt vmcnt(0) lgkmcnt(0)" ::: "memory");
  STAMP(10);
  bar();
  STAMP(11);
  if (rg < 7) {
    const int TT = 32 * sg + 4 * rg + ti, TX = 32 * sg + 28 + rg;
    u32x4 of[18], ef[5];
    const u32 ob = (u32)(TT * 36 + 18 * br) * 1024u;
    const int es = ei == 0 ? 0 : (ei == 1 ? 5 : (ei == 2 ? 10 : 14));
    const u32 eb = ei >= 0 ? (u32)(TX * 36 + 18 * br) * 1024u : 0xf0000000u;
    const u32 l16 = lane * 16u;
    sfor<0, 18>([&](auto KC) { constexpr int k = decltype(KC)::value;
      { int ks = k + rot2; ks = ks >= 18 ? ks - 18 : ks;
        of[k] = __builtin_amdgcn_raw_buffer_load_b128(rsS, l16, ob + ks * 1024u, AUX_SC1); }
      if constexpr (k < 5) { int ks = es + k + rot2; ks = ks >= 18 ? ks - 18 : ks;
        ef[k] = __builtin_amdgcn_raw_buffer_load_b128(rsS, l16, (k == 4 && ei >= 2) ? 0xf0000000u : eb + ks * 1024u, AUX_SC1); }
      SBAR(); });
    const float gm = __uint_as_float(__builtin_amdgcn_raw_buffer_load_b32(rsS, GATE_OFF + (u32)(TT * 16 + c) * 4u, 0, AUX_SC1));
    const float gx = __uint_as_float(__builtin_amdgcn_raw_buffer_load_b32(rsS, GATE_OFF + (u32)((TX & 127) * 16 + c) * 4u, 0, AUX_SC1));
    f32x4 am[4], ax[4];
#pragma unroll
    for (int i = 0; i < 4; ++i) { am[i] = z4; ax[i] = z4; }
    constexpr int NW = PRE + 2;
    sfor<0, 18>([&](auto KC) {
      constexpr int k = decltype(KC)::value, kn = k + PRE + 1;
      constexpr int eg = k < 5 ? 0 : (k < 10 ? 1 : (k < 14 ? 2 : 3)), ej = k - (eg == 0 ? 0 : (eg == 1 ? 5 : (eg == 2 ? 10 : 14)));
      if constexpr (kn == 9) {
#pragma unroll
        for (int i = 0; i < 4; ++i) { WB[i] += 73728; PIN(WB[i]); }
      }
      u32x4 wv[4];
#pragma unroll
      for (int i = 0; i < 4; ++i) wv[i] = (u32x4){flo[k % NW][i][0], flo[k % NW][i][1], fhi[k % NW][i][0], fhi[k % NW][i][1]};
#pragma unroll
      for (int i = 0; i < 4; ++i) {
        am[i] = mfma(wv[i], of[k], am[i]);
        SBAR();
        if constexpr (kn < 18) { flo[kn % NW][i] = trd(WB[i] + (kn % 9) * 4096); fhi[kn % NW][i] = trd(WB[i] + (kn % 9) * 4096 + 2048); }
        SBAR();
      }
      if (ei == eg) {
#pragma unroll
        for (int i = 0; i < 4; ++i) ax[i] = mfma(wv[i], ef[ej], ax[i]);
      }
      SBAR();
    });
    STAMP(12);
    const float fm = br ? 1.f - gm : gm, fx = br ? 1.f - gx : gx;
    lwr128(lds0 + MRED + wid * 2048 + lane * 16, am[2] * fm);
    lwr128(lds0 + MRED + wid * 2048 + 1024 + lane * 16, am[3] * fm);
    if (rg < 4) {
#pragma unroll
      for (int i = 0; i < 4; ++i) lwr128(lds0 + br * 36864 + ti * 4096 + ((i ^ (2 * br)) * 1024) + lane * 16, ax[i] * fx);
    }
    lgkm0();
    bar();
    STAMP(13);
    {
      const f32x4 r0 = *(LAS f32x4*)(uintptr_t)(lds0 + MRED + (wid ^ 4) * 2048 + lane * 16);
      const f32x4 r1 = *(LAS f32x4*)(uintptr_t)(lds0 + MRED + (wid ^ 4) * 2048 + 1024 + lane * 16);
      const f32x4 v0 = am[0] * fm + r0, v1 = am[1] * fm + r1;
      auto s0_ = __builtin_amdgcn_permlane16_swap(pk(v0[0], v0[1]), pk(v1[0], v1[1]), false, false);
      auto s1_ = __builtin_amdgcn_permlane16_swap(pk(v0[2], v0[3]), pk(v1[2], v1[3]), false, false);
      const int row = 2 * (128 * (TT >> 4) + 16 * (TT & 7) + c) + ((TT >> 3) & 1);
      *(u32x4*)(out + (size_t)row * 576 + ct * 64 + br * 32 + (g & 1) * 16 + (g >> 1) * 8) = (u32x4){s0_[0], s1_[0], s0_[1], s1_[1]};
    }
    if (rg < 4) {
      f32x2 s = {0.f, 0.f};
#pragma unroll
      for (int j = 0; j < 8; ++j) s += *(LAS f32x2*)(uintptr_t)(lds0 + (j >> 2) * 36864 + (j & 3) * 4096 + wid * 512 + lane * 8);
      const int L = 32 * (wid & 1) + (lane >> 1);
      const int row = 2 * (128 * (TX >> 4) + 16 * (TX & 7) + (L & 15)) + ((TX >> 3) & 1);
      *(u32*)(out + (size_t)row * 576 + ct * 64 + (wid >> 1) * 16 + (L >> 4) * 4 + (lane & 1) * 2) = pk(s[0], s[1]);
    }
  }
  STAMP(14);
  STAMP(15); RSTAMP(16);
  STAMP_OUT();
}

extern "C" void kernel_launch(void* const* d_in, const int* in_sizes, int n_in, void* d_out, int out_size, void* d_ws, size_t ws_size, hipStream_t stream) {
  const u16* x = (const u16*)d_in[0]; const u16* wqa = (const u16*)d_in[1]; const u16* woa = (const u16*)d_in[2];
  const u16* wqb = (const u16*)d_in[4]; const u16* wob = (const u16*)d_in[5]; const u16* gw = (const u16*)d_in[6];
  (void)hipFuncSetAttribute((const void*)fused, hipFuncAttributeMaxDynamicSharedMemorySize, LDS_BYTES);
  fused<<<256, 512, LDS_BYTES, stream>>>(x, wqa, woa, wqb, wob, gw, (u16*)d_out, (char*)d_ws);
}
```

```cpp
#define PROBE 0
#define PROBE_CYC 0
#include <hip/hip_runtime.h>
typedef unsigned short u16; typedef unsigned int u32; typedef unsigned long long u64;
typedef __attribute__((ext_vector_type(8))) __bf16 bf16x8;
typedef __attribute__((ext_vector_type(2))) __bf16 bf16x2;
typedef __attribute__((ext_vector_type(4))) float f32x4;
typedef __attribute__((ext_vector_type(2))) float f32x2;
typedef __attribute__((ext_vector_type(4))) short s16x4;
typedef __attribute__((ext_vector_type(4))) u32 u32x4;
typedef __attribute__((ext_vector_type(2))) u32 u32x2;
typedef __attribute__((ext_vector_type(10))) u32 u32x10;
#define LAS __attribute__((address_space(3)))
#define DI __device__ __forceinline__
#define SBAR() __builtin_amdgcn_sched_barrier(0)
#define PIN(x) asm volatile("" : "+v"(x))
template <int V> struct IC { static constexpr int value = V; };
template <int A, int B, class F> __device__ __forceinline__ void sfor(F&& f) { if constexpr (A < B) { f(IC<A>{}); sfor<A + 1, B>(f); } }
#define ROT 1
#define ROT2 1
#define PD 6
#define PRE 4

constexpr int WROW = 144, WPART = 9232, XSTG = 136 * 128, STG = 45184;
constexpr int S0 = 0, S1 = 73472, S2 = S1 + STG;
constexpr int LDS_BYTES = 163840;
static_assert(S2 + STG == LDS_BYTES, "lds map");
constexpr int KQB = 4608;
constexpr int XKQ = 73728, XV = XKQ + 9 * KQB, MRED = 147456;
static_assert(XV + 5 * 4608 <= S2 + XSTG + 4608, "exchange area");
constexpr u32 OP_BYTES = 128u * 36u * 1024u;
constexpr u32 GATE_OFF = OP_BYTES;
constexpr u32 NON_OFF = GATE_OFF + 8192;
constexpr u32 INB_OFF = NON_OFF + 1024;
constexpr u32 WS_BYTES = INB_OFF + 256 * 512;
constexpr float CEXP = 0.23570226039551584f * 1.4426950408889634f;
constexpr int AUX_SC1 = 16;

DI u32 pk(float a, float b) { f32x2 v = {a, b}; bf16x2 r = __builtin_convertvector(v, bf16x2); return __builtin_bit_cast(u32, r); }
DI f32x4 mfma(u32x4 a, u32x4 b, f32x4 c) { return __builtin_amdgcn_mfma_f32_16x16x32_bf16(__builtin_bit_cast(bf16x8, a), __builtin_bit_cast(bf16x8, b), c, 0, 0, 0); }
template <bool BI> DI void mfma_acc(f32x4& c, u32x4 a, u32x4 b) {
  if constexpr (BI) c = mfma(a, b, c);
  else asm volatile("v_mfma_f32_16x16x32_bf16 %0, %1, %2, %0" : "+v"(c) : "v"(a), "v"(b));
}
DI u32x2 trd(u32 a) { return __builtin_bit_cast(u32x2, __builtin_amdgcn_ds_read_tr16_b64_v4i16((LAS s16x4*)(uintptr_t)a)); }
DI u32x4 lrd128(u32 a) { return *(LAS u32x4*)(uintptr_t)a; }
DI u32x2 lrd64(u32 a) { return *(LAS u32x2*)(uintptr_t)a; }
DI void lwr64(u32 a, f32x4 v) { *(LAS u32x2*)(uintptr_t)a = (u32x2){pk(v[0], v[1]), pk(v[2], v[3])}; }
DI void lwr128(u32 a, f32x4 v) { *(LAS f32x4*)(uintptr_t)a = v; }
DI void dma16(__amdgpu_buffer_rsrc_t rs, u32 voff, u32 soff, u32 ldsdst) {
  asm volatile("s_mov_b32 m0, %0\n\ts_nop 0\n\tbuffer_load_dwordx4 %1, %2, %3 offen lds" :: "s"(ldsdst), "v"(voff), "s"(rs), "s"(soff) : "memory");
}
template <int N> DI void wait_vm() { asm volatile("s_waitcnt vmcnt(%0)" :: "i"(N) : "memory"); }
DI void bar() { asm volatile("s_barrier" ::: "memory"); }
DI void lgkm0() { asm volatile("s_waitcnt lgkmcnt(0)" ::: "memory"); }
#define STAMP(i) do { if (PROBE == (i)) { prt = (PROBE_CYC ? __builtin_amdgcn_s_memtime() : __builtin_amdgcn_s_memrealtime()); __builtin_amdgcn_s_waitcnt(0xC07F); } } while (0)
#define RSTAMP(i)
#define STAMP_DECL unsigned long long prt = 0
#define STAMP_OUT() do { if (PROBE && wid == 0) { while ((PROBE_CYC ? __builtin_amdgcn_s_memtime() : __builtin_amdgcn_s_memrealtime()) - prt < (PROBE_CYC ? 80000 : 4000)) __builtin_amdgcn_s_sleep(2); } } while (0)
DI float dot2(u32 a, u32 b, float c) { return __builtin_amdgcn_fdot2_f32_bf16(__builtin_bit_cast(bf16x2, a), __builtin_bit_cast(bf16x2, b), c, false); }
DI float fmx(float a, float b) { return __builtin_elementwise_maximum(a, b); }
DI void sw32(float& a, float& b) { auto r = __builtin_amdgcn_permlane32_swap(__float_as_uint(a), __float_as_uint(b), false, false); a = __uint_as_float(r[0]); b = __uint_as_float(r[1]); }
DI void sw16(float& a, float& b) { auto r = __builtin_amdgcn_permlane16_swap(__float_as_uint(a), __float_as_uint(b), false, false); a = __uint_as_float(r[0]); b = __uint_as_float(r[1]); }
DI int nxt9(int s) { return s == 8 ? 0 : s + 1; }
DI int nxtslot(int s) { return s == S0 ? S1 : (s == S1 ? S2 : S0); }
DI __amdgpu_buffer_rsrc_t mkrs(const void* p, u32 n) { return __builtin_amdgcn_make_buffer_rsrc((void*)p, 0, n, 0x00020000); }

DI void wo_block(const u16* woa, const u16* wob, u32 voR, u32 voT, u32 lds0, int hf, int idx, int rot) {
  const bool br = idx >= 9;
  int ks = 9 * hf + (br ? idx - 9 : idx) + rot; ks = ks >= 18 ? ks - 18 : ks;
  const int kq = ks >= 9, kk = kq ? ks - 9 : ks;
  const bool tl = kk == 8;
  const u32 so = (u32)((kq ? 288 : 0) + (tl ? 64 : 72 * (kk >> 1) + 32 * (kk & 1))) * 1152u;
  const u32 dst = lds0 + hf * 73728 + idx * 4096;
  const __amdgpu_buffer_rsrc_t rs = mkrs(br ? wob : woa, 576 * 1152);
  const u32 vo = tl ? voT : voR;
  dma16(rs, vo, so, dst);
  dma16(rs, vo, so + (tl ? 144u : 8u) * 1152u, dst + 1024);
  dma16(rs, vo, so + (tl ? 4u : 16u) * 1152u, dst + 2048);
  dma16(rs, vo, so + (tl ? 148u : 24u) * 1152u, dst + 3072);
}

__global__ __launch_bounds__(512, 2) void fused(const u16* __restrict__ x, const u16* __restrict__ wqa, const u16* __restrict__ woa,
                                                const u16* __restrict__ wqb, const u16* __restrict__ wob, const u16* __restrict__ gw,
                                                u16* __restrict__ out, char* __restrict__ ws) {
  extern __shared__ __attribute__((aligned(16))) char lds[];
  asm volatile("" :: "s"(x), "s"(wqa), "s"(woa), "s"(wqb), "s"(wob), "s"(gw), "s"(out), "s"(ws));
  const int tid = threadIdx.x, lane = tid & 63;
  const int wid = __builtin_amdgcn_readfirstlane(tid >> 6);
  STAMP_DECL; RSTAMP(0); STAMP(1);
  const int b = blockIdx.x, xcd = b & 7, bi = b >> 3;
  const int cp = 8 * (xcd & 1) + (bi & 7), hg = 4 * (xcd >> 1) + (bi >> 3);
  const int par = cp & 1, chunk = cp >> 1;
  const int hq = hg & 7;
  int s0 = ROT ? (bi & 7) + 2 * (bi >> 3) : 0; s0 = s0 >= 9 ? s0 - 9 : s0;
  const int sg = cp >> 2, mi = 16 * (hg >> 2) + 4 * (cp & 3) + (hg & 3);
  const int rg = (mi * 57) >> 9, ct = mi - 9 * rg;
  const int rot2 = ROT2 ? 2 * ct : 0;
  const int c = lane & 15, g = lane >> 4, q = (lane >> 2) & 3, p = lane & 3;
  const int r8 = lane >> 3, sp8 = lane & 7;
  const u32 lds0 = (u32)(uintptr_t)(LAS char*)lds;
  const __amdgpu_buffer_rsrc_t rsS = mkrs(ws, WS_BYTES);
  const f32x4 z4 = {0.f, 0.f, 0.f, 0.f};
  u32 nonce = 0, vn = 0;
  f32x4 bias; u32 mq[5][2], mP; u32 voR, voT;
  auto setup = [&]() {
    const float NINF = -__builtin_inff();
    const bool first = (chunk == 0 && wid == 0);
#pragma unroll
    for (int r = 0; r < 4; ++r) {
      const int i = 4 * g + r;
      const bool ok = (i <= c && i >= c - 7) || (i >= c + 9 && !first);
      bias[r] = ok ? 0.f : NINF;
    }
    const u32 lo = c < 8 ? ~0u : 0u;
    const u32 pr[5] = {~0u, g == 0 ? ~0u : 0u, g >= 1 ? ~0u : 0u, g <= 1 ? ~0u : 0u, g >= 2 ? ~0u : 0u};
#pragma unroll
    for (int i = 0; i < 5; ++i) { mq[i][0] = pr[i] & lo; mq[i][1] = pr[i] & ~lo; PIN(mq[i][0]); PIN(mq[i][1]); }
    mP = ~mq[4][0]; PIN(mP);
    PIN(bias);
    const u32 woc = (u32)(ct * 128 + ((sp8 ^ (2 * ((r8 >> 1) & 3))) * 16));
    voR = (u32)r8 * 1152u + woc; voT = (u32)(72 * (r8 >> 2) + (r8 & 3)) * 1152u + woc; PIN(voR); PIN(voT);
  };

  if (wid >= 4) {
    if (wid < 7) {
      const int a = wid - 4;
      const __amdgpu_buffer_rsrc_t rsX = mkrs(x, 2048 * 1152);
      const __amdgpu_buffer_rsrc_t rsW = mkrs((hg >> 3) ? wqb : wqa, 576 * 3456);
      const u32 vA = ((u32)(2 * (128 * chunk + r8) + par) * 1152u + (u32)(sp8 ^ (r8 >> 1)) * 16u) ^ (u32)((a & 1) * 64), vB = vA ^ 64u;
      int jh = 128 * chunk - 8 + r8; jh = jh < 0 ? 0 : jh;
      const u32 v5 = a == 0 ? vB + 15u * 18432u : (u32)(2 * jh + par) * 1152u + (u32)(sp8 ^ (r8 & 6)) * 16u;
      const u32 xso = a * 18432, xd = lds0 + a * 1024, xd5 = lds0 + (a == 0 ? 15 : 16) * 1024;
      auto xlo = [&](int st, int sl) {
        dma16(rsX, vA, st * 128 + xso, xd + sl); dma16(rsX, vB, st * 128 + xso + 3 * 18432, xd + sl + 3072); dma16(rsX, vA, st * 128 + xso + 6 * 18432, xd + sl + 6144);
      };
      auto xhi = [&](int st, int sl) {
        dma16(rsX, vB, st * 128 + xso + 9 * 18432, xd + sl + 9216); dma16(rsX, vA, st * 128 + xso + 12 * 18432, xd + sl + 12288); dma16(rsX, v5, st * 128, xd5 + sl);
      };
      u32 vw[9];
      const u32 wso = a * 1152 + hq * 144, wd = lds0 + XSTG + a * WPART;
      xlo(s0, S0); xhi(s0, S0);
      SBAR();
#pragma unroll
      for (int i = 0; i < 9; ++i) {
        const u32 ci = (u32)(64 * i) + lane;
        const u32 pos = __umul24(ci, 7282u) >> 16, ch = ci - __umul24(pos, 9u);
        const u32 row = (pos & ~7u) | ((pos & 1u) << 2) | ((pos & 7u) >> 1);
        vw[i] = __umul24(row, 3456u) + ch * 16u;
        dma16(rsW, vw[i], s0 * (64 * 3456) + wso, wd + i * 1024);
        SBAR();
      }
      int st = nxt9(s0), sl = S1;
      xlo(st, sl);
      setup();
#pragma unroll 1
      for (int it = 0; it < 8; ++it) {
        wait_vm<7>(); bar();
#pragma unroll
        for (int i = 0; i < 5; ++i) dma16(rsW, vw[i], st * (64 * 3456) + wso, wd + sl + i * 1024);
        xhi(st, sl);
        wait_vm<11>(); bar();
#pragma unroll
        for (int i = 5; i < 9; ++i) dma16(rsW, vw[i], st * (64 * 3456) + wso, wd + sl + i * 1024);
        st = nxt9(st); sl = nxtslot(sl);
        if (it < 7) xlo(st, sl);
      }
      wait_vm<4>();
    } else {
      const u32x2 inb0 = __builtin_amdgcn_raw_buffer_load_b64(rsS, INB_OFF + (u32)(sg * 64 + mi) * 512u + lane * 8u, 0, AUX_SC1);
      const u32 non0 = __builtin_amdgcn_raw_buffer_load_b32(rsS, NON_OFF + (u32)(sg * 64 + mi) * 4u, 0, AUX_SC1);
      setup();
      bar(); bar();
      nonce = __builtin_amdgcn_readfirstlane(non0) + 1u;
      while (__ballot(inb0[0] == nonce) != 0ull) ++nonce;
      if (lane == 0) __builtin_amdgcn_raw_buffer_store_b32(nonce, rsS, NON_OFF + (u32)(sg * 64 + mi) * 4u, 0, AUX_SC1);
      bar();
      const int jl = 8 * hg + r8;
      const u16* xr = x + (size_t)(2 * (128 * chunk + jl) + par) * 576;
      u32x4 xv[9], gv[9];
#pragma unroll
      for (int i = 0; i < 9; ++i) { xv[i] = *(const u32x4*)(xr + (i * 8 + sp8) * 8); gv[i] = *(const u32x4*)(gw + (i * 8 + sp8) * 8); }
      bar(); bar(); bar(); bar();
      float a0 = 0.f, a1 = 0.f;
#pragma unroll
      for (int i = 0; i < 9; ++i) {
        a0 = dot2(xv[i][0], gv[i][0], a0); a1 = dot2(xv[i][1], gv[i][1], a1);
        a0 = dot2(xv[i][2], gv[i][2], a0); a1 = dot2(xv[i][3], gv[i][3], a1);
      }
      float a = a0 + a1; PIN(a);
      bar();
      a += __shfl_xor(a, 1); a += __shfl_xor(a, 2); a += __shfl_xor(a, 4);
      const float gt = __builtin_amdgcn_rcpf(1.f + __builtin_amdgcn_exp2f(-1.4426950408889634f * a));
      if (sp8 == 0) __builtin_amdgcn_raw_buffer_store_b32(__float_as_uint(gt), rsS, GATE_OFF + (u32)(cp * 128 + jl) * 4u, 0, AUX_SC1);
      for (int i = 0; i < 7; ++i) bar();
      vn = __builtin_amdgcn_raw_buffer_load_b32(rsS, NON_OFF + (u32)(sg * 64 + lane) * 4u, 0, AUX_SC1);
      bar();
    }
    bar();
#pragma unroll 1
    for (int idx = wid - 4; idx < 18; idx += 4) {
      wo_block(woa, wob, voR, voT, lds0, 0, idx, rot2);
      if (idx >= 4 && idx < 8) { wait_vm<8>(); bar(); }
    }
    bar();
  } else {
    const int w = wid;
    const u32 rb = lds0 + XSTG + (8 * g + 2 * q) * WROW + 8 * p;
    u32 B[4], BM, BV, XB[2], HB[2];
#pragma unroll
    for (int i = 0; i < 4; ++i) B[i] = rb + 32 * (i ^ w);
    BM = rb + (p >= 2 ? WPART + 112 : 128); BV = rb + (p >= 2 ? WPART + 112 : 2 * WPART + 128);
    XB[0] = lds0 + (32 * w + c) * 128 + ((g ^ (c >> 1)) * 16); XB[1] = XB[0] ^ 64;
    HB[0] = lds0 + (128 + (c & 7)) * 128 + ((g ^ (c & 6)) * 16); HB[1] = HB[0] ^ 64;
    u32 EK[4], EV[4], EM, EH;
#pragma unroll
    for (int i = 0; i < 4; ++i) {
      const int t = i ^ w;
      EK[i] = lds0 + XKQ + (2 * w + 1) * KQB + (t >> 1) * 1024 + lane * 16 + (t & 1) * 8;
      EV[i] = lds0 + XV + t * 4608 + (2 * w + 1) * 512 + lane * 8;
      PIN(EK[i]); PIN(EV[i]);
    }
    EM = lds0 + XKQ + (2 * w + 1) * KQB + 4096 + (lane & 31) * 16 + (g < 2 ? 8 : 0); PIN(EM);
    EH = lds0 + XKQ + 4096 + (4 * g + w + 16 * ((c & 7) >> 2)) * 16 + (c & 3) * 2; PIN(EH);
    setup();
    f32x4 aQ[2][4], aK[2][4], aM[2], aV[2][5], hK, hV, hV4;
#pragma unroll
    for (int t = 0; t < 2; ++t) { aM[t] = z4; aV[t][4] = z4; PIN(aM[t]); PIN(aV[t][4]);
#pragma unroll
      for (int i = 0; i < 4; ++i) { aQ[t][i] = z4; aK[t][i] = z4; aV[t][i] = z4; PIN(aQ[t][i]); PIN(aK[t][i]); PIN(aV[t][i]); } }
    hK = z4; hV = z4; hV4 = z4; PIN(hK); PIN(hV); PIN(hV4);
    PIN(BM); PIN(BV); PIN(XB[0]); PIN(XB[1]); PIN(HB[0]); PIN(HB[1]);
    __builtin_amdgcn_s_waitcnt(0xC07F);
    u32x2 wlo[7], whi[7]; u32x4 xf[2][3];
    auto faddr = [&](auto NC) -> u32 {
      constexpr int n = decltype(NC)::value; constexpr int f = n % 14, ko = (n / 14) * 32 * WROW;
      if constexpr (f < 4) return B[f] + ko;
      else if constexpr (f < 8) return B[f - 4] + ko + WPART;
      else if constexpr (f == 8) return BM + ko;
      else if constexpr (f < 13) return B[f - 9] + ko + 2 * WPART;
      else return BV + ko;
    };
    auto xw = [&](auto FC, auto TC) {
      constexpr int f = decltype(FC)::value, t = decltype(TC)::value;
      if constexpr (t < 2) {
        if constexpr (f < 4) lwr64(EK[f] + t * KQB + 2048, aQ[t][f]);
        else if constexpr (f < 8) lwr64(EK[f - 4] + t * KQB, aK[t][f - 4]);
        else if constexpr (f == 8) lwr64(EM + t * KQB, aM[t]);
        else if constexpr (f < 13) lwr64(EV[f - 9] + t * 512, aV[t][f - 9]);
        else lwr64(lds0 + XV + 4 * 4608 + lane * 8 + (2 * w + 1 + t) * 512, aV[t][4]);
      } else {
        if constexpr (f == 4) lwr64(EK[0] - (2 * w + 1) * KQB, hK);
        if constexpr (f == 9) lwr64(EV[0] - (2 * w + 1) * 512, hV);
      }
    };
    STAMP(2);
    bar();
    STAMP(3);
    xf[0][0] = lrd128(XB[0]); xf[0][1] = lrd128(XB[0] + 2048);
    sfor<0, PD>([&](auto NC) { constexpr int n = decltype(NC)::value; const u32 a = faddr(NC); wlo[n % 7] = trd(a); whi[n % 7] = trd(a + WROW); if constexpr (n == 1) xf[0][2] = lrd128(HB[0]); SBAR(); });
    auto stage = [&](auto LC, int d) {
      constexpr bool LAST = decltype(LC)::value;
      sfor<0, 28>([&](auto NC) {
        constexpr int n = decltype(NC)::value;
        constexpr int f = n % 14, k2 = n / 14, nn = (n + PD) % 28, kn = nn / 14;
        constexpr bool RD = !(LAST && n + PD >= 28), BI = LAST && k2 == 1;
        constexpr int fx = f - 2;
        if constexpr (nn == 0 || nn == 14) { SBAR(); bar(); SBAR(); }
        const u32x4 wv = {wlo[n % 7][0], wlo[n % 7][1], whi[n % 7][0], whi[n % 7][1]};
        const u32x4 x0 = xf[k2][0], x1 = xf[k2][1], xh = xf[k2][2];
        const u32 a = faddr(IC<nn>{});
        if constexpr (f < 4) mfma_acc<BI>(aQ[0][f], wv, x0); else if constexpr (f < 8) mfma_acc<BI>(aK[0][f - 4], wv, x0);
        else if constexpr (f == 8) mfma_acc<BI>(aM[0], wv, x0); else mfma_acc<BI>(aV[0][f - 9], x0, wv);
        SBAR();
        if constexpr (RD) wlo[nn % 7] = trd(a);
        if constexpr (BI && fx >= 0) xw(IC<(fx >= 0 ? fx : 0)>{}, IC<0>{});
        SBAR();
        if constexpr (f < 4) mfma_acc<BI>(aQ[1][f], wv, x1); else if constexpr (f < 8) mfma_acc<BI>(aK[1][f - 4], wv, x1);
        else if constexpr (f == 8) mfma_acc<BI>(aM[1], wv, x1); else mfma_acc<BI>(aV[1][f - 9], x1, wv);
        SBAR();
        if constexpr (RD) whi[nn % 7] = trd(a + WROW);
        if constexpr (BI && fx >= 0) { xw(IC<(fx >= 0 ? fx : 0)>{}, IC<1>{}); xw(IC<(fx >= 0 ? fx : 0)>{}, IC<2>{}); }
        if constexpr (!LAST) {
          if constexpr (nn >= 23 && nn <= 26) { B[nn - 23] += d; PIN(B[nn - 23]); }
          if constexpr (nn == 22) { BM += d; PIN(BM); }
          if constexpr (nn == 27) { BV += d; PIN(BV); }
          if constexpr (nn == 7) { XB[0] += d; PIN(XB[0]); }
          if constexpr (nn == 8) { HB[0] += d; PIN(HB[0]); }
          if constexpr (nn == 20) { XB[1] += d; PIN(XB[1]); }
          if constexpr (nn == 21) { HB[1] += d; PIN(HB[1]); }
        }
        SBAR();
        if constexpr (f == 4) mfma_acc<BI>(hK, wv, xh);
        if constexpr (f == 9) mfma_acc<BI>(hV, xh, wv);
        if constexpr (f == 13) mfma_acc<BI>(hV4, xh, wv);
        SBAR();
        if constexpr (RD) {
          if constexpr (nn % 14 == 1) xf[kn][0] = lrd128(XB[kn]);
          if constexpr (nn % 14 == 2) xf[kn][1] = lrd128(XB[kn] + 2048);
          if constexpr (nn % 14 == 5) xf[kn][2] = lrd128(HB[kn]);
        }
        SBAR();
      });
    };
    int sl = S0;
#pragma unroll 1
    for (int it = 0; it < 8; ++it) { const int sn = nxtslot(sl), d = sn - sl; sl = sn; stage(IC<0>{}, d); }
    STAMP(4);
    stage(IC<1>{}, 0);
    xw(IC<12>{}, IC<0>{}); xw(IC<12>{}, IC<1>{}); xw(IC<13>{}, IC<0>{}); xw(IC<13>{}, IC<1>{});
    lwr64(lds0 + XV + 4 * 4608 + lane * 8, hV4);
    {
      const float hv = (w & 2) ? ((w & 1) ? hV4[3] : hV4[2]) : ((w & 1) ? hV4[1] : hV4[0]);
      if (c >= 8) *(LAS u16*)(uintptr_t)EH = (u16)(pk(hv, 0.f) & 0xffffu);
    }
    lgkm0();
  }
  STAMP(5);
  bar();
  STAMP(6);
  {
    const u32 kq = lds0 + XKQ + (wid + 1) * KQB + lane * 16;
    const u32 ks = kq - (c >= 8 ? KQB : 0);
    const u32 fix = g >= 2 ? 512 : 0;
    const u32 va = lds0 + XV + wid * 512 + lane * 8;
    u32x10 Ks, Kc; u32 Q[10];
    { const u32x4 a = lrd128(kq + 2048), b0 = lrd128(ks), c0 = lrd128(kq), a2 = lrd128(kq + 3072), b1 = lrd128(ks + 1024), c1 = lrd128(kq + 1024);
      const u32x4 tq = lrd128(kq - fix + 4096); const u32x2 tp = lrd64(ks - fix + 4096);
#pragma unroll
      for (int i = 0; i < 4; ++i) { Q[i] = a[i]; Q[4 + i] = a2[i]; Ks[i] = b0[i]; Ks[4 + i] = b1[i]; Kc[i] = c0[i]; Kc[4 + i] = c1[i]; }
      Kc[8] = tq[0]; Kc[9] = tq[1]; Q[8] = tq[2]; Q[9] = tq[3]; Ks[8] = tp[0]; Ks[9] = tp[1]; }
    u32x4 Vo[5];
#pragma unroll
    for (int t = 0; t < 5; ++t) { const u32x2 a = lrd64(va + t * 4608), bb = lrd64(va + t * 4608 + 512); Vo[t] = (u32x4){a[0], a[1], bb[0], bb[1]}; }
    PIN(Ks); PIN(Kc);
    f32x4 sc[4]; float mx[4];
    sfor<0, 4>([&](auto HC) {
      constexpr int h = decltype(HC)::value;
      constexpr int ia0 = h == 0 ? 0 : (h == 3 ? 4 : 2), ia1 = h <= 1 ? 0 : 2, ib0 = h <= 1 ? 1 : 3, ib1 = h == 0 ? -1 : (h == 3 ? 3 : 1);
      u32x4 qo[2];
#pragma unroll
      for (int s = 0; s < 2; ++s) {
        qo[s][0] = Q[2 * h] & mq[ia0][s]; qo[s][1] = Q[2 * h + 1] & mq[ia1][s]; qo[s][2] = Q[2 * h + 2] & mq[ib0][s];
        if constexpr (ib1 >= 0) qo[s][3] = Q[2 * h + 3] & mq[ib1 >= 0 ? ib1 : 0][s]; else qo[s][3] = 0u;
      }
      const u32x4 k0 = __builtin_shufflevector(Ks, Ks, 2 * h, 2 * h + 1, 2 * h + 2, 2 * h + 3);
      const u32x4 k1 = __builtin_shufflevector(Kc, Kc, 2 * h, 2 * h + 1, 2 * h + 2, 2 * h + 3);
      sc[h] = mfma(k1, qo[1], mfma(k0, qo[0], bias));
    });
#pragma unroll
    for (int h = 0; h < 4; ++h) mx[h] = fmx(fmx(fmx(sc[h][0], sc[h][1]), sc[h][2]), sc[h][3]);
    { sw32(mx[0], mx[1]); float m01 = fmx(mx[0], mx[1]); sw32(mx[2], mx[3]); float m23 = fmx(mx[2], mx[3]);
      sw16(m01, m23); float R = fmx(m01, m23) * CEXP;
      float a = R, bb = R; sw16(a, bb); mx[0] = a; mx[1] = a; sw32(mx[0], mx[1]); mx[2] = bb; mx[3] = bb; sw32(mx[2], mx[3]); }
    f32x4 olo[4], ohi[4]; float inv[4];
    const u32x4 ones = {0x3f803f80u, 0x3f803f80u, 0x3f803f80u, 0x3f803f80u};
    sfor<0, 4>([&](auto HC) {
      constexpr int h = decltype(HC)::value;
      float e[4];
#pragma unroll
      for (int r = 0; r < 4; ++r) e[r] = __builtin_amdgcn_exp2f(__builtin_fmaf(sc[h][r], CEXP, -mx[h]));
      const u32 p0 = pk(e[0], e[1]), p1 = pk(e[2], e[3]);
      const u32x4 po = {p0 & mq[4][0], p1 & mq[4][0], p0 & mP, p1 & mP};
      const f32x4 sm = mfma(ones, po, z4);
      olo[h] = mfma(Vo[h], po, z4); ohi[h] = mfma(Vo[h + 1], po, z4);
      inv[h] = __builtin_amdgcn_rcpf(sm[0]);
    });
    u32 op[10];
#pragma unroll
    for (int i = 0; i < 5; ++i)
#pragma unroll
      for (int e = 0; e < 2; ++e) {
        float v0, v1, iv;
        if (i == 0) { v0 = olo[0][2 * e]; v1 = olo[0][2 * e + 1]; iv = inv[0]; }
        else if (i == 4) { v0 = ohi[3][2 * e]; v1 = ohi[3][2 * e + 1]; iv = inv[3]; }
        else { const bool s = 2 * g + e < i; v0 = s ? ohi[i - 1][2 * e] : olo[i][2 * e]; v1 = s ? ohi[i - 1][2 * e + 1] : olo[i][2 * e + 1]; iv = s ? inv[i - 1] : inv[i]; }
        op[2 * i + e] = pk(v0 * iv, v1 * iv);
      }
    const u32 ob = (u32)((cp * 8 + wid) * 36 + 9 * (hg >> 2)) * 1024u;
    __builtin_amdgcn_raw_buffer_store_b128((u32x4){op[0], op[1], op[2], op[3]}, rsS, lane * 16u, ob + (hg & 3) * 2048u, AUX_SC1);
    __builtin_amdgcn_raw_buffer_store_b128((u32x4){op[4], op[5], op[6], op[7]}, rsS, lane * 16u + 1024u, ob + (hg & 3) * 2048u, AUX_SC1);
    if (g < 2) __builtin_amdgcn_raw_buffer_store_b64((u32x2){op[8], op[9]}, rsS, c * 16u + g * 8u, ob + 8192u + (hg & 3) * 256u, AUX_SC1);
  }
  const int br = wid >> 2, ti = wid & 3, ei = (rg < 4) ? (ti ^ (br << 1)) : -1;
  u32 WB[4];
  { const int pos = 4 * g + q, s = (pos >> 1) & 3;
#pragma unroll
    for (int i = 0; i < 4; ++i) { WB[i] = lds0 + br * 36864 + pos * 128 + (((i ^ (2 * br)) ^ s) * 32) + p * 8; PIN(WB[i]); } }
  u32 myin = INB_OFF + (u32)(sg * 64 + mi) * 512u + lane * 8u, dst = INB_OFF + (u32)(sg * 64 + lane) * 512u + mi * 8u;
  PIN(myin); PIN(dst);
  STAMP(7);
  wait_vm<0>();
  STAMP(8);
  bar();
  STAMP(9);
  u32x2 flo[PRE + 2][4], fhi[PRE + 2][4];
  if (wid == 7) {
    __builtin_amdgcn_raw_buffer_store_b64((u32x2){vn, nonce}, rsS, dst, 0, AUX_SC1);
  } else if (wid < 6) {
#pragma unroll 1
    for (int idx = wid; idx < 18; idx += 6) wo_block(woa, wob, voR, voT, lds0, 1, idx, rot2);
  }
  sfor<0, PRE + 1>([&](auto KC) { constexpr int k = decltype(KC)::value;
#pragma unroll
    for (int i = 0; i < 4; ++i) { flo[k][i] = trd(WB[i] + k * 4096); fhi[k][i] = trd(WB[i] + k * 4096 + 2048); } });
  SBAR();
  if (wid == 7) {
#pragma unroll 1
    for (int spin = 0; spin < (1 << 17); ++spin) {
      const u32x2 v = __builtin_amdgcn_raw_buffer_load_b64(rsS, myin, 0, AUX_SC1);
      const bool ok = v[0] == nonce;
      if (ok && v[1] != vn) { vn = v[1]; __builtin_amdgcn_raw_buffer_store_b64((u32x2){vn, nonce}, rsS, dst, 0, AUX_SC1); }
      if (__ballot(ok) == ~0ull) break;
      __builtin_amdgcn_s_sleep(1);
    }
  }
  asm volatile("s_waitcnt vmcnt(0) lgkmcnt(0)" ::: "memory");
  STAMP(10);
  bar();
  STAMP(11);
  if (rg < 7) {
    const int TT = 32 * sg + 4 * rg + ti, TX = 32 * sg + 28 + rg;
    u32x4 of[18], ef[5];
    const u32 ob = (u32)(TT * 36 + 18 * br) * 1024u;
    const int es = ei == 0 ? 0 : (ei == 1 ? 5 : (ei == 2 ? 10 : 14));
    const u32 eb = ei >= 0 ? (u32)(TX * 36 + 18 * br) * 1024u : 0xf0000000u;
    const u32 l16 = lane * 16u;
    sfor<0, 18>([&](auto KC) { constexpr int k = decltype(KC)::value;
      { int ks = k + rot2; ks = ks >= 18 ? ks - 18 : ks;
        of[k] = __builtin_amdgcn_raw_buffer_load_b128(rsS, l16, ob + ks * 1024u, AUX_SC1); }
      if constexpr (k < 5) { int ks = es + k + rot2; ks = ks >= 18 ? ks - 18 : ks;
        ef[k] = __builtin_amdgcn_raw_buffer_load_b128(rsS, l16, (k == 4 && ei >= 2) ? 0xf0000000u : eb + ks * 1024u, AUX_SC1); }
      SBAR(); });
    const float gm = __uint_as_float(__builtin_amdgcn_raw_buffer_load_b32(rsS, GATE_OFF + (u32)(TT * 16 + c) * 4u, 0, AUX_SC1));
    const float gx = __uint_as_float(__builtin_amdgcn_raw_buffer_load_b32(rsS, GATE_OFF + (u32)((TX & 127) * 16 + c) * 4u, 0, AUX_SC1));
    f32x4 am[4], ax[4];
#pragma unroll
    for (int i = 0; i < 4; ++i) { am[i] = z4; ax[i] = z4; }
    constexpr int NW = PRE + 2;
    sfor<0, 18>([&](auto KC) {
      constexpr int k = decltype(KC)::value, kn = k + PRE + 1;
      constexpr int eg = k < 5 ? 0 : (k < 10 ? 1 : (k < 14 ? 2 : 3)), ej = k - (eg == 0 ? 0 : (eg == 1 ? 5 : (eg == 2 ? 10 : 14)));
      if constexpr (kn == 9) {
#pragma unroll
        for (int i = 0; i < 4; ++i) { WB[i] += 73728; PIN(WB[i]); }
      }
      u32x4 wv[4];
#pragma unroll
      for (int i = 0; i < 4; ++i) wv[i] = (u32x4){flo[k % NW][i][0], flo[k % NW][i][1], fhi[k % NW][i][0], fhi[k % NW][i][1]};
#pragma unroll
      for (int i = 0; i < 4; ++i) {
        am[i] = mfma(wv[i], of[k], am[i]);
        SBAR();
        if constexpr (kn < 18) { flo[kn % NW][i] = trd(WB[i] + (kn % 9) * 4096); fhi[kn % NW][i] = trd(WB[i] + (kn % 9) * 4096 + 2048); }
        SBAR();
      }
      if (ei == eg) {
#pragma unroll
        for (int i = 0; i < 4; ++i) ax[i] = mfma(wv[i], ef[ej], ax[i]);
      }
      SBAR();
    });
    STAMP(12);
    const float fm = br ? 1.f - gm : gm, fx = br ? 1.f - gx : gx;
    lwr128(lds0 + MRED + wid * 2048 + lane * 16, am[2] * fm);
    lwr128(lds0 + MRED + wid * 2048 + 1024 + lane * 16, am[3] * fm);
    if (rg < 4) {
#pragma unroll
      for (int i = 0; i < 4; ++i) lwr128(lds0 + br * 36864 + ti * 4096 + ((i ^ (2 * br)) * 1024) + lane * 16, ax[i] * fx);
    }
    lgkm0();
    bar();
    STAMP(13);
    {
      const f32x4 r0 = *(LAS f32x4*)(uintptr_t)(lds0 + MRED + (wid ^ 4) * 2048 + lane * 16);
      const f32x4 r1 = *(LAS f32x4*)(uintptr_t)(lds0 + MRED + (wid ^ 4) * 2048 + 1024 + lane * 16);
      const f32x4 v0 = am[0] * fm + r0, v1 = am[1] * fm + r1;
      auto s0_ = __builtin_amdgcn_permlane16_swap(pk(v0[0], v0[1]), pk(v1[0], v1[1]), false, false);
      auto s1_ = __builtin_amdgcn_permlane16_swap(pk(v0[2], v0[3]), pk(v1[2], v1[3]), false, false);
      const int row = 2 * (128 * (TT >> 4) + 16 * (TT & 7) + c) + ((TT >> 3) & 1);
      *(u32x4*)(out + (size_t)row * 576 + ct * 64 + br * 32 + (g & 1) * 16 + (g >> 1) * 8) = (u32x4){s0_[0], s1_[0], s0_[1], s1_[1]};
    }
    if (rg < 4) {
      f32x2 s = {0.f, 0.f};
#pragma unroll
      for (int j = 0; j < 8; ++j) s += *(LAS f32x2*)(uintptr_t)(lds0 + (j >> 2) * 36864 + (j & 3) * 4096 + wid * 512 + lane * 8);
      const int L = 32 * (wid & 1) + (lane >> 1);
      const int row = 2 * (128 * (TX >> 4) + 16 * (TX & 7) + (L & 15)) + ((TX >> 3) & 1);
      *(u32*)(out + (size_t)row * 576 + ct * 64 + (wid >> 1) * 16 + (L >> 4) * 4 + (lane & 1) * 2) = pk(s[0], s[1]);
    }
  }
  STAMP(14);
  STAMP(15); RSTAMP(16);
  STAMP_OUT();
}

extern "C" void kernel_launch(void* const* d_in, const int* in_sizes, int n_in, void* d_out, int out_size, void* d_ws, size_t ws_size, hipStream_t stream) {
  const u16* x = (const u16*)d_in[0]; const u16* wqa = (const u16*)d_in[1]; const u16* woa = (const u16*)d_in[2];
  const u16* wqb = (const u16*)d_in[4]; const u16* wob = (const u16*)d_in[5]; const u16* gw = (const u16*)d_in[6];
  (void)hipFuncSetAttribute((const void*)fused, hipFuncAttributeMaxDynamicSharedMemorySize, LDS_BYTES);
  fused<<<256, 512, LDS_BYTES, stream>>>(x, wqa, woa, wqb, wob, gw, (u16*)d_out, (char*)d_ws);
}
```

```cpp
#define PROBE 1
#define PROBE_CYC 0
#include <hip/hip_runtime.h>
typedef unsigned short u16; typedef unsigned int u32; typedef unsigned long long u64;
typedef __attribute__((ext_vector_type(8))) __bf16 bf16x8;
typedef __attribute__((ext_vector_type(2))) __bf16 bf16x2;
typedef __attribute__((ext_vector_type(4))) float f32x4;
typedef __attribute__((ext_vector_type(2))) float f32x2;
typedef __attribute__((ext_vector_type(4))) short s16x4;
typedef __attribute__((ext_vector_type(4))) u32 u32x4;
typedef __attribute__((ext_vector_type(2))) u32 u32x2;
typedef __attribute__((ext_vector_type(10))) u32 u32x10;
#define LAS __attribute__((address_space(3)))
#define DI __device__ __forceinline__
#define SBAR() __builtin_amdgcn_sched_barrier(0)
#define PIN(x) asm volatile("" : "+v"(x))
template <int V> struct IC { static constexpr int value = V; };
template <int A, int B, class F> __device__ __forceinline__ void sfor(F&& f) { if constexpr (A < B) { f(IC<A>{}); sfor<A + 1, B>(f); } }
#define ROT 1
#define ROT2 1
#define PD 6
#define PRE 4

constexpr int WROW = 144, WPART = 9232, XSTG = 136 * 128, STG = 45184;
constexpr int S0 = 0, S1 = 73472, S2 = S1 + STG;
constexpr int LDS_BYTES = 163840;
static_assert(S2 + STG == LDS_BYTES, "lds map");
constexpr int KQB = 4608;
constexpr int XKQ = 73728, XV = XKQ + 9 * KQB, MRED = 147456;
static_assert(XV + 5 * 4608 <= S2 + XSTG + 4608, "exchange area");
constexpr u32 OP_BYTES = 128u * 36u * 1024u;
constexpr u32 GATE_OFF = OP_BYTES;
constexpr u32 NON_OFF = GATE_OFF + 8192;
constexpr u32 INB_OFF = NON_OFF + 1024;
constexpr u32 WS_BYTES = INB_OFF + 256 * 512;
constexpr float CEXP = 0.23570226039551584f * 1.4426950408889634f;
constexpr int AUX_SC1 = 16;

DI u32 pk(float a, float b) { f32x2 v = {a, b}; bf16x2 r = __builtin_convertvector(v, bf16x2); return __builtin_bit_cast(u32, r); }
DI f32x4 mfma(u32x4 a, u32x4 b, f32x4 c) { return __builtin_amdgcn_mfma_f32_16x16x32_bf16(__builtin_bit_cast(bf16x8, a), __builtin_bit_cast(bf16x8, b), c, 0, 0, 0); }
template <bool BI> DI void mfma_acc(f32x4& c, u32x4 a, u32x4 b) {
  if constexpr (BI) c = mfma(a, b, c);
  else asm volatile("v_mfma_f32_16x16x32_bf16 %0, %1, %2, %0" : "+v"(c) : "v"(a), "v"(b));
}
DI u32x2 trd(u32 a) { return __builtin_bit_cast(u32x2, __builtin_amdgcn_ds_read_tr16_b64_v4i16((LAS s16x4*)(uintptr_t)a)); }
DI u32x4 lrd128(u32 a) { return *(LAS u32x4*)(uintptr_t)a; }
DI u32x2 lrd64(u32 a) { return *(LAS u32x2*)(uintptr_t)a; }
DI void lwr64(u32 a, f32x4 v) { *(LAS u32x2*)(uintptr_t)a = (u32x2){pk(v[0], v[1]), pk(v[2], v[3])}; }
DI void lwr128(u32 a, f32x4 v) { *(LAS f32x4*)(uintptr_t)a = v; }
DI void dma16(__amdgpu_buffer_rsrc_t rs, u32 voff, u32 soff, u32 ldsdst) {
  asm volatile("s_mov_b32 m0, %0\n\ts_nop 0\n\tbuffer_load_dwordx4 %1, %2, %3 offen lds" :: "s"(ldsdst), "v"(voff), "s"(rs), "s"(soff) : "memory");
}
template <int N> DI void wait_vm() { asm volatile("s_waitcnt vmcnt(%0)" :: "i"(N) : "memory"); }
DI void bar() { asm volatile("s_barrier" ::: "memory"); }
DI void lgkm0() { asm volatile("s_waitcnt lgkmcnt(0)" ::: "memory"); }
#define STAMP(i) do { if (PROBE == (i)) { prt = (PROBE_CYC ? __builtin_amdgcn_s_memtime() : __builtin_amdgcn_s_memrealtime()); __builtin_amdgcn_s_waitcnt(0xC07F); } } while (0)
#define RSTAMP(i)
#define STAMP_DECL unsigned long long prt = 0
#define STAMP_OUT() do { if (PROBE && wid == 0 && ((blockIdx.x & 7) == 0)) { while ((PROBE_CYC ? __builtin_amdgcn_s_memtime() : __builtin_amdgcn_s_memrealtime()) - prt < (PROBE_CYC ? 80000 : 4000)) __builtin_amdgcn_s_sleep(2); } } while (0)
DI float dot2(u32 a, u32 b, float c) { return __builtin_amdgcn_fdot2_f32_bf16(__builtin_bit_cast(bf16x2, a), __builtin_bit_cast(bf16x2, b), c, false); }
DI float fmx(float a, float b) { return __builtin_elementwise_maximum(a, b); }
DI void sw32(float& a, float& b) { auto r = __builtin_amdgcn_permlane32_swap(__float_as_uint(a), __float_as_uint(b), false, false); a = __uint_as_float(r[0]); b = __uint_as_float(r[1]); }
DI void sw16(float& a, float& b) { auto r = __builtin_amdgcn_permlane16_swap(__float_as_uint(a), __float_as_uint(b), false, false); a = __uint_as_float(r[0]); b = __uint_as_float(r[1]); }
DI int nxt9(int s) { return s == 8 ? 0 : s + 1; }
DI int nxtslot(int s) { return s == S0 ? S1 : (s == S1 ? S2 : S0); }
DI __amdgpu_buffer_rsrc_t mkrs(const void* p, u32 n) { return __builtin_amdgcn_make_buffer_rsrc((void*)p, 0, n, 0x00020000); }

DI void wo_block(const u16* woa, const u16* wob, u32 voR, u32 voT, u32 lds0, int hf, int idx, int rot) {
  const bool br = idx >= 9;
  int ks = 9 * hf + (br ? idx - 9 : idx) + rot; ks = ks >= 18 ? ks - 18 : ks;
  const int kq = ks >= 9, kk = kq ? ks - 9 : ks;
  const bool tl = kk == 8;
  const u32 so = (u32)((kq ? 288 : 0) + (tl ? 64 : 72 * (kk >> 1) + 32 * (kk & 1))) * 1152u;
  const u32 dst = lds0 + hf * 73728 + idx * 4096;
  const __amdgpu_buffer_rsrc_t rs = mkrs(br ? wob : woa, 576 * 1152);
  const u32 vo = tl ? voT : voR;
  dma16(rs, vo, so, dst);
  dma16(rs, vo, so + (tl ? 144u : 8u) * 1152u, dst + 1024);
  dma16(rs, vo, so + (tl ? 4u : 16u) * 1152u, dst + 2048);
  dma16(rs, vo, so + (tl ? 148u : 24u) * 1152u, dst + 3072);
}

__global__ __launch_bounds__(512, 2) void fused(const u16* __restrict__ x, const u16* __restrict__ wqa, const u16* __restrict__ woa,
                                                const u16* __restrict__ wqb, const u16* __restrict__ wob, const u16* __restrict__ gw,
                                                u16* __restrict__ out, char* __restrict__ ws) {
  extern __shared__ __attribute__((aligned(16))) char lds[];
  unsigned long long prt0; asm volatile("s_memrealtime %0" : "=s"(prt0)); SBAR();
  asm volatile("s_waitcnt lgkmcnt(0)" :: "s"(x), "s"(wqa), "s"(woa), "s"(wqb), "s"(wob), "s"(gw), "s"(out), "s"(ws));
  const int tid = threadIdx.x, lane = tid & 63;
  const int wid = __builtin_amdgcn_readfirstlane(tid >> 6);
  STAMP_DECL; prt = prt0;
  const int b = blockIdx.x, xcd = b & 7, bi = b >> 3;
  const int cp = 8 * (xcd & 1) + (bi & 7), hg = 4 * (xcd >> 1) + (bi >> 3);
  const int par = cp & 1, chunk = cp >> 1;
  const int hq = hg & 7;
  int s0 = ROT ? (bi & 7) + 2 * (bi >> 3) : 0; s0 = s0 >= 9 ? s0 - 9 : s0;
  const int sg = cp >> 2, mi = 16 * (hg >> 2) + 4 * (cp & 3) + (hg & 3);
  const int rg = (mi * 57) >> 9, ct = mi - 9 * rg;
  const int rot2 = ROT2 ? 2 * ct : 0;
  const int c = lane & 15, g = lane >> 4, q = (lane >> 2) & 3, p = lane & 3;
  const int r8 = lane >> 3, sp8 = lane & 7;
  const u32 lds0 = (u32)(uintptr_t)(LAS char*)lds;
  const __amdgpu_buffer_rsrc_t rsS = mkrs(ws, WS_BYTES);
  const f32x4 z4 = {0.f, 0.f, 0.f, 0.f};
  u32 nonce = 0, vn = 0;
  f32x4 bias; u32 mq[5][2], mP; u32 voR, voT;
  auto setup = [&]() {
    const float NINF = -__builtin_inff();
    const bool first = (chunk == 0 && wid == 0);
#pragma unroll
    for (int r = 0; r < 4; ++r) {
      const int i = 4 * g + r;
      const bool ok = (i <= c && i >= c - 7) || (i >= c + 9 && !first);
      bias[r] = ok ? 0.f : NINF;
    }
    const u32 lo = c < 8 ? ~0u : 0u;
    const u32 pr[5] = {~0u, g == 0 ? ~0u : 0u, g >= 1 ? ~0u : 0u, g <= 1 ? ~0u : 0u, g >= 2 ? ~0u : 0u};
#pragma unroll
    for (int i = 0; i < 5; ++i) { mq[i][0] = pr[i] & lo; mq[i][1] = pr[i] & ~lo; PIN(mq[i][0]); PIN(mq[i][1]); }
    mP = ~mq[4][0]; PIN(mP);
    PIN(bias);
    const u32 woc = (u32)(ct * 128 + ((sp8 ^ (2 * ((r8 >> 1) & 3))) * 16));
    voR = (u32)r8 * 1152u + woc; voT = (u32)(72 * (r8 >> 2) + (r8 & 3)) * 1152u + woc; PIN(voR); PIN(voT);
  };

  if (wid >= 4) {
    if (wid < 7) {
      const int a = wid - 4;
      const __amdgpu_buffer_rsrc_t rsX = mkrs(x, 2048 * 1152);
      const __amdgpu_buffer_rsrc_t rsW = mkrs((hg >> 3) ? wqb : wqa, 576 * 3456);
      const u32 vA = ((u32)(2 * (128 * chunk + r8) + par) * 1152u + (u32)(sp8 ^ (r8 >> 1)) * 16u) ^ (u32)((a & 1) * 64), vB = vA ^ 64u;
      int jh = 128 * chunk - 8 + r8; jh = jh < 0 ? 0 : jh;
      const u32 v5 = a == 0 ? vB + 15u * 18432u : (u32)(2 * jh + par) * 1152u + (u32)(sp8 ^ (r8 & 6)) * 16u;
      const u32 xso = a * 18432, xd = lds0 + a * 1024, xd5 = lds0 + (a == 0 ? 15 : 16) * 1024;
      auto xlo = [&](int st, int sl) {
        dma16(rsX, vA, st * 128 + xso, xd + sl); dma16(rsX, vB, st * 128 + xso + 3 * 18432, xd + sl + 3072); dma16(rsX, vA, st * 128 + xso + 6 * 18432, xd + sl + 6144);
      };
      auto xhi = [&](int st, int sl) {
        dma16(rsX, vB, st * 128 + xso + 9 * 18432, xd + sl + 9216); dma16(rsX, vA, st * 128 + xso + 12 * 18432, xd + sl + 12288); dma16(rsX, v5, st * 128, xd5 + sl);
      };
      u32 vw[9];
      const u32 wso = a * 1152 + hq * 144, wd = lds0 + XSTG + a * WPART;
      xlo(s0, S0); xhi(s0, S0);
      SBAR();
#pragma unroll
      for (int i = 0; i < 9; ++i) {
        const u32 ci = (u32)(64 * i) + lane;
        const u32 pos = __umul24(ci, 7282u) >> 16, ch = ci - __umul24(pos, 9u);
        const u32 row = (pos & ~7u) | ((pos & 1u) << 2) | ((pos & 7u) >> 1);
        vw[i] = __umul24(row, 3456u) + ch * 16u;
        dma16(rsW, vw[i], s0 * (64 * 3456) + wso, wd + i * 1024);
        SBAR();
      }
      int st = nxt9(s0), sl = S1;
      xlo(st, sl);
      setup();
#pragma unroll 1
      for (int it = 0; it < 8; ++it) {
        wait_vm<7>(); bar();
#pragma unroll
        for (int i = 0; i < 5; ++i) dma16(rsW, vw[i], st * (64 * 3456) + wso, wd + sl + i * 1024);
        xhi(st, sl);
        wait_vm<11>(); bar();
#pragma unroll
        for (int i = 5; i < 9; ++i) dma16(rsW, vw[i], st * (64 * 3456) + wso, wd + sl + i * 1024);
        st = nxt9(st); sl = nxtslot(sl);
        if (it < 7) xlo(st, sl);
      }
      wait_vm<4>();
    } else {
      const u32x2 inb0 = __builtin_amdgcn_raw_buffer_load_b64(rsS, INB_OFF + (u32)(sg * 64 + mi) * 512u + lane * 8u, 0, AUX_SC1);
      const u32 non0 = __builtin_amdgcn_raw_buffer_load_b32(rsS, NON_OFF + (u32)(sg * 64 + mi) * 4u, 0, AUX_SC1);
      setup();
      bar(); bar();
      nonce = __builtin_amdgcn_readfirstlane(non0) + 1u;
      while (__ballot(inb0[0] == nonce) != 0ull) ++nonce;
      if (lane == 0) __builtin_amdgcn_raw_buffer_store_b32(nonce, rsS, NON_OFF + (u32)(sg * 64 + mi) * 4u, 0, AUX_SC1);
      bar();
      const int jl = 8 * hg + r8;
      const u16* xr = x + (size_t)(2 * (128 * chunk + jl) + par) * 576;
      u32x4 xv[9], gv[9];
#pragma unroll
      for (int i = 0; i < 9; ++i) { xv[i] = *(const u32x4*)(xr + (i * 8 + sp8) * 8); gv[i] = *(const u32x4*)(gw + (i * 8 + sp8) * 8); }
      bar(); bar(); bar(); bar();
      float a0 = 0.f, a1 = 0.f;
#pragma unroll
      for (int i = 0; i < 9; ++i) {
        a0 = dot2(xv[i][0], gv[i][0], a0); a1 = dot2(xv[i][1], gv[i][1], a1);
        a0 = dot2(xv[i][2], gv[i][2], a0); a1 = dot2(xv[i][3], gv[i][3], a1);
      }
      float a = a0 + a1; PIN(a);
      bar();
      a += __shfl_xor(a, 1); a += __shfl_xor(a, 2); a += __shfl_xor(a, 4);
      const float gt = __builtin_amdgcn_rcpf(1.f + __builtin_amdgcn_exp2f(-1.4426950408889634f * a));
      if (sp8 == 0) __builtin_amdgcn_raw_buffer_store_b32(__float_as_uint(gt), rsS, GATE_OFF + (u32)(cp * 128 + jl) * 4u, 0, AUX_SC1);
      for (int i = 0; i < 7; ++i) bar();
      vn = __builtin_amdgcn_raw_buffer_load_b32(rsS, NON_OFF + (u32)(sg * 64 + lane) * 4u, 0, AUX_SC1);
      bar();
    }
    bar();
#pragma unroll 1
    for (int idx = wid - 4; idx < 18; idx += 4) {
      wo_block(woa, wob, voR, voT, lds0, 0, idx, rot2);
      if (idx >= 4 && idx < 8) { wait_vm<8>(); bar(); }
    }
    bar();
  } else {
    const int w = wid;
    const u32 rb = lds0 + XSTG + (8 * g + 2 * q) * WROW + 8 * p;
    u32 B[4], BM, BV, XB[2], HB[2];
#pragma unroll
    for (int i = 0; i < 4; ++i) B[i] = rb + 32 * (i ^ w);
    BM = rb + (p >= 2 ? WPART + 112 : 128); BV = rb + (p >= 2 ? WPART + 112 : 2 * WPART + 128);
    XB[0] = lds0 + (32 * w + c) * 128 + ((g ^ (c >> 1)) * 16); XB[1] = XB[0] ^ 64;
    HB[0] = lds0 + (128 + (c & 7)) * 128 + ((g ^ (c & 6)) * 16); HB[1] = HB[0] ^ 64;
    u32 EK[4], EV[4], EM, EH;
#pragma unroll
    for (int i = 0; i < 4; ++i) {
      const int t = i ^ w;
      EK[i] = lds0 + XKQ + (2 * w + 1) * KQB + (t >> 1) * 1024 + lane * 16 + (t & 1) * 8;
      EV[i] = lds0 + XV + t * 4608 + (2 * w + 1) * 512 + lane * 8;
      PIN(EK[i]); PIN(EV[i]);
    }
    EM = lds0 + XKQ + (2 * w + 1) * KQB + 4096 + (lane & 31) * 16 + (g < 2 ? 8 : 0); PIN(EM);
    EH = lds0 + XKQ + 4096 + (4 * g + w + 16 * ((c & 7) >> 2)) * 16 + (c & 3) * 2; PIN(EH);
    setup();
    f32x4 aQ[2][4], aK[2][4], aM[2], aV[2][5], hK, hV, hV4;
#pragma unroll
    for (int t = 0; t < 2; ++t) { aM[t] = z4; aV[t][4] = z4; PIN(aM[t]); PIN(aV[t][4]);
#pragma unroll
      for (int i = 0; i < 4; ++i) { aQ[t][i] = z4; aK[t][i] = z4; aV[t][i] = z4; PIN(aQ[t][i]); PIN(aK[t][i]); PIN(aV[t][i]); } }
    hK = z4; hV = z4; hV4 = z4; PIN(hK); PIN(hV); PIN(hV4);
    PIN(BM); PIN(BV); PIN(XB[0]); PIN(XB[1]); PIN(HB[0]); PIN(HB[1]);
    __builtin_amdgcn_s_waitcnt(0xC07F);
    u32x2 wlo[7], whi[7]; u32x4 xf[2][3];
    auto faddr = [&](auto NC) -> u32 {
      constexpr int n = decltype(NC)::value; constexpr int f = n % 14, ko = (n / 14) * 32 * WROW;
      if constexpr (f < 4) return B[f] + ko;
      else if constexpr (f < 8) return B[f - 4] + ko + WPART;
      else if constexpr (f == 8) return BM + ko;
      else if constexpr (f < 13) return B[f - 9] + ko + 2 * WPART;
      else return BV + ko;
    };
    auto xw = [&](auto FC, auto TC) {
      constexpr int f = decltype(FC)::value, t = decltype(TC)::value;
      if constexpr (t < 2) {
        if constexpr (f < 4) lwr64(EK[f] + t * KQB + 2048, aQ[t][f]);
        else if constexpr (f < 8) lwr64(EK[f - 4] + t * KQB, aK[t][f - 4]);
        else if constexpr (f == 8) lwr64(EM + t * KQB, aM[t]);
        else if constexpr (f < 13) lwr64(EV[f - 9] + t * 512, aV[t][f - 9]);
        else lwr64(lds0 + XV + 4 * 4608 + lane * 8 + (2 * w + 1 + t) * 512, aV[t][4]);
      } else {
        if constexpr (f == 4) lwr64(EK[0] - (2 * w + 1) * KQB, hK);
        if constexpr (f == 9) lwr64(EV[0] - (2 * w + 1) * 512, hV);
      }
    };
    STAMP(2);
    bar();
    STAMP(3);
    xf[0][0] = lrd128(XB[0]); xf[0][1] = lrd128(XB[0] + 2048);
    sfor<0, PD>([&](auto NC) { constexpr int n = decltype(NC)::value; const u32 a = faddr(NC); wlo[n % 7] = trd(a); whi[n % 7] = trd(a + WROW); if constexpr (n == 1) xf[0][2] = lrd128(HB[0]); SBAR(); });
    auto stage = [&](auto LC, int d) {
      constexpr bool LAST = decltype(LC)::value;
      sfor<0, 28>([&](auto NC) {
        constexpr int n = decltype(NC)::value;
        constexpr int f = n % 14, k2 = n / 14, nn = (n + PD) % 28, kn = nn / 14;
        constexpr bool RD = !(LAST && n + PD >= 28), BI = LAST && k2 == 1;
        constexpr int fx = f - 2;
        if constexpr (nn == 0 || nn == 14) { SBAR(); bar(); SBAR(); }
        const u32x4 wv = {wlo[n % 7][0], wlo[n % 7][1], whi[n % 7][0], whi[n % 7][1]};
        const u32x4 x0 = xf[k2][0], x1 = xf[k2][1], xh = xf[k2][2];
        const u32 a = faddr(IC<nn>{});
        if constexpr (f < 4) mfma_acc<BI>(aQ[0][f], wv, x0); else if constexpr (f < 8) mfma_acc<BI>(aK[0][f - 4], wv, x0);
        else if constexpr (f == 8) mfma_acc<BI>(aM[0], wv, x0); else mfma_acc<BI>(aV[0][f - 9], x0, wv);
        SBAR();
        if constexpr (RD) wlo[nn % 7] = trd(a);
        if constexpr (BI && fx >= 0) xw(IC<(fx >= 0 ? fx : 0)>{}, IC<0>{});
        SBAR();
        if constexpr (f < 4) mfma_acc<BI>(aQ[1][f], wv, x1); else if constexpr (f < 8) mfma_acc<BI>(aK[1][f - 4], wv, x1);
        else if constexpr (f == 8) mfma_acc<BI>(aM[1], wv, x1); else mfma_acc<BI>(aV[1][f - 9], x1, wv);
        SBAR();
        if constexpr (RD) whi[nn % 7] = trd(a + WROW);
        if constexpr (BI && fx >= 0) { xw(IC<(fx >= 0 ? fx : 0)>{}, IC<1>{}); xw(IC<(fx >= 0 ? fx : 0)>{}, IC<2>{}); }
        if constexpr (!LAST) {
          if constexpr (nn >= 23 && nn <= 26) { B[nn - 23] += d; PIN(B[nn - 23]); }
          if constexpr (nn == 22) { BM += d; PIN(BM); }
          if constexpr (nn == 27) { BV += d; PIN(BV); }
          if constexpr (nn == 7) { XB[0] += d; PIN(XB[0]); }
          if constexpr (nn == 8) { HB[0] += d; PIN(HB[0]); }
          if constexpr (nn == 20) { XB[1] += d; PIN(XB[1]); }
          if constexpr (nn == 21) { HB[1] += d; PIN(HB[1]); }
        }
        SBAR();
        if constexpr (f == 4) mfma_acc<BI>(hK, wv, xh);
        if constexpr (f == 9) mfma_acc<BI>(hV, xh, wv);
        if constexpr (f == 13) mfma_acc<BI>(hV4, xh, wv);
        SBAR();
        if constexpr (RD) {
          if constexpr (nn % 14 == 1) xf[kn][0] = lrd128(XB[kn]);
          if constexpr (nn % 14 == 2) xf[kn][1] = lrd128(XB[kn] + 2048);
          if constexpr (nn % 14 == 5) xf[kn][2] = lrd128(HB[kn]);
        }
        SBAR();
      });
    };
    int sl = S0;
#pragma unroll 1
    for (int it = 0; it < 8; ++it) { const int sn = nxtslot(sl), d = sn - sl; sl = sn; stage(IC<0>{}, d); }
    STAMP(4);
    stage(IC<1>{}, 0);
    xw(IC<12>{}, IC<0>{}); xw(IC<12>{}, IC<1>{}); xw(IC<13>{}, IC<0>{}); xw(IC<13>{}, IC<1>{});
    lwr64(lds0 + XV + 4 * 4608 + lane * 8, hV4);
    {
      const float hv = (w & 2) ? ((w & 1) ? hV4[3] : hV4[2]) : ((w & 1) ? hV4[1] : hV4[0]);
      if (c >= 8) *(LAS u16*)(uintptr_t)EH = (u16)(pk(hv, 0.f) & 0xffffu);
    }
    lgkm0();
  }
  STAMP(5);
  bar();
  STAMP(6);
  {
    const u32 kq = lds0 + XKQ + (wid + 1) * KQB + lane * 16;
    const u32 ks = kq - (c >= 8 ? KQB : 0);
    const u32 fix = g >= 2 ? 512 : 0;
    const u32 va = lds0 + XV + wid * 512 + lane * 8;
    u32x10 Ks, Kc; u32 Q[10];
    { const u32x4 a = lrd128(kq + 2048), b0 = lrd128(ks), c0 = lrd128(kq), a2 = lrd128(kq + 3072), b1 = lrd128(ks + 1024), c1 = lrd128(kq + 1024);
      const u32x4 tq = lrd128(kq - fix + 4096); const u32x2 tp = lrd64(ks - fix + 4096);
#pragma unroll
      for (int i = 0; i < 4; ++i) { Q[i] = a[i]; Q[4 + i] = a2[i]; Ks[i] = b0[i]; Ks[4 + i] = b1[i]; Kc[i] = c0[i]; Kc[4 + i] = c1[i]; }
      Kc[8] = tq[0]; Kc[9] = tq[1]; Q[8] = tq[2]; Q[9] = tq[3]; Ks[8] = tp[0]; Ks[9] = tp[1]; }
    u32x4 Vo[5];
#pragma unroll
    for (int t = 0; t < 5; ++t) { const u32x2 a = lrd64(va + t * 4608), bb = lrd64(va + t * 4608 + 512); Vo[t] = (u32x4){a[0], a[1], bb[0], bb[1]}; }
    PIN(Ks); PIN(Kc);
    f32x4 sc[4]; float mx[4];
    sfor<0, 4>([&](auto HC) {
      constexpr int h = decltype(HC)::value;
      constexpr int ia0 = h == 0 ? 0 : (h == 3 ? 4 : 2), ia1 = h <= 1 ? 0 : 2, ib0 = h <= 1 ? 1 : 3, ib1 = h == 0 ? -1 : (h == 3 ? 3 : 1);
      u32x4 qo[2];
#pragma unroll
      for (int s = 0; s < 2; ++s) {
        qo[s][0] = Q[2 * h] & mq[ia0][s]; qo[s][1] = Q[2 * h + 1] & mq[ia1][s]; qo[s][2] = Q[2 * h + 2] & mq[ib0][s];
        if constexpr (ib1 >= 0) qo[s][3] = Q[2 * h + 3] & mq[ib1 >= 0 ? ib1 : 0][s]; else qo[s][3] = 0u;
      }
      const u32x4 k0 = __builtin_shufflevector(Ks, Ks, 2 * h, 2 * h + 1, 2 * h + 2, 2 * h + 3);
      const u32x4 k1 = __builtin_shufflevector(Kc, Kc, 2 * h, 2 * h + 1, 2 * h + 2, 2 * h + 3);
      sc[h] = mfma(k1, qo[1], mfma(k0, qo[0], bias));
    });
#pragma unroll
    for (int h = 0; h < 4; ++h) mx[h] = fmx(fmx(fmx(sc[h][0], sc[h][1]), sc[h][2]), sc[h][3]);
    { sw32(mx[0], mx[1]); float m01 = fmx(mx[0], mx[1]); sw32(mx[2], mx[3]); float m23 = fmx(mx[2], mx[3]);
      sw16(m01, m23); float R = fmx(m01, m23) * CEXP;
      float a = R, bb = R; sw16(a, bb); mx[0] = a; mx[1] = a; sw32(mx[0], mx[1]); mx[2] = bb; mx[3] = bb; sw32(mx[2], mx[3]); }
    f32x4 olo[4], ohi[4]; float inv[4];
    const u32x4 ones = {0x3f803f80u, 0x3f803f80u, 0x3f803f80u, 0x3f803f80u};
    sfor<0, 4>([&](auto HC) {
      constexpr int h = decltype(HC)::value;
      float e[4];
#pragma unroll
      for (int r = 0; r < 4; ++r) e[r] = __builtin_amdgcn_exp2f(__builtin_fmaf(sc[h][r], CEXP, -mx[h]));
      const u32 p0 = pk(e[0], e[1]), p1 = pk(e[2], e[3]);
      const u32x4 po = {p0 & mq[4][0], p1 & mq[4][0], p0 & mP, p1 & mP};
      const f32x4 sm = mfma(ones, po, z4);
      olo[h] = mfma(Vo[h], po, z4); ohi[h] = mfma(Vo[h + 1], po, z4);
      inv[h] = __builtin_amdgcn_rcpf(sm[0]);
    });
    u32 op[10];
#pragma unroll
    for (int i = 0; i < 5; ++i)
#pragma unroll
      for (int e = 0; e < 2; ++e) {
        float v0, v1, iv;
        if (i == 0) { v0 = olo[0][2 * e]; v1 = olo[0][2 * e + 1]; iv = inv[0]; }
        else if (i == 4) { v0 = ohi[3][2 * e]; v1 = ohi[3][2 * e + 1]; iv = inv[3]; }
        else { const bool s = 2 * g + e < i; v0 = s ? ohi[i - 1][2 * e] : olo[i][2 * e]; v1 = s ? ohi[i - 1][2 * e + 1] : olo[i][2 * e + 1]; iv = s ? inv[i - 1] : inv[i]; }
        op[2 * i + e] = pk(v0 * iv, v1 * iv);
      }
    const u32 ob = (u32)((cp * 8 + wid) * 36 + 9 * (hg >> 2)) * 1024u;
    __builtin_amdgcn_raw_buffer_store_b128((u32x4){op[0], op[1], op[2], op[3]}, rsS, lane * 16u, ob + (hg & 3) * 2048u, AUX_SC1);
    __builtin_amdgcn_raw_buffer_store_b128((u32x4){op[4], op[5], op[6], op[7]}, rsS, lane * 16u + 1024u, ob + (hg & 3) * 2048u, AUX_SC1);
    if (g < 2) __builtin_amdgcn_raw_buffer_store_b64((u32x2){op[8], op[9]}, rsS, c * 16u + g * 8u, ob + 8192u + (hg & 3) * 256u, AUX_SC1);
  }
  const int br = wid >> 2, ti = wid & 3, ei = (rg < 4) ? (ti ^ (br << 1)) : -1;
  u32 WB[4];
  { const int pos = 4 * g + q, s = (pos >> 1) & 3;
#pragma unroll
    for (int i = 0; i < 4; ++i) { WB[i] = lds0 + br * 36864 + pos * 128 + (((i ^ (2 * br)) ^ s) * 32) + p * 8; PIN(WB[i]); } }
  u32 myin = INB_OFF + (u32)(sg * 64 + mi) * 512u + lane * 8u, dst = INB_OFF + (u32)(sg * 64 + lane) * 512u + mi * 8u;
  PIN(myin); PIN(dst);
  STAMP(7);
  wait_vm<0>();
  STAMP(8);
  bar();
  STAMP(9);
  u32x2 flo[PRE + 2][4], fhi[PRE + 2][4];
  if (wid == 7) {
    __builtin_amdgcn_raw_buffer_store_b64((u32x2){vn, nonce}, rsS, dst, 0, AUX_SC1);
  } else if (wid < 6) {
#pragma unroll 1
    for (int idx = wid; idx < 18; idx += 6) wo_block(woa, wob, voR, voT, lds0, 1, idx, rot2);
  }
  sfor<0, PRE + 1>([&](auto KC) { constexpr int k = decltype(KC)::value;
#pragma unroll
    for (int i = 0; i < 4; ++i) { flo[k][i] = trd(WB[i] + k * 4096); fhi[k][i] = trd(WB[i] + k * 4096 + 2048); } });
  SBAR();
  if (wid == 7) {
#pragma unroll 1
    for (int spin = 0; spin < (1 << 17); ++spin) {
      const u32x2 v = __builtin_amdgcn_raw_buffer_load_b64(rsS, myin, 0, AUX_SC1);
      const bool ok = v[0] == nonce;
      if (ok && v[1] != vn) { vn = v[1]; __builtin_amdgcn_raw_buffer_store_b64((u32x2){vn, nonce}, rsS, dst, 0, AUX_SC1); }
      if (__ballot(ok) == ~0ull) break;
      __builtin_amdgcn_s_sleep(1);
    }
  }
  asm volatile("s_waitcnt vmcnt(0) lgkmcnt(0)" ::: "memory");
  STAMP(10);
  bar();
  STAMP(11);
  if (rg < 7) {
    const int TT = 32 * sg + 4 * rg + ti, TX = 32 * sg + 28 + rg;
    u32x4 of[18], ef[5];
    const u32 ob = (u32)(TT * 36 + 18 * br) * 1024u;
    const int es = ei == 0 ? 0 : (ei == 1 ? 5 : (ei == 2 ? 10 : 14));
    const u32 eb = ei >= 0 ? (u32)(TX * 36 + 18 * br) * 1024u : 0xf0000000u;
    const u32 l16 = lane * 16u;
    sfor<0, 18>([&](auto KC) { constexpr int k = decltype(KC)::value;
      { int ks = k + rot2; ks = ks >= 18 ? ks - 18 : ks;
        of[k] = __builtin_amdgcn_raw_buffer_load_b128(rsS, l16, ob + ks * 1024u, AUX_SC1); }
      if constexpr (k < 5) { int ks = es + k + rot2; ks = ks >= 18 ? ks - 18 : ks;
        ef[k] = __builtin_amdgcn_raw_buffer_load_b128(rsS, l16, (k == 4 && ei >= 2) ? 0xf0000000u : eb + ks * 1024u, AUX_SC1); }
      SBAR(); });
    const float gm = __uint_as_float(__builtin_amdgcn_raw_buffer_load_b32(rsS, GATE_OFF + (u32)(TT * 16 + c) * 4u, 0, AUX_SC1));
    const float gx = __uint_as_float(__builtin_amdgcn_raw_buffer_load_b32(rsS, GATE_OFF + (u32)((TX & 127) * 16 + c) * 4u, 0, AUX_SC1));
    f32x4 am[4], ax[4];
#pragma unroll
    for (int i = 0; i < 4; ++i) { am[i] = z4; ax[i] = z4; }
    constexpr int NW = PRE + 2;
    sfor<0, 18>([&](auto KC) {
      constexpr int k = decltype(KC)::value, kn = k + PRE + 1;
      constexpr int eg = k < 5 ? 0 : (k < 10 ? 1 : (k < 14 ? 2 : 3)), ej = k - (eg == 0 ? 0 : (eg == 1 ? 5 : (eg == 2 ? 10 : 14)));
      if constexpr (kn == 9) {
#pragma unroll
        for (int i = 0; i < 4; ++i) { WB[i] += 73728; PIN(WB[i]); }
      }
      u32x4 wv[4];
#pragma unroll
      for (int i = 0; i < 4; ++i) wv[i] = (u32x4){flo[k % NW][i][0], flo[k % NW][i][1], fhi[k % NW][i][0], fhi[k % NW][i][1]};
#pragma unroll
      for (int i = 0; i < 4; ++i) {
        am[i] = mfma(wv[i], of[k], am[i]);
        SBAR();
        if constexpr (kn < 18) { flo[kn % NW][i] = trd(WB[i] + (kn % 9) * 4096); fhi[kn % NW][i] = trd(WB[i] + (kn % 9) * 4096 + 2048); }
        SBAR();
      }
      if (ei == eg) {
#pragma unroll
        for (int i = 0; i < 4; ++i) ax[i] = mfma(wv[i], ef[ej], ax[i]);
      }
      SBAR();
    });
    STAMP(12);
    const float fm = br ? 1.f - gm : gm, fx = br ? 1.f - gx : gx;
    lwr128(lds0 + MRED + wid * 2048 + lane * 16, am[2] * fm);
    lwr128(lds0 + MRED + wid * 2048 + 1024 + lane * 16, am[3] * fm);
    if (rg < 4) {
#pragma unroll
      for (int i = 0; i < 4; ++i) lwr128(lds0 + br * 36864 + ti * 4096 + ((i ^ (2 * br)) * 1024) + lane * 16, ax[i] * fx);
    }
    lgkm0();
    bar();
    STAMP(13);
    {
      const f32x4 r0 = *(LAS f32x4*)(uintptr_t)(lds0 + MRED + (wid ^ 4) * 2048 + lane * 16);
      const f32x4 r1 = *(LAS f32x4*)(uintptr_t)(lds0 + MRED + (wid ^ 4) * 2048 + 1024 + lane * 16);
      const f32x4 v0 = am[0] * fm + r0, v1 = am[1] * fm + r1;
      auto s0_ = __builtin_amdgcn_permlane16_swap(pk(v0[0], v0[1]), pk(v1[0], v1[1]), false, false);
      auto s1_ = __builtin_amdgcn_permlane16_swap(pk(v0[2], v0[3]), pk(v1[2], v1[3]), false, false);
      const int row = 2 * (128 * (TT >> 4) + 16 * (TT & 7) + c) + ((TT >> 3) & 1);
      *(u32x4*)(out + (size_t)row * 576 + ct * 64 + br * 32 + (g & 1) * 16 + (g >> 1) * 8) = (u32x4){s0_[0], s1_[0], s0_[1], s1_[1]};
    }
    if (rg < 4) {
      f32x2 s = {0.f, 0.f};
#pragma unroll
      for (int j = 0; j < 8; ++j) s += *(LAS f32x2*)(uintptr_t)(lds0 + (j >> 2) * 36864 + (j & 3) * 4096 + wid * 512 + lane * 8);
      const int L = 32 * (wid & 1) + (lane >> 1);
      const int row = 2 * (128 * (TX >> 4) + 16 * (TX & 7) + (L & 15)) + ((TX >> 3) & 1);
      *(u32*)(out + (size_t)row * 576 + ct * 64 + (wid >> 1) * 16 + (L >> 4) * 4 + (lane & 1) * 2) = pk(s[0], s[1]);
    }
  }
  STAMP(14);
  STAMP(15); RSTAMP(16);
  STAMP_OUT();
}

extern "C" void kernel_launch(void* const* d_in, const int* in_sizes, int n_in, void* d_out, int out_size, void* d_ws, size_t ws_size, hipStream_t stream) {
  const u16* x = (const u16*)d_in[0]; const u16* wqa = (const u16*)d_in[1]; const u16* woa = (const u16*)d_in[2];
  const u16* wqb = (const u16*)d_in[4]; const u16* wob = (const u16*)d_in[5]; const u16* gw = (const u16*)d_in[6];
  (void)hipFuncSetAttribute((const void*)fused, hipFuncAttributeMaxDynamicSharedMemorySize, LDS_BYTES);
  fused<<<256, 512, LDS_BYTES, stream>>>(x, wqa, woa, wqb, wob, gw, (u16*)d_out, (char*)d_ws);
}
```

```cpp
#include <hip/hip_runtime.h>
typedef unsigned short u16;
__device__ __forceinline__ float b2f(u16 v){ return __uint_as_float(((unsigned)v)<<16); }
__device__ __forceinline__ u16 f2b(float f){ unsigned u=__float_as_uint(f); u += 0x7fffu + ((u>>16)&1u); return (u16)(u>>16); }
#define SPIN 800000
__global__ __launch_bounds__(256) void light(const u16* __restrict__ x, const u16* __restrict__ wqa, const u16* __restrict__ woa, const u16* __restrict__ wqb, const u16* __restrict__ wob, const u16* __restrict__ gw, u16* __restrict__ out, char* ws){
  const unsigned long long t0 = __builtin_amdgcn_s_memrealtime();
  const int b = blockIdx.x, t = threadIdx.x, r0 = 8*b - 14;
  u16* qkv = (u16*)(ws + (size_t)b * 262144);
  u16* o = qkv + 22*3456;
  for (int n = t; n < 3456; n += 256) {
    const u16* w = n < 1728 ? wqa + n : wqb + (n - 1728);
    float acc[22];
#pragma unroll
    for (int i = 0; i < 22; ++i) acc[i] = 0.f;
    for (int k = 0; k < 576; ++k) {
      const float wv = b2f(w[k*1728]);
#pragma unroll
      for (int i = 0; i < 22; ++i) { int r = r0 + i; r = r < 0 ? 0 : r; acc[i] += b2f(x[r*576+k]) * wv; }
    }
#pragma unroll
    for (int i = 0; i < 22; ++i) qkv[i*3456+n] = f2b(acc[i]);
  }
  __threadfence(); __syncthreads();
  for (int it = t; it < 512; it += 256) {
    const int i = it >> 6, hb = it & 63, br = hb >> 5, h = hb & 31, s = 8*b + i;
    const u16* q = qkv + (14+i)*3456 + br*1728 + h*18;
    float lg[8], m = -1e30f;
#pragma unroll
    for (int w = 0; w < 8; ++w) { float a = -1e9f; if (s - 2*w >= 0) { const u16* k = qkv + (14+i-2*w)*3456 + br*1728 + 576 + h*18; a = 0; for (int d = 0; d < 18; ++d) a += b2f(q[d])*b2f(k[d]); a *= 0.23570226039551584f; } lg[w] = a; m = fmaxf(m, a); }
    float sum = 0;
#pragma unroll
    for (int w = 0; w < 8; ++w) { lg[w] = __expf(lg[w]-m); sum += lg[w]; }
    for (int d = 0; d < 18; ++d) { float a = 0;
#pragma unroll
      for (int w = 0; w < 8; ++w) a += lg[w]*b2f(qkv[(14+i-2*w)*3456 + br*1728 + 1152 + h*18 + d]);
      o[i*1152 + br*576 + h*18 + d] = f2b(a/sum); }
  }
  __threadfence(); __syncthreads();
  for (int n = t; n < 576; n += 256) for (int i = 0; i < 8; ++i) {
    const int s = 8*b + i; float g = 0, a = 0, c = 0;
    for (int k = 0; k < 576; ++k) { g += b2f(x[s*576+k])*b2f(gw[k]); a += b2f(o[i*1152+k])*b2f(woa[k*576+n]); c += b2f(o[i*1152+576+k])*b2f(wob[k*576+n]); }
    g = 1.f/(1.f+__expf(-g));
    out[s*576+n] = f2b(g*a + (1.f-g)*c);
  }
  if (t < 64) while (__builtin_amdgcn_s_memrealtime() - t0 < SPIN) __builtin_amdgcn_s_sleep(2);
}
extern "C" void kernel_launch(void* const* d_in, const int* in_sizes, int n_in, void* d_out, int out_size, void* d_ws, size_t ws_size, hipStream_t stream){
  light<<<256,256,0,stream>>>((const u16*)d_in[0],(const u16*)d_in[1],(const u16*)d_in[2],(const u16*)d_in[4],(const u16*)d_in[5],(const u16*)d_in[6],(u16*)d_out,(char*)d_ws);
}
```

```cpp
#include <hip/hip_runtime.h>
typedef unsigned short u16; typedef unsigned int u32; typedef unsigned long long u64;
typedef __attribute__((ext_vector_type(8))) __bf16 bf16x8;
typedef __attribute__((ext_vector_type(2))) __bf16 bf16x2;
typedef __attribute__((ext_vector_type(4))) float f32x4;
typedef __attribute__((ext_vector_type(2))) float f32x2;
typedef __attribute__((ext_vector_type(4))) short s16x4;
typedef __attribute__((ext_vector_type(4))) u32 u32x4;
typedef __attribute__((ext_vector_type(2))) u32 u32x2;
typedef __attribute__((ext_vector_type(6))) u32 u32x6;
typedef __attribute__((ext_vector_type(10))) u32 u32x10;
#define LAS __attribute__((address_space(3)))
#define DI __device__ __forceinline__
#define SBAR() __builtin_amdgcn_sched_barrier(0)
#define PIN(x) asm volatile("" : "+v"(x))
template <int V> struct IC { static constexpr int value = V; };
template <int A, int B, class F> __device__ __forceinline__ void sfor(F&& f) { if constexpr (A < B) { f(IC<A>{}); sfor<A + 1, B>(f); } }
#define ROT 2
#define ROT2 1
#define PD 6
#define PRE 4
#define DIAG 0
#define DEPTH 2

constexpr int WROW = 144, WPART = 9232, XSTG = 136 * 128, STG = 45184;
constexpr int LDS_BYTES = 163840;
constexpr int MRED = 147456, XCH = 147456, XCB = 2560;
constexpr u32 OP_BYTES = 128u * 36u * 1024u;
constexpr u32 GATE_OFF = OP_BYTES;
constexpr u32 NON_OFF = GATE_OFF + 8192;
constexpr u32 INB_OFF = NON_OFF + 1024;
constexpr u32 WS_BYTES = INB_OFF + 256 * 512;
constexpr float CEXP = 0.23570226039551584f * 1.4426950408889634f;
constexpr int AUX_SC1 = 16;
constexpr u32 CODE_BYTES = 16896;

DI u32 pk(float a, float b) { f32x2 v = {a, b}; bf16x2 r = __builtin_convertvector(v, bf16x2); return __builtin_bit_cast(u32, r); }
DI f32x4 mfma(u32x4 a, u32x4 b, f32x4 c) { return __builtin_amdgcn_mfma_f32_16x16x32_bf16(__builtin_bit_cast(bf16x8, a), __builtin_bit_cast(bf16x8, b), c, 0, 0, 0); }
template <bool BI> DI void mfma_acc(f32x4& c, u32x4 a, u32x4 b) {
  if constexpr (BI) c = mfma(a, b, c);
  else asm volatile("v_mfma_f32_16x16x32_bf16 %0, %1, %2, %0" : "+v"(c) : "v"(a), "v"(b));
}
DI u32x2 trd(u32 a) { return __builtin_bit_cast(u32x2, __builtin_amdgcn_ds_read_tr16_b64_v4i16((LAS s16x4*)(uintptr_t)a)); }
DI u32x4 trd2(u32 a, int st) { const u32x2 x = trd(a), y = trd(a + st); return (u32x4){x[0], x[1], y[0], y[1]}; }
DI u32x4 lrd128(u32 a) { return *(LAS u32x4*)(uintptr_t)a; }
DI u32x2 lrd64(u32 a) { return *(LAS u32x2*)(uintptr_t)a; }
DI void lwr64(u32 a, u32 x, u32 y) { *(LAS u32x2*)(uintptr_t)a = (u32x2){x, y}; }
DI void lwr64f(u32 a, f32x4 v) { lwr64(a, pk(v[0], v[1]), pk(v[2], v[3])); }
DI void lwr128(u32 a, f32x4 v) { *(LAS f32x4*)(uintptr_t)a = v; }
#define NT 0
#define NT0 ""
#define NT1 ""
DI void dma16(__amdgpu_buffer_rsrc_t rs, u32 voff, u32 soff, u32 ldsdst) {
  asm volatile("s_mov_b32 m0, %0\n\ts_nop 0\n\tbuffer_load_dwordx4 %1, %2, %3 offen" NT0 " lds" :: "s"(ldsdst), "v"(voff), "s"(rs), "s"(soff) : "memory");
}
#define WIN 9
#define WINO 11
template <int WN = WIN> DI void dma16w(__amdgpu_buffer_rsrc_t rs, u32 voff, u32 soff, u32 ldsdst) {
  asm volatile("s_mov_b32 m0, %0\n\ts_waitcnt vmcnt(%4)\n\tbuffer_load_dwordx4 %1, %2, %3 offen" NT1 " lds" :: "s"(ldsdst), "v"(voff), "s"(rs), "s"(soff), "i"(WN) : "memory");
}
template <int N> DI void wait_vm() { asm volatile("s_waitcnt vmcnt(%0)" :: "i"(N) : "memory"); }
DI void bar() { asm volatile("s_barrier" ::: "memory"); }
DI void lgkm0() { asm volatile("s_waitcnt lgkmcnt(0)" ::: "memory"); }
#define STAMP(i)
#define RSTAMP(i)
#define STAMP_DECL
#define STAMP_OUT()
DI float dot2(u32 a, u32 b, float c) { return __builtin_amdgcn_fdot2_f32_bf16(__builtin_bit_cast(bf16x2, a), __builtin_bit_cast(bf16x2, b), c, false); }
DI float fmx(float a, float b) { return __builtin_elementwise_maximum(a, b); }
DI void sw32(float& a, float& b) { auto r = __builtin_amdgcn_permlane32_swap(__float_as_uint(a), __float_as_uint(b), false, false); a = __uint_as_float(r[0]); b = __uint_as_float(r[1]); }
DI void sw16(float& a, float& b) { auto r = __builtin_amdgcn_permlane16_swap(__float_as_uint(a), __float_as_uint(b), false, false); a = __uint_as_float(r[0]); b = __uint_as_float(r[1]); }
DI int nxt9(int s) { return s == 8 ? 0 : s + 1; }
DI int nxtslot(int s) { return s == 2 * STG ? 0 : s + STG; }
DI __amdgpu_buffer_rsrc_t mkrs(const void* p, u32 n) { return __builtin_amdgcn_make_buffer_rsrc((void*)p, 0, n, 0x00020000); }
DI u32 wvoff(u32 ci) {
  const u32 pos = __umul24(ci, 7282u) >> 16, ch = ci - __umul24(pos, 9u);
  const u32 row = (pos & ~7u) | ((pos & 1u) << 2) | ((pos & 7u) >> 1);
  return __umul24(row, 3456u) + ch * 16u;
}

DI void wo_block(const u16* woa, const u16* wob, u32 voR, u32 voT, u32 lds0, int idx, int rot) {
  int kk = (idx >> 2) + rot; kk = kk >= 9 ? kk - 9 : kk;
  const bool tl = kk == 8;
  const u32 so = (u32)(((idx & 1) ? 288 : 0) + (tl ? 64 : 36 * kk - 4 * (kk & 1))) * 1152u;
  const u32 dst = lds0 + idx * 4096;
  const __amdgpu_buffer_rsrc_t rs = mkrs((idx & 2) ? wob : woa, 576 * 1152);
  const u32 vo = tl ? voT : voR;
  dma16w<WINO>(rs, vo, so, dst);
  dma16w<WINO>(rs, vo, so + (tl ? 144u : 8u) * 1152u, dst + 1024);
  dma16w<WINO>(rs, vo, so + (tl ? 4u : 16u) * 1152u, dst + 2048);
  dma16w<WINO>(rs, vo, so + (tl ? 148u : 24u) * 1152u, dst + 3072);
}

__global__ __launch_bounds__(512, 2) void fused(const u16* __restrict__ x, const u16* __restrict__ wqa, const u16* __restrict__ woa,
                                                const u16* __restrict__ wqb, const u16* __restrict__ wob, const u16* __restrict__ gw,
                                                u16* __restrict__ out, char* __restrict__ ws) {
  extern __shared__ __attribute__((aligned(16))) char lds[];
  const int tid = threadIdx.x, lane = tid & 63;
  const int wid = __builtin_amdgcn_readfirstlane(tid >> 6);
  STAMP_DECL; RSTAMP(0); STAMP(1);
  const int b = blockIdx.x, xcd = b & 7, bi = b >> 3;
  const int cp = 8 * (xcd & 1) + (bi & 7), hg = 4 * (xcd >> 1) + (bi >> 3);
  const int par = cp & 1, chunk = cp >> 1;
  const int hq = hg & 7;
  int s0 = 0;
  if (ROT == 1) { s0 = (bi & 7) + 2 * (bi >> 3); s0 = s0 >= 9 ? s0 - 9 : s0; }
  if (ROT == 2) s0 = 2 * (((bi & 7) + (bi >> 3)) & 3);
  if (ROT == 3) s0 = 3 * (((bi & 7) + (bi >> 3)) % 3);
  const int r8 = lane >> 3, sp8 = lane & 7;
  const u32 lds0 = (u32)(uintptr_t)(LAS char*)lds;
  {
    unsigned long long pc; asm volatile("s_getpc_b64 %0" : "=s"(pc));
    if (wid == 7 && lane < 5) {
      const unsigned long long pa = (pc & ~255ull) + min((unsigned)(bi * 5 + lane) * 128u, CODE_BYTES - 128u);
      asm volatile("s_mov_b32 m0, %0\n\ts_nop 0\n\tglobal_load_lds_dword %1, off" :: "s"(lds0 + 3 * STG + 1024), "v"(pa) : "memory");
    }
  }
  u32 voE = (u32)(2 * (128 * chunk + r8) + par) * 1152u + (u32)(sp8 ^ (r8 >> 1)) * 16u;
  int jh = 128 * chunk - 8 + r8; jh = jh < 0 ? 0 : jh;
  u32 voH = (u32)(2 * jh + par) * 1152u + (u32)(sp8 ^ (r8 & 6)) * 16u;
  const int u0 = 2 * wid, p0 = (u0 * 52) >> 8, i0 = u0 - 5 * p0, u1 = u0 + 1, p1 = (u1 * 52) >> 8, i1 = u1 - 5 * p1;
  u32 voO = voE ^ 64u, vw0 = wvoff(64 * i0 + lane), vw1 = wvoff(64 * i1 + lane);
  u32 sx = s0 * 128 + wid * 36864, dx = lds0 + wid * 2048;
  u32 sw0 = s0 * (64 * 3456) + p0 * 1152 + hq * 144, dw0 = lds0 + XSTG + p0 * WPART + i0 * 1024;
  u32 sw1 = s0 * (64 * 3456) + p1 * 1152 + hq * 144, dw1 = lds0 + XSTG + p1 * WPART + i1 * 1024;
  asm volatile("" : "+v"(voE), "+v"(voH), "+v"(voO), "+v"(vw0), "+v"(vw1), "+s"(sx), "+s"(dx), "+s"(sw0), "+s"(dw0), "+s"(sw1), "+s"(dw1));
  SBAR();
  const __amdgpu_buffer_rsrc_t rsX = mkrs(x, 2048 * 1152);
  const __amdgpu_buffer_rsrc_t rsW = mkrs((hg >> 3) ? wqb : wqa, 576 * 3456);
  {
    dma16(rsX, voE, sx, dx);
    dma16(rsX, voO, sx + 18432, dx + 1024);
    dma16(rsW, vw0, sw0, dw0);
    if (wid < 7) dma16(rsW, vw1, sw1, dw1);
    else dma16(rsX, voH, s0 * 128, lds0 + 16384);
  }
  SBAR();
  STAMP(25);
  asm volatile("" :: "s"(woa), "s"(wob), "s"(gw), "s"(out), "s"(ws));
  const int sg = cp >> 2, mi = 16 * (hg >> 2) + 4 * (cp & 3) + (hg & 3);
  const int rg = (mi * 57) >> 9, ct = mi - 9 * rg;
  int rot2 = ROT2 ? ct + 2 * (rg & 1) + 4 * (sg & 1) : 0; rot2 = rot2 >= 9 ? rot2 - 9 : rot2;
  const int c = lane & 15, g = lane >> 4, q = (lane >> 2) & 3, p = lane & 3;
  const __amdgpu_buffer_rsrc_t rsS = mkrs(ws, WS_BYTES);
  const f32x4 z4 = {0.f, 0.f, 0.f, 0.f};
  u32 nonce = 0, vn = 0;
  const u32 rb = lds0 + XSTG + (8 * g + 2 * q) * WROW + 8 * p;

  if (wid >= 4) {
    __builtin_amdgcn_s_setprio(2);
    const int hw = wid - 4;
    u32 HB = lds0 + (128 + (c & 7)) * 128 + ((g ^ (c & 6)) * 16), HW = rb + WPART + 32 * hw;
    f32x4 hK = z4, hV = z4;
    const u32 ek = lds0 + XCH + (g * 8 + (c & 7)) * 8, ev = lds0 + XCH + 1280 + (lane & 31) * 8;
    if (wid < 7) {
      const int a = hw;
      const u32 vA = voE ^ (u32)((a & 1) * 64), vB = vA ^ 64u;
      const u32 v5 = a == 0 ? vB + 15u * 18432u : (a == 1 ? voH : 0x80000000u);
      const u32 xso = a * 18432, xd = lds0 + a * 1024, xd5 = lds0 + (a == 0 ? 15 : 16) * 1024;
      const int sl5m = a == 2 ? 0 : -1;
      auto xlo = [&](int st, int sl) {
        dma16w(rsX, vA, st * 128 + xso, xd + sl); dma16w(rsX, vB, st * 128 + xso + 3 * 18432, xd + sl + 3072); dma16w(rsX, vA, st * 128 + xso + 6 * 18432, xd + sl + 6144);
      };
      auto xhi = [&](int st, int sl) {
        dma16w(rsX, vB, st * 128 + xso + 9 * 18432, xd + sl + 9216); dma16w(rsX, vA, st * 128 + xso + 12 * 18432, xd + sl + 12288); dma16w(rsX, v5, st * 128, a == 2 ? lds0 + 3 * STG : xd5 + sl);
      };
      u32 vw[9];
      const u32 wso = a * 1152 + hq * 144, wd = lds0 + XSTG + a * WPART;
      auto wlo = [&](int st, int sl) {
#pragma unroll
        for (int i = 0; i < 5; ++i) dma16w(rsW, vw[i], st * (64 * 3456) + wso, wd + sl + i * 1024);
      };
      auto whi = [&](int st, int sl) {
#pragma unroll
        for (int i = 5; i < 9; ++i) dma16w(rsW, vw[i], st * (64 * 3456) + wso, wd + sl + i * 1024);
      };
#pragma unroll
      for (int i = 5; i < 9; ++i) { vw[i] = wvoff(64 * i + lane); dma16w(rsW, vw[i], s0 * (64 * 3456) + wso, wd + i * 1024); SBAR(); }
      int st = nxt9(s0), sl = STG;
      xlo(st, sl);
#pragma unroll
      for (int i = 0; i < 5; ++i) vw[i] = wvoff(64 * i + lane);
      if constexpr (DEPTH == 3) { wlo(st, sl); xhi(st, sl); }
      STAMP(2);
      int cs = 0;
#pragma unroll 1
      for (int it = 0; it < 9; ++it) {
        const int st2 = nxt9(st), sl2 = nxtslot(sl);
        if constexpr (DEPTH == 2) { if (it < 8) wait_vm<7>(); else wait_vm<4>(); }
        else { if (it < 8) wait_vm<15>(); else wait_vm<4>(); }
        if (it == 0) STAMP(3);
        bar();
        if (it < 6) STAMP(19 + it);
        { const u32x4 xh = lrd128(HB + cs), fk = trd2(HW + cs, WROW), fv = trd2(HW + cs + WPART, WROW);
          if constexpr (DEPTH == 2) { if (it < 8) { wlo(st, sl); xhi(st, sl); } }
          else { if (it < 8) whi(st, sl); if (it < 7) xlo(st2, sl2); }
          if (it == 8) {
            const u32 woc = (u32)(ct * 128 + ((sp8 ^ (2 * ((r8 >> 1) & 3))) * 16));
            vw[0] = (u32)r8 * 1152u + woc; vw[1] = (u32)(72 * (r8 >> 2) + (r8 & 3)) * 1152u + woc;
            wo_block(woa, wob, vw[0], vw[1], lds0, a, rot2);
          }
          hK = mfma(fk, xh, hK); hV = mfma(xh, fv, hV); }
        if constexpr (DEPTH == 2) { if (it < 8) wait_vm<11>(); else wait_vm<4>(); }
        else { if (it < 7) wait_vm<18>(); else if (it == 7) wait_vm<15>(); else wait_vm<4>(); }
        bar();
        { const u32x4 xh = lrd128((HB ^ 64u) + cs), fk = trd2(HW + cs + 32 * WROW, WROW), fv = trd2(HW + cs + WPART + 32 * WROW, WROW);
          if constexpr (DEPTH == 2) { if (it < 8) whi(st, sl); if (it < 7) xlo(st2, sl2); }
          else { if (it < 7) { wlo(st2, sl2); xhi(st2, sl2); } }
          hK = mfma(fk, xh, hK); hV = mfma(xh, fv, hV); }
        cs = sl; st = st2; sl = sl2;
      }
      if (c >= 8) lwr64f(ek + hw * 256, hK);
      if (g >= 2) lwr64f(ev + hw * 256, hV);
#pragma unroll 1
      for (int idx = 3 + a; idx < 12; idx += 3) wo_block(woa, wob, vw[0], vw[1], lds0, idx, rot2);
      lgkm0();
      STAMP(5);
      bar();
#pragma unroll 1
      for (int idx = 12 + a; idx < 36; idx += 3) wo_block(woa, wob, vw[0], vw[1], lds0, idx, rot2);

    } else {
      const u32x2 inb0 = __builtin_amdgcn_raw_buffer_load_b64(rsS, INB_OFF + (u32)(sg * 64 + mi) * 512u + lane * 8u, 0, AUX_SC1);
      const u32 non0 = __builtin_amdgcn_raw_buffer_load_b32(rsS, NON_OFF + (u32)(sg * 64 + mi) * 4u, 0, AUX_SC1);
      const u32 BM = rb + (p >= 2 ? WPART + 112 : 128), BV = rb + 2 * WPART + 128;
      f32x4 hM = z4, hV4 = z4;
      const int jl = 8 * hg + r8;
      const u16* xr = x + (size_t)(2 * (128 * chunk + jl) + par) * 576;
      STAMP(2);
      nonce = __builtin_amdgcn_readfirstlane(non0) + 1u;
      while (__ballot(inb0[0] == nonce) != 0ull) ++nonce;
      if (lane == 0) __builtin_amdgcn_raw_buffer_store_b32(nonce, rsS, NON_OFF + (u32)(sg * 64 + mi) * 4u, 0, AUX_SC1);
      wait_vm<1>();
      STAMP(3);
      int cs = 0;
#pragma unroll 1
      for (int it = 0; it < 9; ++it) {
#pragma unroll
        for (int k2 = 0; k2 < 2; ++k2) {
          bar();
          const u32 ko = cs + k2 * 32 * WROW;
          const u32x4 xh = lrd128((HB ^ (u32)(64 * k2)) + cs), fk = trd2(HW + ko, WROW), fv = trd2(HW + ko + WPART, WROW), fm = trd2(BM + ko, WROW), f4 = trd2(BV + ko, WROW);
          hK = mfma(fk, xh, hK); hV = mfma(xh, fv, hV); hM = mfma(fm, xh, hM); hV4 = mfma(xh, f4, hV4);
        }
        cs = nxtslot(cs);
      }
      if (c >= 8) { lwr64f(ek + 3 * 256, hK); if (g >= 2) { lwr64f(ek + 4 * 256, hM); lwr64f(ek + 4 * 256 - 128, hM); } }
      if (g >= 2) { lwr64f(ev + 3 * 256, hV); lwr64f(ev + 4 * 256, hV4); }
      lgkm0();
      STAMP(5);
      bar();
      {
        u32x4 xv[9], gv[9];
        vn = __builtin_amdgcn_raw_buffer_load_b32(rsS, NON_OFF + (u32)(sg * 64 + lane) * 4u, 0, AUX_SC1);
#pragma unroll
        for (int i = 0; i < 9; ++i) { xv[i] = *(const u32x4*)(xr + (i * 8 + sp8) * 8); gv[i] = *(const u32x4*)(gw + (i * 8 + sp8) * 8); }
        float a0 = 0.f, a1 = 0.f;
#pragma unroll
        for (int i = 0; i < 9; ++i) {
          a0 = dot2(xv[i][0], gv[i][0], a0); a1 = dot2(xv[i][1], gv[i][1], a1);
          a0 = dot2(xv[i][2], gv[i][2], a0); a1 = dot2(xv[i][3], gv[i][3], a1);
        }
        float ga = a0 + a1;
        ga += __shfl_xor(ga, 1); ga += __shfl_xor(ga, 2); ga += __shfl_xor(ga, 4);
        const float gt = __builtin_amdgcn_rcpf(1.f + __builtin_amdgcn_exp2f(-1.4426950408889634f * ga));
        if (sp8 == 0) __builtin_amdgcn_raw_buffer_store_b32(__float_as_uint(gt), rsS, GATE_OFF + (u32)(cp * 128 + jl) * 4u, 0, AUX_SC1);
      }
    }
    __builtin_amdgcn_s_setprio(0);
  } else {
    const int w = wid;
    u32 B = rb, BM = rb + (p >= 2 ? WPART + 112 : 128), XB[2];
    XB[0] = lds0 + (32 * w + c) * 128 + ((g ^ (c >> 1)) * 16); XB[1] = XB[0] ^ 64;
    f32x4 bias[2]; u32 mq[5], mP0, mP1;
    {
      const float NINF = -__builtin_inff();
      const bool first = (chunk == 0 && w == 0);
#pragma unroll
      for (int r = 0; r < 4; ++r) {
        const int i = 4 * g + r;
        const bool okc = (i <= c && i >= c - 7), okp = i >= c + 9;
        bias[0][r] = (okc || (okp && !first)) ? 0.f : NINF; bias[1][r] = (okc || okp) ? 0.f : NINF;
      }
      mq[0] = ~0u; mq[1] = g == 0 ? ~0u : 0u; mq[2] = ~mq[1]; mq[3] = g <= 1 ? ~0u : 0u; mq[4] = ~mq[3];
      mP0 = c < 8 ? mq[4] : 0u; mP1 = ~mP0;
      PIN(bias[0]); PIN(bias[1]); PIN(mq[1]); PIN(mq[2]); PIN(mq[3]); PIN(mq[4]); PIN(mP0); PIN(mP1);
    }
    u32 ek = lds0 + XCH + w * XCB + (g * 8 + (c & 7)) * 8, ev = lds0 + XCH + w * XCB + 1280 + (lane & 31) * 8;
    PIN(ek); PIN(ev);
    u32 so[2], st[2]; u32x4 ones = {0x3f803f80u, 0x3f803f80u, 0x3f803f80u, 0x3f803f80u};
    { const u32 ob = (u32)((cp * 8 + 2 * w) * 36 + 9 * (hg >> 2)) * 1024u;
#pragma unroll
      for (int t = 0; t < 2; ++t) { so[t] = lane * 16u + ob + t * 36864u + (hg & 3) * 2048u; st[t] = ((c * 16u + g * 8u) | (mq[4] & 0x80000000u)) + ob + t * 36864u + 8192u + (hg & 3) * 256u; PIN(so[t]); PIN(st[t]); }
      PIN(ones); }
    f32x4 acc[2][14];
#pragma unroll
    for (int t = 0; t < 2; ++t)
#pragma unroll
      for (int i = 0; i < 14; ++i) { acc[t][i] = z4; PIN(acc[t][i]); }
    PIN(B); PIN(BM); PIN(XB[0]); PIN(XB[1]);
    __builtin_amdgcn_s_waitcnt(0xC07F);
    u32x2 wlo[7], whi[7]; u32x4 xf[2][2];
    auto faddr = [&](auto NC) -> u32 {
      constexpr int n = decltype(NC)::value; constexpr int f = n % 14, ko = (n / 14) * 32 * WROW;
      if constexpr (f < 4) return B + ko + WPART + 32 * f;
      else if constexpr (f == 4) return BM + ko;
      else if constexpr (f < 10) return B + ko + 2 * WPART + 32 * (f - 5);
      else return B + ko + 32 * (f - 10);
    };
    u32x10 Kp[3]; u32 Qp[2][10]; u32x6 Vp[5];
    auto pack = [&](auto FC, auto TC) {
      constexpr int f = decltype(FC)::value, t = decltype(TC)::value;
      u32 a0 = pk(acc[t][f][0], acc[t][f][1]), a1 = pk(acc[t][f][2], acc[t][f][3]);
      asm volatile("" : "+v"(a0), "+v"(a1));
      if constexpr (f < 4) { Kp[t + 1][2 * f] = a0; Kp[t + 1][2 * f + 1] = a1; }
      else if constexpr (f == 4) {
        auto r0 = __builtin_amdgcn_permlane32_swap(a0, a0, false, false); auto r1 = __builtin_amdgcn_permlane32_swap(a1, a1, false, false);
        Qp[t][8] = r0[0]; Qp[t][9] = r1[0]; Kp[t + 1][8] = r0[1]; Kp[t + 1][9] = r1[1];
      }
      else if constexpr (f < 10) { Vp[f - 5][2 * t + 2] = a0; Vp[f - 5][2 * t + 3] = a1; }
      else { Qp[t][2 * (f - 10)] = a0; Qp[t][2 * (f - 10) + 1] = a1; }
    };
    wait_vm<0>();
    STAMP(2);
    bar();
    STAMP(3);
    xf[0][0] = lrd128(XB[0]); xf[0][1] = lrd128(XB[0] + 2048);
    sfor<0, PD>([&](auto NC) { constexpr int n = decltype(NC)::value; const u32 a = faddr(NC); wlo[n % 7] = trd(a); whi[n % 7] = trd(a + WROW); SBAR(); });
    auto stage = [&](auto LC, int d) {
      constexpr bool LAST = decltype(LC)::value;
      sfor<0, 28>([&](auto NC) {
        constexpr int n = decltype(NC)::value;
        constexpr int f = n % 14, k2 = n / 14, nn = (n + PD) % 28, kn = nn / 14;
        constexpr bool RD = !(LAST && n + PD >= 28), BI = LAST && k2 == 1;
        constexpr int fx = f - 2;
        if constexpr ((nn == 0 && !LAST) || nn == 14) { SBAR(); bar(); SBAR(); }
        const u32x4 wv = {wlo[n % 7][0], wlo[n % 7][1], whi[n % 7][0], whi[n % 7][1]};
        const u32 a = faddr(IC<nn>{});
        if constexpr (f >= 5 && f < 10) mfma_acc<BI>(acc[0][f], xf[k2][0], wv); else mfma_acc<BI>(acc[0][f], wv, xf[k2][0]);
        SBAR();
        if constexpr (RD) wlo[nn % 7] = trd(a);
        if constexpr (BI && fx >= 0) pack(IC<(fx >= 0 ? fx : 0)>{}, IC<1>{});
        SBAR();
        if constexpr (f >= 5 && f < 10) mfma_acc<BI>(acc[1][f], xf[k2][1], wv); else mfma_acc<BI>(acc[1][f], wv, xf[k2][1]);
        SBAR();
        if constexpr (RD) whi[nn % 7] = trd(a + WROW);
        if constexpr (BI && !RD) pack(IC<2 * (f - 8)>{}, IC<0>{});
        if constexpr (!LAST) {
          if constexpr (nn == 27) { B += d; PIN(B); }
          if constexpr (nn == 19) { BM += d; PIN(BM); }
          if constexpr (nn == 7) { XB[0] += d; PIN(XB[0]); }
          if constexpr (nn == 20) { XB[1] += d; PIN(XB[1]); }
        }
        if constexpr (RD) {
          if constexpr (nn % 14 == 1) xf[kn][0] = lrd128(XB[kn]);
          if constexpr (nn % 14 == 3) xf[kn][1] = lrd128(XB[kn] + 2048);
        }
        SBAR();
      });
    };
    int sl = 0;
#pragma unroll 1
    for (int it = 0; it < 8; ++it) { const int sn = nxtslot(sl), d = sn - sl; sl = sn; stage(IC<0>{}, d); }
    STAMP(4);
    stage(IC<1>{}, 0);
    static_assert(PD == 6, "pack schedule");
    if (w < 3) {
      if (c >= 8) {
#pragma unroll
        for (int i = 0; i < 5; ++i) lwr64(ek + XCB + i * 256, Kp[2][2 * i], Kp[2][2 * i + 1]);
      }
      if (g >= 2) {
#pragma unroll
        for (int i = 0; i < 5; ++i) lwr64(ev + XCB + i * 256, Vp[i][4], Vp[i][5]);
      }
    }
    SBAR();
    pack(IC<12>{}, IC<1>{}); pack(IC<13>{}, IC<1>{});
    sfor<0, 6>([&](auto J) { pack(IC<2 * decltype(J)::value + 1>{}, IC<0>{}); });
    pack(IC<12>{}, IC<0>{}); pack(IC<13>{}, IC<0>{});
    lgkm0();
    STAMP(5);
    bar();
    STAMP(6);
#pragma unroll
    for (int i = 0; i < 5; ++i) {
      const u32x2 a = lrd64(ek + i * 256), v = lrd64(ev + i * 256);
      Kp[0][2 * i] = a[0]; Kp[0][2 * i + 1] = a[1]; Vp[i][0] = v[0]; Vp[i][1] = v[1];
    }
    PIN(Kp[1]); PIN(Kp[2]);
    SBAR();
    const bool selp = (c < 8) && (g >= 2);
    f32x4 sp[2][4], sc[2][4]; float mx[2][4];
    sfor<0, 8>([&](auto IC_) {
      constexpr int t = 1 - (decltype(IC_)::value >> 2), h = decltype(IC_)::value & 3;
      if constexpr (t == 0 && h == 0) PIN(Kp[0]);
      constexpr int ia0 = h == 0 ? 0 : (h == 3 ? 4 : 2), ia1 = h <= 1 ? 0 : 2, ib0 = h <= 1 ? 1 : 3, ib1 = h == 0 ? -1 : (h == 3 ? 3 : 1);
      u32x4 qo;
      qo[0] = Qp[t][2 * h] & mq[ia0]; qo[1] = Qp[t][2 * h + 1] & mq[ia1]; qo[2] = Qp[t][2 * h + 2] & mq[ib0];
      if constexpr (ib1 >= 0) qo[3] = Qp[t][2 * h + 3] & mq[ib1 >= 0 ? ib1 : 0]; else qo[3] = 0u;
      const u32x4 k0 = __builtin_shufflevector(Kp[t], Kp[t], 2 * h, 2 * h + 1, 2 * h + 2, 2 * h + 3);
      const u32x4 k1 = __builtin_shufflevector(Kp[t + 1], Kp[t + 1], 2 * h, 2 * h + 1, 2 * h + 2, 2 * h + 3);
      sp[t][h] = mfma(k0, qo, bias[t]); sc[t][h] = mfma(k1, qo, bias[t]);
    });
    __builtin_amdgcn_sched_group_barrier(0x2, 4, 0);
#pragma unroll
    for (int i = 0; i < 16; ++i) { __builtin_amdgcn_sched_group_barrier(0x8, 1, 0); __builtin_amdgcn_sched_group_barrier(0x2, 2, 0); }
    SBAR();
    if (selp) {
#pragma unroll
      for (int t = 1; t >= 0; --t)
#pragma unroll
        for (int h = 0; h < 4; ++h) { sc[t][h] = sp[t][h]; PIN(sc[t][h]); }
    }
    SBAR();
#pragma unroll
    for (int t = 0; t < 2; ++t)
#pragma unroll
      for (int h = 0; h < 4; ++h) mx[t][h] = fmx(fmx(fmx(sc[t][h][0], sc[t][h][1]), sc[t][h][2]), sc[t][h][3]);
#pragma unroll
    for (int t = 0; t < 2; ++t) {
      float* m = mx[t];
      sw32(m[0], m[1]); float m01 = fmx(m[0], m[1]); sw32(m[2], m[3]); float m23 = fmx(m[2], m[3]);
      sw16(m01, m23); float R = fmx(m01, m23) * -CEXP;
      float a = R, bb = R; sw16(a, bb); m[0] = a; m[1] = a; sw32(m[0], m[1]); m[2] = bb; m[3] = bb; sw32(m[2], m[3]);
    }
#pragma unroll
    for (int i = 0; i < 5; ++i) PIN(Vp[i]);
    SBAR();
    STAMP(18);
    f32x4 olo[2][4], ohi[2][4], sm[2][4];
    sfor<0, 8>([&](auto IC_) {
      constexpr int t = decltype(IC_)::value >> 2, h = decltype(IC_)::value & 3;
      const f32x2 cc = {CEXP, CEXP}, mm = {mx[t][h], mx[t][h]};
      const f32x2 e0 = __builtin_elementwise_fma((f32x2){sc[t][h][0], sc[t][h][1]}, cc, mm), e1 = __builtin_elementwise_fma((f32x2){sc[t][h][2], sc[t][h][3]}, cc, mm);
      const u32 p0 = pk(__builtin_amdgcn_exp2f(e0[0]), __builtin_amdgcn_exp2f(e0[1])), p1 = pk(__builtin_amdgcn_exp2f(e1[0]), __builtin_amdgcn_exp2f(e1[1]));
      const u32x4 po = {p0 & mP0, p1 & mP0, p0 & mP1, p1 & mP1};
      sm[t][h] = mfma(ones, po, z4);
      olo[t][h] = mfma(__builtin_shufflevector(Vp[h], Vp[h], 2 * t, 2 * t + 1, 2 * t + 2, 2 * t + 3), po, z4);
      ohi[t][h] = mfma(__builtin_shufflevector(Vp[h + 1], Vp[h + 1], 2 * t, 2 * t + 1, 2 * t + 2, 2 * t + 3), po, z4);
    });
    SBAR();
    f32x2 ov[2][10]; float inv[2][4];
#pragma unroll
    for (int t = 0; t < 2; ++t) {
#pragma unroll
      for (int h = 0; h < 4; ++h) inv[t][h] = __builtin_amdgcn_rcpf(sm[t][h][0]);
#pragma unroll
      for (int h = 0; h < 4; ++h) { ov[t][2 * h] = (f32x2){olo[t][h][0], olo[t][h][1]} * inv[t][h]; ov[t][2 * h + 1] = (f32x2){olo[t][h][2], olo[t][h][3]} * inv[t][h]; }
      ov[t][8] = (f32x2){ohi[t][3][0], ohi[t][3][1]} * inv[t][3]; ov[t][9] = (f32x2){ohi[t][3][2], ohi[t][3][3]} * inv[t][3];
    }
    if (g < 2) {
#pragma unroll
      for (int t = 0; t < 2; ++t) { ov[t][6] = (f32x2){ohi[t][2][0], ohi[t][2][1]} * inv[t][2]; PIN(ov[t][6]); }
      if (g == 0) {
#pragma unroll
        for (int t = 0; t < 2; ++t) {
          ov[t][2] = (f32x2){ohi[t][0][0], ohi[t][0][1]} * inv[t][0]; PIN(ov[t][2]);
          ov[t][4] = (f32x2){ohi[t][1][0], ohi[t][1][1]} * inv[t][1]; PIN(ov[t][4]);
          ov[t][5] = (f32x2){ohi[t][1][2], ohi[t][1][3]} * inv[t][1]; PIN(ov[t][5]);
          ov[t][7] = (f32x2){ohi[t][2][2], ohi[t][2][3]} * inv[t][2]; PIN(ov[t][7]);
        }
      }
    }
    STAMP(17);
#pragma unroll
    for (int t = 0; t < 2; ++t) {
      u32 op[10];
#pragma unroll
      for (int i = 0; i < 10; ++i) op[i] = pk(ov[t][i][0], ov[t][i][1]);
      __builtin_amdgcn_raw_buffer_store_b128((u32x4){op[0], op[1], op[2], op[3]}, rsS, so[t], 0, AUX_SC1);
      __builtin_amdgcn_raw_buffer_store_b128((u32x4){op[4], op[5], op[6], op[7]}, rsS, so[t] + 1024u, 0, AUX_SC1);
      __builtin_amdgcn_raw_buffer_store_b64((u32x2){op[8], op[9]}, rsS, st[t], 0, AUX_SC1);
    }
  }
  const int kq = wid & 3, th = wid >> 2;
  u32 WB[4];
  { const int pos = 4 * g + q, s = (pos >> 1) & 3;
#pragma unroll
    for (int i = 0; i < 4; ++i) { WB[i] = lds0 + (kq + (th ? 20 : 0)) * 4096 + pos * 128 + (((i ^ (2 * (kq & 1))) ^ s) * 32) + p * 8; PIN(WB[i]); } }
  u32 myin = INB_OFF + (u32)(sg * 64 + mi) * 512u + lane * 8u, dst = INB_OFF + (u32)(sg * 64 + lane) * 512u + mi * 8u;
  PIN(myin); PIN(dst);
  const int T0 = 32 * sg + 4 * rg + 2 * th + (kq >> 1), TX = 32 * sg + 28 + rg;
  u32 l16 = lane * 16u; PIN(l16);
  u32 so_k, so_b[3];
  { int r = rot2 + (th ? 5 : 0); r = r >= 9 ? r - 9 : r; so_k = r * 1024u;
    so_b[0] = rg < 4 ? (u32)(TX * 36 + 9 * kq) * 1024u : 0xf0000000u;
    so_b[1] = (u32)((T0 ^ 1) * 36 + 9 * kq) * 1024u; so_b[2] = (u32)(T0 * 36 + 9 * kq) * 1024u;
    asm volatile("" : "+s"(so_k), "+s"(so_b[0]), "+s"(so_b[1]), "+s"(so_b[2])); }
  u32 RW[3];
#pragma unroll
  for (int j = 1; j < 4; ++j) { RW[j - 1] = lds0 + 32768 + ((th * 4 + (kq ^ j)) * 6 + (j - 1) * 2) * 1024 + lane * 16; PIN(RW[j - 1]); }
  STAMP(7);
  wait_vm<0>();
  STAMP(8);
  bar();
  STAMP(9);
  u32x4 wf[9][4];
  if (wid == 7) __builtin_amdgcn_raw_buffer_store_b64((u32x2){vn, nonce}, rsS, dst, 0, AUX_SC1);
  sfor<0, 9>([&](auto KC) { constexpr int k = decltype(KC)::value;
    if constexpr (k == 4) {
      const u32 d = th ? (u32)(4 - 9) * 16384u : 4u * 16384u;
#pragma unroll
      for (int i = 0; i < 4; ++i) { WB[i] += d; PIN(WB[i]); }
    }
    if constexpr (k == 8) {
#pragma unroll
      for (int i = 0; i < 4; ++i) { WB[i] += 4u * 16384u; PIN(WB[i]); }
    }
#pragma unroll
    for (int i = 0; i < 4; ++i) wf[k][i] = trd2(WB[i] + (k & 3) * 16384, 2048); });
  SBAR();
  bool allok = true;
  if (wid == 7) {
    const int cl = (lane >> 2) & 3;
    const u64 need = __ballot(cl == (rg >> 1) || (rg < 4 && cl == 3));
    u64 got = 0;
#pragma unroll 1
    for (int spin = 0; spin < (1 << 17); ++spin) {
      const u32x2 v = __builtin_amdgcn_raw_buffer_load_b64(rsS, myin, 0, AUX_SC1);
      const bool ok = v[0] == nonce;
      if (ok && v[1] != vn) { vn = v[1]; __builtin_amdgcn_raw_buffer_store_b64((u32x2){vn, nonce}, rsS, dst, 0, AUX_SC1); }
      got = __ballot(ok);
      if ((got & need) == need) break;
      __builtin_amdgcn_s_sleep(1);
    }
    allok = got == ~0ull;
  }
  lgkm0();
  STAMP(10);
  bar();
  STAMP(11);
  if (rg < 7) {
    constexpr int OW = 6, NL = 23;
    u32x4 of[OW + 1];
    const u32 so_k0 = so_k;
    auto ldo = [&](auto NC) { constexpr int n = decltype(NC)::value; constexpr int ph = n < 5 ? 0 : (n < 14 ? 1 : 2);
      if constexpr (n == 5) so_k = so_k0;
      of[n % (OW + 1)] = __builtin_amdgcn_raw_buffer_load_b128(rsS, l16, (n == 4 && th) ? 0xf0000000u : so_b[ph] + so_k, AUX_SC1);
      so_k = (so_k == 8192u) ? 0u : so_k + 1024u; };
    sfor<0, OW>([&](auto NC) { ldo(NC); SBAR(); });
    f32x4 ac[3][4];
    float gm = 0.f, gx = 0.f;
    const int L = 32 * (wid & 1) + (lane >> 1);
    sfor<0, NL>([&](auto NC) {
      constexpr int n = decltype(NC)::value; constexpr int ph = n < 5 ? 0 : (n < 14 ? 1 : 2), k = n < 5 ? n : (n < 14 ? n - 5 : n - 14);
      if constexpr (n + OW < NL) ldo(IC<n + OW>{});
      if constexpr (n + OW == NL) {
        gm = __uint_as_float(__builtin_amdgcn_raw_buffer_load_b32(rsS, c * 4u, GATE_OFF + (u32)T0 * 64u, AUX_SC1));
        gx = __uint_as_float(__builtin_amdgcn_raw_buffer_load_b32(rsS, (L & 15) * 4u, GATE_OFF + (u32)(TX & 127) * 64u, AUX_SC1));
      }
      SBAR();
#pragma unroll
      for (int ii = 0; ii < 4; ++ii) { const int i = ii ^ 2; ac[ph][i] = mfma(wf[k][i], of[n % (OW + 1)], k == 0 ? z4 : ac[ph][i]); }
      SBAR();
      if constexpr (n >= 6 && n < 10) lwr128(lds0 + wid * 4096 + (((n - 6) ^ (2 * (kq & 1))) * 1024) + l16, ac[0][n - 6]);
      if constexpr (n >= 15 && n < 19) lwr128(RW[1 + ((n - 15) >> 1)] + ((n - 15) & 1) * 1024, ac[1][n - 15]);
      SBAR();
    });
    STAMP(12);
    lwr128(RW[0], ac[2][2]); lwr128(RW[0] + 1024, ac[2][3]);
    const float fm = (kq >> 1) ? 1.f - gm : gm;
    const int row = 2 * (128 * (T0 >> 4) + 16 * (T0 & 7) + c) + ((T0 >> 3) & 1);
    u16* po = out + (size_t)row * 576 + ct * 64 + (kq & 1) * 32 + (g & 1) * 16 + (g >> 1) * 8;
    const int rowx = 2 * (128 * (TX >> 4) + 16 * (TX & 7) + (L & 15)) + ((TX >> 3) & 1);
    u16* px = out + (size_t)rowx * 576 + ct * 64 + (wid >> 1) * 16 + (L >> 4) * 4 + (lane & 1) * 2;
    PIN(po); PIN(px);
    lgkm0();
    bar();
    STAMP(13);
    {
      const u32 ra = lds0 + 32768 + wid * 6144 + l16;
      f32x4 v[2];
#pragma unroll
      for (int t = 0; t < 2; ++t) {
        const f32x4 r1 = *(LAS f32x4*)(uintptr_t)(ra + t * 1024), r2 = *(LAS f32x4*)(uintptr_t)(ra + 2048 + t * 1024), r3 = *(LAS f32x4*)(uintptr_t)(ra + 4096 + t * 1024);
        const f32x4 S = ac[2][t] + r1, T = r2 + r3;
        v[t] = (S - T) * fm + T;
      }
      auto s0_ = __builtin_amdgcn_permlane16_swap(pk(v[0][0], v[0][1]), pk(v[1][0], v[1][1]), false, false);
      auto s1_ = __builtin_amdgcn_permlane16_swap(pk(v[0][2], v[0][3]), pk(v[1][2], v[1][3]), false, false);
      *(u32x4*)po = (u32x4){s0_[0], s1_[0], s0_[1], s1_[1]};
    }
    if (rg < 4) {
      f32x2 e[8];
#pragma unroll
      for (int j = 0; j < 8; ++j) e[j] = *(LAS f32x2*)(uintptr_t)(lds0 + j * 4096 + wid * 512 + lane * 8);
      const f32x2 A = (e[0] + e[1]) + (e[4] + e[5]), Bv = (e[2] + e[3]) + (e[6] + e[7]);
      const f32x2 r = (A - Bv) * gx + Bv;
      *(u32*)px = pk(r[0], r[1]);
    }
  }
  if (!allok) {
#pragma unroll 1
    for (int spin = 0; spin < (1 << 17); ++spin) {
      const u32x2 v = __builtin_amdgcn_raw_buffer_load_b64(rsS, myin, 0, AUX_SC1);
      const bool ok = v[0] == nonce;
      if (ok && v[1] != vn) { vn = v[1]; __builtin_amdgcn_raw_buffer_store_b64((u32x2){vn, nonce}, rsS, dst, 0, AUX_SC1); }
      if (__ballot(ok) == ~0ull) break;
      __builtin_amdgcn_s_sleep(1);
    }
  }
  STAMP(14);
  STAMP(15); RSTAMP(16);
  STAMP_OUT();
}

extern "C" void kernel_launch(void* const* d_in, const int* in_sizes, int n_in, void* d_out, int out_size, void* d_ws, size_t ws_size, hipStream_t stream) {
  const u16* x = (const u16*)d_in[0]; const u16* wqa = (const u16*)d_in[1]; const u16* woa = (const u16*)d_in[2];
  const u16* wqb = (const u16*)d_in[4]; const u16* wob = (const u16*)d_in[5]; const u16* gw = (const u16*)d_in[6];
  (void)hipFuncSetAttribute((const void*)fused, hipFuncAttributeMaxDynamicSharedMemorySize, LDS_BYTES);
  fused<<<256, 512, LDS_BYTES, stream>>>(x, wqa, woa, wqb, wob, gw, (u16*)d_out, (char*)d_ws);
}
```

```cpp
#include <hip/hip_runtime.h>
typedef unsigned short u16; typedef unsigned int u32; typedef unsigned long long u64;
typedef __attribute__((ext_vector_type(8))) __bf16 bf16x8;
typedef __attribute__((ext_vector_type(2))) __bf16 bf16x2;
typedef __attribute__((ext_vector_type(4))) float f32x4;
typedef __attribute__((ext_vector_type(2))) float f32x2;
typedef __attribute__((ext_vector_type(4))) short s16x4;
typedef __attribute__((ext_vector_type(4))) u32 u32x4;
typedef __attribute__((ext_vector_type(2))) u32 u32x2;
typedef __attribute__((ext_vector_type(6))) u32 u32x6;
typedef __attribute__((ext_vector_type(10))) u32 u32x10;
#define LAS __attribute__((address_space(3)))
#define DI __device__ __forceinline__
#define SBAR() __builtin_amdgcn_sched_barrier(0)
#define PIN(x) asm volatile("" : "+v"(x))
template <int V> struct IC { static constexpr int value = V; };
template <int A, int B, class F> __device__ __forceinline__ void sfor(F&& f) { if constexpr (A < B) { f(IC<A>{}); sfor<A + 1, B>(f); } }
#define ROT 7
#define ROT2 1
#define PD 6
#define PRE 4
#define DIAG 0
#define MID 19
#define XALL 0
#define WOA 1
#define WOB 3
#define FLIP 0
#define DEPTH 2

constexpr int WROW = 144, WPART = 9232, XSTG = 136 * 128, STG = 45184;
constexpr int LDS_BYTES = 163840;
constexpr int XCH = 147456, XCB = 2560, GWL = XCH + 4 * XCB, CNT = GWL + 2048;
constexpr u32 OP_BYTES = 128u * 36u * 1024u;
constexpr u32 GATE_OFF = OP_BYTES;
constexpr u32 NON_OFF = GATE_OFF + 8192;
constexpr u32 INB_OFF = NON_OFF + 1024;
constexpr u32 WS_BYTES = INB_OFF + 256 * 512;
constexpr float CEXP = 0.23570226039551584f * 1.4426950408889634f;
constexpr int AUX_SC1 = 16;
constexpr u32 CODE_BYTES = 16768;

DI u32 pk(float a, float b) { f32x2 v = {a, b}; bf16x2 r = __builtin_convertvector(v, bf16x2); return __builtin_bit_cast(u32, r); }
DI f32x4 mfma(u32x4 a, u32x4 b, f32x4 c) { return __builtin_amdgcn_mfma_f32_16x16x32_bf16(__builtin_bit_cast(bf16x8, a), __builtin_bit_cast(bf16x8, b), c, 0, 0, 0); }
template <bool BI> DI void mfma_acc(f32x4& c, u32x4 a, u32x4 b) {
  if constexpr (BI) c = mfma(a, b, c);
  else asm volatile("v_mfma_f32_16x16x32_bf16 %0, %1, %2, %0" : "+v"(c) : "v"(a), "v"(b));
}
DI u32x2 trd(u32 a) { return __builtin_bit_cast(u32x2, __builtin_amdgcn_ds_read_tr16_b64_v4i16((LAS s16x4*)(uintptr_t)a)); }
DI u32x4 trd2(u32 a, int st) { const u32x2 x = trd(a), y = trd(a + st); return (u32x4){x[0], x[1], y[0], y[1]}; }
DI u32x4 lrd128(u32 a) { return *(LAS u32x4*)(uintptr_t)a; }
DI u32x2 lrd64(u32 a) { return *(LAS u32x2*)(uintptr_t)a; }
DI void lwr64(u32 a, u32 x, u32 y) { *(LAS u32x2*)(uintptr_t)a = (u32x2){x, y}; }
DI void lwr64f(u32 a, f32x4 v) { lwr64(a, pk(v[0], v[1]), pk(v[2], v[3])); }
DI void lwr128(u32 a, f32x4 v) { *(LAS f32x4*)(uintptr_t)a = v; }
#define NT 0
#define NT0 ""
#define NT1 ""
DI void dma16(__amdgpu_buffer_rsrc_t rs, u32 voff, u32 soff, u32 ldsdst) {
  asm volatile("s_mov_b32 m0, %0\n\ts_nop 0\n\tbuffer_load_dwordx4 %1, %2, %3 offen" NT0 " lds" :: "s"(ldsdst), "v"(voff), "s"(rs), "s"(soff) : "memory");
}
#define WIN 12
#define WINO 6
template <int WN = WIN> DI void dma16w(__amdgpu_buffer_rsrc_t rs, u32 voff, u32 soff, u32 ldsdst) {
  asm volatile("s_mov_b32 m0, %0\n\ts_waitcnt vmcnt(%4)\n\tbuffer_load_dwordx4 %1, %2, %3 offen" NT1 " lds" :: "s"(ldsdst), "v"(voff), "s"(rs), "s"(soff), "i"(WN) : "memory");
}
template <int N> DI void wait_vm() { asm volatile("s_waitcnt vmcnt(%0)" :: "i"(N) : "memory"); }
DI void bar() { asm volatile("s_barrier" ::: "memory"); }
DI void lgkm0() { asm volatile("s_waitcnt lgkmcnt(0)" ::: "memory"); }
#define STAMP(i)
#define RSTAMP(i)
#define STAMP_DECL
#define STAMP_OUT()
DI float dot2(u32 a, u32 b, float c) { return __builtin_amdgcn_fdot2_f32_bf16(__builtin_bit_cast(bf16x2, a), __builtin_bit_cast(bf16x2, b), c, false); }
DI float fmx(float a, float b) { return __builtin_elementwise_maximum(a, b); }
DI void sw32(float& a, float& b) { auto r = __builtin_amdgcn_permlane32_swap(__float_as_uint(a), __float_as_uint(b), false, false); a = __uint_as_float(r[0]); b = __uint_as_float(r[1]); }
DI void sw16(float& a, float& b) { auto r = __builtin_amdgcn_permlane16_swap(__float_as_uint(a), __float_as_uint(b), false, false); a = __uint_as_float(r[0]); b = __uint_as_float(r[1]); }
DI int nxt9(int s) { return s == 8 ? 0 : s + 1; }
DI int nxtslot(int s) { return s == 2 * STG ? 0 : s + STG; }
DI __amdgpu_buffer_rsrc_t mkrs(const void* p, u32 n) { return __builtin_amdgcn_make_buffer_rsrc((void*)p, 0, n, 0x00020000); }
DI u32 wvoff(u32 ci) {
  const u32 pos = __umul24(ci, 7282u) >> 16, ch = ci - __umul24(pos, 9u);
  const u32 row = (pos & ~7u) | ((pos & 1u) << 2) | ((pos & 7u) >> 1);
  return __umul24(row, 3456u) + ch * 16u;
}

struct WoState { u32 vr[4], vt[4]; u32 dst; int kk; u32 base; };
DI void wo_blocks(__amdgpu_buffer_rsrc_t rs, WoState w, int v0, int n) {
  w.dst += v0 * 16384; w.kk += v0; w.kk = w.kk >= 9 ? w.kk - 9 : w.kk;
#pragma unroll 1
  for (int i = 0; i < n; ++i) {
    if (w.kk < 8) {
      const u32 so = w.base + (u32)(36 * w.kk - 4 * (w.kk & 1)) * 1152u;
      asm volatile("s_mov_b32 m0, %0\n\ts_waitcnt vmcnt(%7)\n\tbuffer_load_dwordx4 %1, %5, %6 offen lds\n\tbuffer_load_dwordx4 %2, %5, %6 offen offset:1024 lds\n\t"
                   "buffer_load_dwordx4 %3, %5, %6 offen offset:2048 lds\n\tbuffer_load_dwordx4 %4, %5, %6 offen offset:3072 lds"
                   :: "s"(w.dst), "v"(w.vr[0]), "v"(w.vr[1]), "v"(w.vr[2]), "v"(w.vr[3]), "s"(rs), "s"(so), "i"(WINO) : "memory");
      ++w.kk;
    } else {
      asm volatile("s_mov_b32 m0, %0\n\ts_waitcnt vmcnt(%7)\n\tbuffer_load_dwordx4 %1, %5, %6 offen lds\n\tbuffer_load_dwordx4 %2, %5, %6 offen offset:1024 lds\n\t"
                   "buffer_load_dwordx4 %3, %5, %6 offen offset:2048 lds\n\tbuffer_load_dwordx4 %4, %5, %6 offen offset:3072 lds"
                   :: "s"(w.dst), "v"(w.vt[0]), "v"(w.vt[1]), "v"(w.vt[2]), "v"(w.vt[3]), "s"(rs), "s"(w.base), "i"(WINO) : "memory");
      w.kk = 0;
    }
    w.dst += 16384;
  }
}

#define ORD 0
#define OWN 6
#define GL 8
constexpr int ord_ph(int n) { return n >= 14 ? 2 : (ORD == 0 ? (n < 5 ? 0 : 1) : ORD == 1 ? (n < 10 ? 1 - (n & 1) : 1) : (n < 9 ? 1 : 0)); }
constexpr int ord_k(int n) { return n >= 14 ? n - 14 : (ORD == 0 ? (n < 5 ? n : n - 5) : ORD == 1 ? (n < 10 ? n >> 1 : n - 5) : (n < 9 ? n : n - 9)); }
__global__ __launch_bounds__(512, 2) void fused(const u16* __restrict__ x, const u16* __restrict__ wqa, const u16* __restrict__ woa,
                                                const u16* __restrict__ wqb, const u16* __restrict__ wob, const u16* __restrict__ gw,
                                                u16* __restrict__ out, char* __restrict__ ws) {
  extern __shared__ __attribute__((aligned(16))) char lds[];
  const int tid = threadIdx.x, lane = tid & 63;
  const int wid = __builtin_amdgcn_readfirstlane(tid >> 6);
  STAMP_DECL; RSTAMP(0); STAMP(1);
  const int b = blockIdx.x, xcd = b & 7, bi = b >> 3;
  const int cp = 8 * (xcd & 1) + (bi & 7), hg = 4 * (xcd >> 1) + (bi >> 3);
  const int par = cp & 1, chunk = cp >> 1;
  const int hq = hg & 7;
  int s0 = 0;
  if (ROT == 1) { s0 = (bi & 7) + 2 * (bi >> 3); s0 = s0 >= 9 ? s0 - 9 : s0; }
  if (ROT == 2) s0 = 2 * (((bi & 7) + (bi >> 3)) & 3);
  if (ROT == 7) s0 = 2 * (((bi & 7) + (bi >> 3)) & 3) + (((xcd >> 1) + xcd) & 1);
  const int r8 = lane >> 3, sp8 = lane & 7;
  const u32 lds0 = (u32)(uintptr_t)(LAS char*)lds;
  {
    unsigned long long pc; asm volatile("s_getpc_b64 %0" : "=s"(pc));
    if (wid == 7 && lane < 5) {
      const unsigned long long pa = (pc & ~255ull) + min((unsigned)(bi * 5 + lane) * 128u, CODE_BYTES - 128u);
      asm volatile("s_mov_b32 m0, %0\n\ts_nop 0\n\tglobal_load_lds_dword %1, off" :: "s"(lds0 + 3 * STG + 1024), "v"(pa) : "memory");
    }
  }
  u32 voE = (u32)(2 * (128 * chunk + r8) + par) * 1152u + (u32)(sp8 ^ (r8 >> 1)) * 16u;
  int jh = 128 * chunk - 8 + r8; jh = jh < 0 ? 0 : jh;
  u32 voH = (u32)(2 * jh + par) * 1152u + (u32)(sp8 ^ (r8 & 6)) * 16u;
  const int u0 = 2 * wid, p0 = (u0 * 52) >> 8, i0 = u0 - 5 * p0, u1 = u0 + 1, p1 = (u1 * 52) >> 8, i1 = u1 - 5 * p1;
  u32 voO = voE ^ 64u, vw0 = wvoff(64 * i0 + lane), vw1 = wvoff(64 * i1 + lane);
  u32 sx = s0 * 128 + wid * 36864, dx = lds0 + wid * 2048;
  u32 sw0 = s0 * (64 * 3456) + p0 * 1152 + hq * 144, dw0 = lds0 + XSTG + p0 * WPART + i0 * 1024;
  u32 sw1 = s0 * (64 * 3456) + p1 * 1152 + hq * 144, dw1 = lds0 + XSTG + p1 * WPART + i1 * 1024;
  asm volatile("" : "+v"(voE), "+v"(voH), "+v"(voO), "+v"(vw0), "+v"(vw1), "+s"(sx), "+s"(dx), "+s"(sw0), "+s"(dw0), "+s"(sw1), "+s"(dw1));
  SBAR();
  const __amdgpu_buffer_rsrc_t rsX = mkrs(x, 2048 * 1152);
  const __amdgpu_buffer_rsrc_t rsW = mkrs((hg >> 3) ? wqb : wqa, 576 * 3456);
  {
    dma16(rsX, voE, sx, dx);
    dma16(rsX, voO, sx + 18432, dx + 1024);
    dma16(rsW, vw0, sw0, dw0);
    if (wid < 7) dma16(rsW, vw1, sw1, dw1);
    else {
      dma16(rsX, voH, s0 * 128, lds0 + 16384);
      const __amdgpu_buffer_rsrc_t rsG = mkrs(gw, 1152);
      dma16(rsG, lane * 16u, 0, lds0 + GWL); dma16(rsG, lane * 16u, 1024, lds0 + GWL + 1024);
    }
  }
  SBAR();
  STAMP(25);
  asm volatile("" :: "s"(woa), "s"(wob), "s"(gw), "s"(out), "s"(ws));
  const int sg = cp >> 2, mi = 16 * (hg >> 2) + 4 * (cp & 3) + (hg & 3);
  const int rg = (mi * 57) >> 9, ct = mi - 9 * rg;
  int rot2 = ROT2 ? ct + 2 * (rg & 1) + 4 * (sg & 1) : 0; rot2 = rot2 >= 9 ? rot2 - 9 : rot2;
  const int c = lane & 15, g = lane >> 4, q = (lane >> 2) & 3, p = lane & 3;
  const __amdgpu_buffer_rsrc_t rsS = mkrs(ws, WS_BYTES);
  const f32x4 z4 = {0.f, 0.f, 0.f, 0.f};
  u32 nonce = 0, vn = 0;
  const u32 rb = lds0 + XSTG + (8 * g + 2 * q) * WROW + 8 * p;

  if (wid >= 4) {
    __builtin_amdgcn_s_setprio(2);
    const int hw = wid - 4;
    u32 HB = lds0 + (128 + (c & 7)) * 128 + ((g ^ (c & 6)) * 16), HW = rb + WPART + 32 * hw;
    f32x4 hK = z4, hV = z4;
    const u32 ek = lds0 + XCH + (g * 8 + (c & 7)) * 8, ev = lds0 + XCH + 1280 + (lane & 31) * 8;
    WoState wo;
    const __amdgpu_buffer_rsrc_t rsO = mkrs((hw & 2) ? wob : woa, 576 * 1152);
    { const u32 woc = (u32)(ct * 128 + ((sp8 ^ (2 * ((r8 >> 1) & 3))) * 16));
      const u32 voR = (u32)r8 * 1152u + woc, voT = (u32)(64 + 72 * (r8 >> 2) + (r8 & 3)) * 1152u + woc;
#pragma unroll
      for (int m = 0; m < 4; ++m) { wo.vr[m] = voR + m * (9216 - 1024); wo.vt[m] = voT + (144 * (m & 1) + 4 * (m >> 1)) * 1152 - m * 1024; PIN(wo.vr[m]); PIN(wo.vt[m]); }
      wo.dst = lds0 + hw * 4096; wo.kk = rot2; wo.base = (hw & 1) ? 288 * 1152 : 0; }
    if (wid < 7) {
      const int a = hw;
      u32 vA = voE ^ (u32)((a & 1) * 64), vB = vA ^ 64u;
      u32 v5 = a == 0 ? vB + 15u * 18432u : (a == 1 ? voH : 0x80000000u);
      const u32 xso = a * 18432, xd = lds0 + a * 1024, xd5 = lds0 + (a == 0 ? 15 : 16) * 1024;
      u32 vw[9];
      const u32 wso = a * 1152 + hq * 144, wd = lds0 + XSTG + a * WPART;
#pragma unroll
      for (int i = 0; i < 9; ++i) { vw[i] = wvoff(64 * i + lane); PIN(vw[i]); }
      PIN(vA); PIN(vB); PIN(v5); PIN(HB); PIN(HW);
      u32x4 xh = {0u, 0u, 0u, 0u}, fk = xh, fv = xh;
      int st = s0, cs = 0;
      auto xlo = [&](int sn, int sl) { dma16w(rsX, vA, sn * 128 + xso, xd + sl); dma16w(rsX, vB, sn * 128 + xso + 3 * 18432, xd + sl + 3072); dma16w(rsX, vA, sn * 128 + xso + 6 * 18432, xd + sl + 6144); };
      auto wl = [&](int i, int sn, int sl) { dma16w(rsW, vw[i], sn * (64 * 3456) + wso, wd + sl + i * 1024); };
      auto hm = [&]() { SBAR(); hK = mfma(fk, xh, hK); hV = mfma(xh, fv, hV); SBAR(); };
      auto rd0 = [&]() { xh = lrd128(HB + cs); fk = trd2(HW + cs, WROW); fv = trd2(HW + cs + WPART, WROW); SBAR(); };
      auto rd1 = [&]() { xh = lrd128((HB ^ 64u) + cs); fk = trd2(HW + cs + 32 * WROW, WROW); fv = trd2(HW + cs + WPART + 32 * WROW, WROW); SBAR(); };
      xlo(nxt9(st), STG);
      STAMP(2);
#pragma unroll 1
      for (int it = 0; it < 8; ++it) {
        const int sn = nxt9(st), sl = nxtslot(cs);
        wl(5, st, cs); wl(6, st, cs); hm(); wl(7, st, cs); wl(8, st, cs);
        wl(0, sn, sl); wl(1, sn, sl); wl(2, sn, sl); wl(3, sn, sl);
        wait_vm<11>();
        if (it == 0) STAMP(3);
        bar();
        if (it < 6) STAMP(19 + it);
        wl(4, sn, sl);
        rd0();
        dma16w(rsX, vB, sn * 128 + xso + 9 * 18432, xd + sl + 9216); dma16w(rsX, vA, sn * 128 + xso + 12 * 18432, xd + sl + 12288);
        hm();
        dma16w(rsX, v5, sn * 128, a == 2 ? lds0 + 3 * STG : xd5 + sl);
        if (it < 7) { xlo(nxt9(sn), nxtslot(sl)); wait_vm<11>(); } else wait_vm<8>();
        bar();
        rd1();
        cs = sl; st = sn;
      }
      wl(5, st, cs); wl(6, st, cs); hm(); wl(7, st, cs); wl(8, st, cs);
      wait_vm<4>();
      bar();
      rd0();
      wo_blocks(rsO, wo, 0, WOA);
      hm();
      wait_vm<4 * WOA>();
      bar();
      rd1();
      hm();
      if (c >= 8) lwr64f(ek + hw * 256, hK);
      if (g >= 2) lwr64f(ev + hw * 256, hV);
    } else {
      const u32x2 inb0 = __builtin_amdgcn_raw_buffer_load_b64(rsS, INB_OFF + (u32)(sg * 64 + mi) * 512u + lane * 8u, 0, AUX_SC1);
      const u32 non0 = __builtin_amdgcn_raw_buffer_load_b32(rsS, NON_OFF + (u32)(sg * 64 + mi) * 4u, 0, AUX_SC1);
      const u32 BM = rb + (p >= 2 ? WPART + 112 : 128), BV = rb + 2 * WPART + 128;
      const u32 XG = lds0 + (16 * (hg >> 1) + c) * 128 + ((g ^ (c >> 1)) * 16), GG = lds0 + GWL + g * 16;
      f32x4 hM = z4, hV4 = z4, hG = z4;
      STAMP(2);
      wait_vm<2>();
      STAMP(3);
      int cs = 0, st = s0;
#pragma unroll 1
      for (int it = 0; it < 9; ++it) {
#pragma unroll
        for (int k2 = 0; k2 < 2; ++k2) {
          bar();
          const u32 ko = cs + k2 * 32 * WROW;
          const u32x4 xh = lrd128((HB ^ (u32)(64 * k2)) + cs), fk = trd2(HW + ko, WROW), fv = trd2(HW + ko + WPART, WROW), fm = trd2(BM + ko, WROW), f4 = trd2(BV + ko, WROW);
          const u32x4 xg = lrd128((XG ^ (u32)(64 * k2)) + cs), gg = lrd128(GG + st * 128 + k2 * 64);
          hK = mfma(fk, xh, hK); hV = mfma(xh, fv, hV); hM = mfma(fm, xh, hM); hV4 = mfma(xh, f4, hV4); hG = mfma(gg, xg, hG);
          if (k2 == 0 && it == 0) {
            nonce = __builtin_amdgcn_readfirstlane(non0) + 1u;
            while (__ballot(inb0[0] == nonce) != 0ull) ++nonce;
            if (lane == 0) __builtin_amdgcn_raw_buffer_store_b32(nonce, rsS, NON_OFF + (u32)(sg * 64 + mi) * 4u, 0, AUX_SC1);
          }
          if (k2 == 0 && it == 8) {
            vn = __builtin_amdgcn_raw_buffer_load_b32(rsS, NON_OFF + (u32)(sg * 64 + lane) * 4u, 0, AUX_SC1);
            wo_blocks(rsO, wo, 0, WOA);
          }
        }
        cs = nxtslot(cs); st = nxt9(st);
      }
      if (c >= 8) { lwr64f(ek + 3 * 256, hK); if (g >= 2) { lwr64f(ek + 4 * 256, hM); lwr64f(ek + 4 * 256 - 128, hM); } }
      if (g >= 2) { lwr64f(ev + 3 * 256, hV); lwr64f(ev + 4 * 256, hV4); }
      const float gt = __builtin_amdgcn_rcpf(1.f + __builtin_amdgcn_exp2f(-1.4426950408889634f * hG[0]));
      if (r8 == (hg & 1)) __builtin_amdgcn_raw_buffer_store_b32(__float_as_uint(gt), rsS, GATE_OFF + (u32)(cp * 128 + 16 * (hg >> 1) + lane) * 4u, 0, AUX_SC1);
    }
    wo_blocks(rsO, wo, WOA, WOB - WOA);
    lgkm0();
    STAMP(5);
    bar();
    wo_blocks(rsO, wo, WOB, 9 - WOB);
    __builtin_amdgcn_s_setprio(0);
  } else {
    const int w = wid;
    u32 B = rb, BM = rb + (p >= 2 ? WPART + 112 : 128), XB[2];
    XB[0] = lds0 + (32 * w + c) * 128 + ((g ^ (c >> 1)) * 16); XB[1] = XB[0] ^ 64;
    f32x4 bias[2]; u32 mq[5], mP0, mP1;
    {
      const float NINF = -__builtin_inff();
      const bool first = (chunk == 0 && w == 0);
#pragma unroll
      for (int r = 0; r < 4; ++r) {
        const int i = 4 * g + r;
        const bool okc = (i <= c && i >= c - 7), okp = i >= c + 9;
        bias[0][r] = (okc || (okp && !first)) ? 0.f : NINF; bias[1][r] = (okc || okp) ? 0.f : NINF;
      }
      mq[0] = ~0u; mq[1] = g == 0 ? ~0u : 0u; mq[2] = ~mq[1]; mq[3] = g <= 1 ? ~0u : 0u; mq[4] = ~mq[3];
      mP0 = c < 8 ? mq[4] : 0u; mP1 = ~mP0;
      PIN(bias[0]); PIN(bias[1]); PIN(mq[1]); PIN(mq[2]); PIN(mq[3]); PIN(mq[4]); PIN(mP0); PIN(mP1);
    }
    u32 ek = lds0 + XCH + w * XCB + (g * 8 + (c & 7)) * 8, ev = lds0 + XCH + w * XCB + 1280 + (lane & 31) * 8;
    PIN(ek); PIN(ev);
    u32 so[2], st[2]; u32x4 ones = {0x3f803f80u, 0x3f803f80u, 0x3f803f80u, 0x3f803f80u};
    { const u32 ob = (u32)((cp * 8 + 2 * w) * 36 + 9 * (hg >> 2)) * 1024u;
#pragma unroll
      for (int t = 0; t < 2; ++t) { so[t] = lane * 16u + ob + t * 36864u + (hg & 3) * 2048u; st[t] = ((c * 16u + g * 8u) | (mq[4] & 0x80000000u)) + ob + t * 36864u + 8192u + (hg & 3) * 256u; PIN(so[t]); PIN(st[t]); }
      PIN(ones); }
    f32x4 acc[2][14];
#pragma unroll
    for (int t = 0; t < 2; ++t)
#pragma unroll
      for (int i = 0; i < 14; ++i) { acc[t][i] = z4; PIN(acc[t][i]); }
    PIN(B); PIN(BM); PIN(XB[0]); PIN(XB[1]);
    __builtin_amdgcn_s_waitcnt(0xC07F);
    u32x2 wlo[7], whi[7]; u32x4 xf[2][2];
    auto faddr = [&](auto NC) -> u32 {
      constexpr int n = decltype(NC)::value; constexpr int f = n % 14, ko = (n / 14) * 32 * WROW;
      if constexpr (f < 4) return B + ko + WPART + 32 * f;
      else if constexpr (f == 4) return BM + ko;
      else if constexpr (f < 10) return B + ko + 2 * WPART + 32 * (f - 5);
      else return B + ko + 32 * (f - 10);
    };
    u32x10 Kp[3]; u32 Qp[2][10]; u32x6 Vp[5];
    auto pack = [&](auto FC, auto TC) {
      constexpr int f = decltype(FC)::value, t = decltype(TC)::value;
      u32 a0 = pk(acc[t][f][0], acc[t][f][1]), a1 = pk(acc[t][f][2], acc[t][f][3]);
      asm volatile("" : "+v"(a0), "+v"(a1));
      if constexpr (f < 4) { Kp[t + 1][2 * f] = a0; Kp[t + 1][2 * f + 1] = a1; }
      else if constexpr (f == 4) {
        auto r0 = __builtin_amdgcn_permlane32_swap(a0, a0, false, false); auto r1 = __builtin_amdgcn_permlane32_swap(a1, a1, false, false);
        Qp[t][8] = r0[0]; Qp[t][9] = r1[0]; Kp[t + 1][8] = r0[1]; Kp[t + 1][9] = r1[1];
      }
      else if constexpr (f < 10) { Vp[f - 5][2 * t + 2] = a0; Vp[f - 5][2 * t + 3] = a1; }
      else { Qp[t][2 * (f - 10)] = a0; Qp[t][2 * (f - 10) + 1] = a1; }
    };
    wait_vm<0>();
    STAMP(2);
    bar();
    STAMP(3);
    xf[0][0] = lrd128(XB[0]); xf[0][1] = lrd128(XB[0] + 2048);
    sfor<0, PD>([&](auto NC) { constexpr int n = decltype(NC)::value; const u32 a = faddr(NC); wlo[n % 7] = trd(a); whi[n % 7] = trd(a + WROW); SBAR(); });
    auto stage = [&](auto LC, int d) {
      constexpr bool LAST = decltype(LC)::value;
      sfor<0, 28>([&](auto NC) {
        constexpr int n = decltype(NC)::value;
        constexpr int f = n % 14, k2 = n / 14, nn = (n + PD) % 28, kn = nn / 14;
        constexpr bool RD = !(LAST && n + PD >= 28), BI = LAST && k2 == 1;
        constexpr int fx = f - 2;
        if constexpr ((nn == 0 && !LAST) || nn == 14) { SBAR(); bar(); SBAR(); }
        const u32x4 wv = {wlo[n % 7][0], wlo[n % 7][1], whi[n % 7][0], whi[n % 7][1]};
        const u32 a = faddr(IC<nn>{});
        if constexpr (f >= 5 && f < 10) mfma_acc<BI>(acc[0][f], xf[k2][0], wv); else mfma_acc<BI>(acc[0][f], wv, xf[k2][0]);
        SBAR();
        if constexpr (RD) wlo[nn % 7] = trd(a);
        if constexpr (BI && fx >= 0) pack(IC<(fx >= 0 ? fx : 0)>{}, IC<1>{});
        SBAR();
        if constexpr (f >= 5 && f < 10) mfma_acc<BI>(acc[1][f], xf[k2][1], wv); else mfma_acc<BI>(acc[1][f], wv, xf[k2][1]);
        SBAR();
        if constexpr (RD) whi[nn % 7] = trd(a + WROW);
        if constexpr (BI && !RD) pack(IC<2 * (f - 8)>{}, IC<0>{});
        if constexpr (!LAST) {
          if constexpr (nn == 27) { B += d; PIN(B); }
          if constexpr (nn == 19) { BM += d; PIN(BM); }
          if constexpr (nn == 7) { XB[0] += d; PIN(XB[0]); }
          if constexpr (nn == 20) { XB[1] += d; PIN(XB[1]); }
        }
        if constexpr (RD) {
          if constexpr (nn % 14 == 1) xf[kn][0] = lrd128(XB[kn]);
          if constexpr (nn % 14 == 3) xf[kn][1] = lrd128(XB[kn] + 2048);
        }
        SBAR();
      });
    };
    int sl = 0;
#pragma unroll 1
    for (int it = 0; it < 8; ++it) { const int sn = nxtslot(sl), d = sn - sl; sl = sn; stage(IC<0>{}, d); }
    STAMP(4);
    stage(IC<1>{}, 0);
    static_assert(PD == 6, "pack schedule");
    if (w < 3) {
      if (c >= 8) {
#pragma unroll
        for (int i = 0; i < 5; ++i) lwr64(ek + XCB + i * 256, Kp[2][2 * i], Kp[2][2 * i + 1]);
      }
      if (g >= 2) {
#pragma unroll
        for (int i = 0; i < 5; ++i) lwr64(ev + XCB + i * 256, Vp[i][4], Vp[i][5]);
      }
    }
    SBAR();
    pack(IC<12>{}, IC<1>{}); pack(IC<13>{}, IC<1>{});
    sfor<0, 6>([&](auto J) { pack(IC<2 * decltype(J)::value + 1>{}, IC<0>{}); });
    pack(IC<12>{}, IC<0>{}); pack(IC<13>{}, IC<0>{});
    lgkm0();
    STAMP(5);
    bar();
    STAMP(6);
#pragma unroll
    for (int i = 0; i < 5; ++i) {
      const u32x2 a = lrd64(ek + i * 256), v = lrd64(ev + i * 256);
      Kp[0][2 * i] = a[0]; Kp[0][2 * i + 1] = a[1]; Vp[i][0] = v[0]; Vp[i][1] = v[1];
    }
    PIN(Kp[1]); PIN(Kp[2]);
    SBAR();
    const bool selp = (c < 8) && (g >= 2);
    f32x4 sp[2][4], sc[2][4]; float mx[2][4];
    sfor<0, 8>([&](auto IC_) {
      constexpr int t = 1 - (decltype(IC_)::value >> 2), h = decltype(IC_)::value & 3;
      if constexpr (t == 0 && h == 0) PIN(Kp[0]);
      constexpr int ia0 = h == 0 ? 0 : (h == 3 ? 4 : 2), ia1 = h <= 1 ? 0 : 2, ib0 = h <= 1 ? 1 : 3, ib1 = h == 0 ? -1 : (h == 3 ? 3 : 1);
      u32x4 qo;
      qo[0] = Qp[t][2 * h] & mq[ia0]; qo[1] = Qp[t][2 * h + 1] & mq[ia1]; qo[2] = Qp[t][2 * h + 2] & mq[ib0];
      if constexpr (ib1 >= 0) qo[3] = Qp[t][2 * h + 3] & mq[ib1 >= 0 ? ib1 : 0]; else qo[3] = 0u;
      const u32x4 k0 = __builtin_shufflevector(Kp[t], Kp[t], 2 * h, 2 * h + 1, 2 * h + 2, 2 * h + 3);
      const u32x4 k1 = __builtin_shufflevector(Kp[t + 1], Kp[t + 1], 2 * h, 2 * h + 1, 2 * h + 2, 2 * h + 3);
      sp[t][h] = mfma(k0, qo, bias[t]); sc[t][h] = mfma(k1, qo, bias[t]);
    });
    __builtin_amdgcn_sched_group_barrier(0x2, 4, 0);
#pragma unroll
    for (int i = 0; i < 16; ++i) { __builtin_amdgcn_sched_group_barrier(0x8, 1, 0); __builtin_amdgcn_sched_group_barrier(0x2, 2, 0); }
    SBAR();
    if (selp) {
#pragma unroll
      for (int t = 1; t >= 0; --t)
#pragma unroll
        for (int h = 0; h < 4; ++h) { sc[t][h] = sp[t][h]; PIN(sc[t][h]); }
    }
    SBAR();
#pragma unroll
    for (int t = 0; t < 2; ++t)
#pragma unroll
      for (int h = 0; h < 4; ++h) mx[t][h] = fmx(fmx(fmx(sc[t][h][0], sc[t][h][1]), sc[t][h][2]), sc[t][h][3]);
#pragma unroll
    for (int t = 0; t < 2; ++t) {
      float* m = mx[t];
      sw32(m[0], m[1]); float m01 = fmx(m[0], m[1]); sw32(m[2], m[3]); float m23 = fmx(m[2], m[3]);
      sw16(m01, m23); float R = fmx(m01, m23) * -CEXP;
      float a = R, bb = R; sw16(a, bb); m[0] = a; m[1] = a; sw32(m[0], m[1]); m[2] = bb; m[3] = bb; sw32(m[2], m[3]);
    }
#pragma unroll
    for (int i = 0; i < 5; ++i) PIN(Vp[i]);
    SBAR();
    STAMP(18);
    f32x4 olo[2][4], ohi[2][4], sm[2][4];
    sfor<0, 8>([&](auto IC_) {
      constexpr int t = decltype(IC_)::value >> 2, h = decltype(IC_)::value & 3;
      const f32x2 cc = {CEXP, CEXP}, mm = {mx[t][h], mx[t][h]};
      const f32x2 e0 = __builtin_elementwise_fma((f32x2){sc[t][h][0], sc[t][h][1]}, cc, mm), e1 = __builtin_elementwise_fma((f32x2){sc[t][h][2], sc[t][h][3]}, cc, mm);
      const u32 p0 = pk(__builtin_amdgcn_exp2f(e0[0]), __builtin_amdgcn_exp2f(e0[1])), p1 = pk(__builtin_amdgcn_exp2f(e1[0]), __builtin_amdgcn_exp2f(e1[1]));
      const u32x4 po = {p0 & mP0, p1 & mP0, p0 & mP1, p1 & mP1};
      sm[t][h] = mfma(ones, po, z4);
      olo[t][h] = mfma(__builtin_shufflevector(Vp[h], Vp[h], 2 * t, 2 * t + 1, 2 * t + 2, 2 * t + 3), po, z4);
      ohi[t][h] = mfma(__builtin_shufflevector(Vp[h + 1], Vp[h + 1], 2 * t, 2 * t + 1, 2 * t + 2, 2 * t + 3), po, z4);
    });
    SBAR();
    f32x2 ov[2][10]; float inv[2][4];
#pragma unroll
    for (int t = 0; t < 2; ++t) {
#pragma unroll
      for (int h = 0; h < 4; ++h) inv[t][h] = __builtin_amdgcn_rcpf(sm[t][h][0]);
#pragma unroll
      for (int h = 0; h < 4; ++h) { ov[t][2 * h] = (f32x2){olo[t][h][0], olo[t][h][1]} * inv[t][h]; ov[t][2 * h + 1] = (f32x2){olo[t][h][2], olo[t][h][3]} * inv[t][h]; }
      ov[t][8] = (f32x2){ohi[t][3][0], ohi[t][3][1]} * inv[t][3]; ov[t][9] = (f32x2){ohi[t][3][2], ohi[t][3][3]} * inv[t][3];
    }
    if (g < 2) {
#pragma unroll
      for (int t = 0; t < 2; ++t) { ov[t][6] = (f32x2){ohi[t][2][0], ohi[t][2][1]} * inv[t][2]; PIN(ov[t][6]); }
      if (g == 0) {
#pragma unroll
        for (int t = 0; t < 2; ++t) {
          ov[t][2] = (f32x2){ohi[t][0][0], ohi[t][0][1]} * inv[t][0]; PIN(ov[t][2]);
          ov[t][4] = (f32x2){ohi[t][1][0], ohi[t][1][1]} * inv[t][1]; PIN(ov[t][4]);
          ov[t][5] = (f32x2){ohi[t][1][2], ohi[t][1][3]} * inv[t][1]; PIN(ov[t][5]);
          ov[t][7] = (f32x2){ohi[t][2][2], ohi[t][2][3]} * inv[t][2]; PIN(ov[t][7]);
        }
      }
    }
    STAMP(17);
#pragma unroll
    for (int t = 0; t < 2; ++t) {
      u32 op[10];
#pragma unroll
      for (int i = 0; i < 10; ++i) op[i] = pk(ov[t][i][0], ov[t][i][1]);
      __builtin_amdgcn_raw_buffer_store_b128((u32x4){op[0], op[1], op[2], op[3]}, rsS, so[t], 0, AUX_SC1);
      __builtin_amdgcn_raw_buffer_store_b128((u32x4){op[4], op[5], op[6], op[7]}, rsS, so[t] + 1024u, 0, AUX_SC1);
      __builtin_amdgcn_raw_buffer_store_b64((u32x2){op[8], op[9]}, rsS, st[t], 0, AUX_SC1);
    }
  }
  const int kq = wid & 3, th = wid >> 2;
  u32 WB[4];
  { const int pos = 4 * g + q, s = (pos >> 1) & 3;
#pragma unroll
    for (int i = 0; i < 4; ++i) { WB[i] = lds0 + (kq + (th ? 20 : 0)) * 4096 + pos * 128 + (((i ^ (2 * (kq & 1))) ^ s) * 32) + p * 8; PIN(WB[i]); } }
  u32 myin = INB_OFF + (u32)(sg * 64 + mi) * 512u + lane * 8u, dst = INB_OFF + (u32)(sg * 64 + lane) * 512u + mi * 8u;
  PIN(myin); PIN(dst);
  const int T0 = 32 * sg + 4 * rg + 2 * th + ((kq >> 1) ^ (FLIP & ct & 1)), TX = 32 * sg + 28 + rg;
  u32 l16 = lane * 16u; PIN(l16);
  u32 so_k, so_b[3];
  { int r = rot2 + (th ? 5 : 0); r = r >= 9 ? r - 9 : r; so_k = r * 1024u;
    so_b[0] = rg < 4 ? (u32)(TX * 36 + 9 * kq) * 1024u : 0xf0000000u;
    so_b[1] = (u32)((T0 ^ 1) * 36 + 9 * kq) * 1024u; so_b[2] = (u32)(T0 * 36 + 9 * kq) * 1024u;
    asm volatile("" : "+s"(so_k), "+s"(so_b[0]), "+s"(so_b[1]), "+s"(so_b[2])); }
  u32 RW[3];
#pragma unroll
  for (int j = 1; j < 4; ++j) { RW[j - 1] = lds0 + 32768 + ((th * 4 + (kq ^ j)) * 6 + (j - 1) * 2) * 1024 + lane * 16; PIN(RW[j - 1]); }
  STAMP(7);
  wait_vm<0>();
  STAMP(8);
  bar();
  STAMP(9);
  if (wid == 7) __builtin_amdgcn_raw_buffer_store_b64((u32x2){vn, nonce}, rsS, dst, 0, AUX_SC1);
  *(LAS u32*)(uintptr_t)(lds0 + CNT + wid * 4) = 0u;
  u32x4 wf[9][4];
  sfor<0, 9>([&](auto KC) { constexpr int k = decltype(KC)::value;
    if constexpr (k == 4) {
      const u32 d = th ? (u32)(4 - 9) * 16384u : 4u * 16384u;
#pragma unroll
      for (int i = 0; i < 4; ++i) { WB[i] += d; PIN(WB[i]); }
    }
    if constexpr (k == 8) {
#pragma unroll
      for (int i = 0; i < 4; ++i) { WB[i] += 4u * 16384u; PIN(WB[i]); }
    }
#pragma unroll
    for (int i = 0; i < 4; ++i) wf[k][i] = trd2(WB[i] + (k & 3) * 16384, 2048); });
  SBAR();
  bool allok = true;
  if (wid == 7) {
    const int cl = (lane >> 2) & 3;
    const u64 need = __ballot(cl == (rg >> 1) || (rg < 4 && cl == 3));
    u64 got = 0;
#pragma unroll 1
    for (int spin = 0; spin < (1 << 17); ++spin) {
      const u32x2 v = __builtin_amdgcn_raw_buffer_load_b64(rsS, myin, 0, AUX_SC1);
      const bool ok = v[0] == nonce;
      if (ok && v[1] != vn) { vn = v[1]; __builtin_amdgcn_raw_buffer_store_b64((u32x2){vn, nonce}, rsS, dst, 0, AUX_SC1); }
      got = __ballot(ok);
      if ((got & need) == need) break;
      __builtin_amdgcn_s_sleep(1);
    }
    allok = got == ~0ull;
  }
  lgkm0();
  STAMP(10);
  bar();
  STAMP(11);
  if (rg < 7) {
    constexpr int OW = OWN, NL = 23;
    u32x4 of[OW + 1];
    const u32 so_k0 = so_k;
    auto ldo = [&](auto NC) { constexpr int n = decltype(NC)::value; constexpr int ph = ord_ph(n), k = ord_k(n);
      if constexpr (k == 0) so_k = so_k0;
      of[n % (OW + 1)] = __builtin_amdgcn_raw_buffer_load_b128(rsS, l16, (ph == 0 && k == 4 && th) ? 0xf0000000u : so_b[ph] + so_k, AUX_SC1);
      if constexpr (n + 1 < NL && ord_k(n + 1) == k + 1) so_k = (so_k == 8192u) ? 0u : so_k + 1024u; };
    const int L = 32 * (wid & 1) + (lane >> 1);
    float gm = 0.f, gx = 0.f;
    auto ldg = [&]() {
      gm = __uint_as_float(__builtin_amdgcn_raw_buffer_load_b32(rsS, c * 4u, GATE_OFF + (u32)T0 * 64u, AUX_SC1));
      gx = __uint_as_float(__builtin_amdgcn_raw_buffer_load_b32(rsS, (L & 15) * 4u, GATE_OFF + (u32)(TX & 127) * 64u, AUX_SC1)); };
    if (GL == 0) ldg();
    sfor<0, OW>([&](auto NC) { ldo(NC); SBAR(); });
    f32x4 ac[3][4], T[2];
    const __amdgpu_buffer_rsrc_t rsD = mkrs(out, 2048 * 1152);
    const int row = 2 * (128 * (T0 >> 4) + 16 * (T0 & 7) + c) + ((T0 >> 3) & 1);
    u32 po = (u32)(row * 576 + ct * 64 + (kq & 1) * 32 + (g & 1) * 16 + (g >> 1) * 8) * 2u;
    const int rowx = 2 * (128 * (TX >> 4) + 16 * (TX & 7) + (L & 15)) + ((TX >> 3) & 1);
    u32 px = (u32)(rowx * 576 + ct * 64 + (wid >> 1) * 16 + (L >> 4) * 4 + (lane & 1) * 2) * 2u;
    u32 ra = lds0 + 32768 + wid * 6144 + l16, rx = lds0 + wid * 512 + lane * 8, wx = (lds0 + wid * 4096 + l16) ^ (u32)((kq & 1) * 2048), wx2 = wx ^ 2048u;
    u32 fw = lds0 + CNT + wid * 4, fg = lds0 + CNT + th * 16;
    PIN(po); PIN(px); PIN(ra); PIN(rx); PIN(wx); PIN(wx2); PIN(fw); PIN(fg);
    auto flag = [&](u32 v) { asm volatile("" ::: "memory"); *(volatile LAS u32*)(uintptr_t)fw = v; };
    float fm = 0.f;
    sfor<0, NL>([&](auto NC) {
      constexpr int n = decltype(NC)::value; constexpr int ph = ord_ph(n), k = ord_k(n);
      if constexpr (n + OW < NL) ldo(IC<n + OW>{});
      if constexpr (GL > 0 && n == GL) ldg();
      SBAR();
#pragma unroll
      for (int ii = 0; ii < 4; ++ii) { const int i = ii ^ 2; ac[ph][i] = mfma(wf[k][i], of[n % (OW + 1)], k == 0 ? z4 : ac[ph][i]); }
      SBAR();
      if constexpr (ORD < 2) {
        if constexpr (n >= 10 && n < 14) lwr128(((n - 10) & 2 ? wx2 : wx) + ((n - 10) & 1) * 1024, ac[0][n - 10]);
        if constexpr (n >= 14 && n < 16) { lwr128(RW[n - 13], ac[1][2 * (n - 14)]); lwr128(RW[n - 13] + 1024, ac[1][2 * (n - 14) + 1]); }
      } else {
        if constexpr (n >= 10 && n < 14) lwr128(RW[1 + ((n - 10) >> 1)] + ((n - 10) & 1) * 1024, ac[1][n - 10]);
        if constexpr (n >= 14 && n < 16) { lwr128((n == 14 ? wx : wx2), ac[0][2 * (n - 14)]); lwr128((n == 14 ? wx : wx2) + 1024, ac[0][2 * (n - 14) + 1]); }
      }
      if constexpr (n == 15) flag(2u);
      if constexpr (n == MID) {
#pragma unroll 1
        for (;;) {
          const u32x4 f = *(volatile LAS u32x4*)(uintptr_t)fg;
          asm volatile("" ::: "memory");
#pragma unroll
          for (int t = 0; t < 2; ++t) T[t] = *(LAS f32x4*)(uintptr_t)(ra + 2048 + t * 1024) + *(LAS f32x4*)(uintptr_t)(ra + 4096 + t * 1024);
          asm volatile("" ::: "memory");
          if (__builtin_amdgcn_readfirstlane(f[0] & f[1] & f[2] & f[3]) & 2u) break;
        }
        fm = (kq >> 1) ? 1.f - gm : gm;
        PIN(T[0]); PIN(T[1]); PIN(fm);
      }
      SBAR();
    });
    STAMP(12);
    lwr128(RW[0], ac[2][2]); lwr128(RW[0] + 1024, ac[2][3]);
    flag(3u);
    f32x4 D[2];
#pragma unroll
    for (int t = 0; t < 2; ++t) { D[t] = (ac[2][t] - T[t]) * fm + T[t]; PIN(D[t]); }
    {
      f32x4 r[2];
#pragma unroll 1
      for (;;) {
        const u32 f = *(volatile LAS u32*)(uintptr_t)(fw ^ 4u);
        asm volatile("" ::: "memory");
#pragma unroll
        for (int t = 0; t < 2; ++t) r[t] = *(LAS f32x4*)(uintptr_t)(ra + t * 1024);
        asm volatile("" ::: "memory");
        if (__builtin_amdgcn_readfirstlane(f) == 3u) break;
      }
      STAMP(13);
      f32x4 v[2];
#pragma unroll
      for (int t = 0; t < 2; ++t) v[t] = r[t] * fm + D[t];
      auto s0_ = __builtin_amdgcn_permlane16_swap(pk(v[0][0], v[0][1]), pk(v[1][0], v[1][1]), false, false);
      auto s1_ = __builtin_amdgcn_permlane16_swap(pk(v[0][2], v[0][3]), pk(v[1][2], v[1][3]), false, false);
      __builtin_amdgcn_raw_buffer_store_b128((u32x4){s0_[0], s1_[0], s0_[1], s1_[1]}, rsD, po, 0, 0);
    }
    if (rg < 4 && (XALL || th == 0)) {
#pragma unroll
      for (int hf = 0; hf < (XALL ? 1 : 2); ++hf) {
        f32x2 e[8];
#pragma unroll 1
        for (;;) {
          const u32x4 f = *(volatile LAS u32x4*)(uintptr_t)(fg ^ 16u);
          asm volatile("" ::: "memory");
#pragma unroll
          for (int j = 0; j < 8; ++j) e[j] = *(LAS f32x2*)(uintptr_t)(rx + j * 4096 + hf * 2048);
          asm volatile("" ::: "memory");
          if (__builtin_amdgcn_readfirstlane(f[0] & f[1] & f[2] & f[3]) & 2u) break;
        }
        const f32x2 A = (e[0] + e[1]) + (e[4] + e[5]), Bv = (e[2] + e[3]) + (e[6] + e[7]);
        const f32x2 r = (A - Bv) * gx + Bv;
        __builtin_amdgcn_raw_buffer_store_b32(pk(r[0], r[1]), rsD, px + hf * 64, 0, 0);
      }
    }
  }
  if (!allok) {
#pragma unroll 1
    for (int spin = 0; spin < (1 << 17); ++spin) {
      const u32x2 v = __builtin_amdgcn_raw_buffer_load_b64(rsS, myin, 0, AUX_SC1);
      const bool ok = v[0] == nonce;
      if (ok && v[1] != vn) { vn = v[1]; __builtin_amdgcn_raw_buffer_store_b64((u32x2){vn, nonce}, rsS, dst, 0, AUX_SC1); }
      if (__ballot(ok) == ~0ull) break;
      __builtin_amdgcn_s_sleep(1);
    }
  }
  STAMP(14);
  STAMP(15); RSTAMP(16);
  STAMP_OUT();
}

extern "C" void kernel_launch(void* const* d_in, const int* in_sizes, int n_in, void* d_out, int out_size, void* d_ws, size_t ws_size, hipStream_t stream) {
  const u16* x = (const u16*)d_in[0]; const u16* wqa = (const u16*)d_in[1]; const u16* woa = (const u16*)d_in[2];
  const u16* wqb = (const u16*)d_in[4]; const u16* wob = (const u16*)d_in[5]; const u16* gw = (const u16*)d_in[6];
  (void)hipFuncSetAttribute((const void*)fused, hipFuncAttributeMaxDynamicSharedMemorySize, LDS_BYTES);
  fused<<<256, 512, LDS_BYTES, stream>>>(x, wqa, woa, wqb, wob, gw, (u16*)d_out, (char*)d_ws);
}
```

```cpp
#define PROBE 1
#define PW 0
#include <hip/hip_runtime.h>
typedef unsigned short u16; typedef unsigned int u32; typedef unsigned long long u64;
typedef __attribute__((ext_vector_type(8))) __bf16 bf16x8;
typedef __attribute__((ext_vector_type(2))) __bf16 bf16x2;
typedef __attribute__((ext_vector_type(4))) float f32x4;
typedef __attribute__((ext_vector_type(2))) float f32x2;
typedef __attribute__((ext_vector_type(4))) short s16x4;
typedef __attribute__((ext_vector_type(4))) u32 u32x4;
typedef __attribute__((ext_vector_type(2))) u32 u32x2;
typedef __attribute__((ext_vector_type(6))) u32 u32x6;
typedef __attribute__((ext_vector_type(10))) u32 u32x10;
#define LAS __attribute__((address_space(3)))
#define DI __device__ __forceinline__
#define SBAR() __builtin_amdgcn_sched_barrier(0)
#define PIN(x) asm volatile("" : "+v"(x))
template <int V> struct IC { static constexpr int value = V; };
template <int A, int B, class F> __device__ __forceinline__ void sfor(F&& f) { if constexpr (A < B) { f(IC<A>{}); sfor<A + 1, B>(f); } }
#define ROT 7
#define ROT2 1
#define PD 6
#define PRE 4
#define DIAG 0
#define MID 19
#define XALL 0
#define WOA 1
#define WOB 3
#define FLIP 0
#define DEPTH 2

constexpr int WROW = 144, WPART = 9232, XSTG = 136 * 128, STG = 45184;
constexpr int LDS_BYTES = 163840;
constexpr int XCH = 147456, XCB = 2560, GWL = XCH + 4 * XCB, CNT = GWL + 2048;
constexpr u32 OP_BYTES = 128u * 36u * 1024u;
constexpr u32 GATE_OFF = OP_BYTES;
constexpr u32 NON_OFF = GATE_OFF + 8192;
constexpr u32 INB_OFF = NON_OFF + 1024;
constexpr u32 WS_BYTES = INB_OFF + 256 * 512;
constexpr float CEXP = 0.23570226039551584f * 1.4426950408889634f;
constexpr int AUX_SC1 = 16;
constexpr u32 CODE_BYTES = 16768;

DI u32 pk(float a, float b) { f32x2 v = {a, b}; bf16x2 r = __builtin_convertvector(v, bf16x2); return __builtin_bit_cast(u32, r); }
DI f32x4 mfma(u32x4 a, u32x4 b, f32x4 c) { return __builtin_amdgcn_mfma_f32_16x16x32_bf16(__builtin_bit_cast(bf16x8, a), __builtin_bit_cast(bf16x8, b), c, 0, 0, 0); }
template <bool BI> DI void mfma_acc(f32x4& c, u32x4 a, u32x4 b) {
  if constexpr (BI) c = mfma(a, b, c);
  else asm volatile("v_mfma_f32_16x16x32_bf16 %0, %1, %2, %0" : "+v"(c) : "v"(a), "v"(b));
}
DI u32x2 trd(u32 a) { return __builtin_bit_cast(u32x2, __builtin_amdgcn_ds_read_tr16_b64_v4i16((LAS s16x4*)(uintptr_t)a)); }
DI u32x4 trd2(u32 a, int st) { const u32x2 x = trd(a), y = trd(a + st); return (u32x4){x[0], x[1], y[0], y[1]}; }
DI u32x4 lrd128(u32 a) { return *(LAS u32x4*)(uintptr_t)a; }
DI u32x2 lrd64(u32 a) { return *(LAS u32x2*)(uintptr_t)a; }
DI void lwr64(u32 a, u32 x, u32 y) { *(LAS u32x2*)(uintptr_t)a = (u32x2){x, y}; }
DI void lwr64f(u32 a, f32x4 v) { lwr64(a, pk(v[0], v[1]), pk(v[2], v[3])); }
DI void lwr128(u32 a, f32x4 v) { *(LAS f32x4*)(uintptr_t)a = v; }
#define NT 0
#define NT0 ""
#define NT1 ""
DI void dma16(__amdgpu_buffer_rsrc_t rs, u32 voff, u32 soff, u32 ldsdst) {
  asm volatile("s_mov_b32 m0, %0\n\ts_nop 0\n\tbuffer_load_dwordx4 %1, %2, %3 offen" NT0 " lds" :: "s"(ldsdst), "v"(voff), "s"(rs), "s"(soff) : "memory");
}
#define WIN 9
#define WINO 6
template <int WN = WIN> DI void dma16w(__amdgpu_buffer_rsrc_t rs, u32 voff, u32 soff, u32 ldsdst) {
  asm volatile("s_mov_b32 m0, %0\n\ts_waitcnt vmcnt(%4)\n\tbuffer_load_dwordx4 %1, %2, %3 offen" NT1 " lds" :: "s"(ldsdst), "v"(voff), "s"(rs), "s"(soff), "i"(WN) : "memory");
}
template <int N> DI void wait_vm() { asm volatile("s_waitcnt vmcnt(%0)" :: "i"(N) : "memory"); }
DI void bar() { asm volatile("s_barrier" ::: "memory"); }
DI void lgkm0() { asm volatile("s_waitcnt lgkmcnt(0)" ::: "memory"); }
#define STAMP(i) do { if (PROBE == (i)) { prt = __builtin_amdgcn_s_memrealtime(); __builtin_amdgcn_s_waitcnt(0xC07F); } } while (0)
#define RSTAMP(i)
#define STAMP_DECL unsigned long long prt = 0
#define STAMP_OUT() do { if (PROBE && wid == PW) { while (__builtin_amdgcn_s_memrealtime() - prt < 3000) __builtin_amdgcn_s_sleep(2); } } while (0)
DI float dot2(u32 a, u32 b, float c) { return __builtin_amdgcn_fdot2_f32_bf16(__builtin_bit_cast(bf16x2, a), __builtin_bit_cast(bf16x2, b), c, false); }
DI float fmx(float a, float b) { return __builtin_elementwise_maximum(a, b); }
DI void sw32(float& a, float& b) { auto r = __builtin_amdgcn_permlane32_swap(__float_as_uint(a), __float_as_uint(b), false, false); a = __uint_as_float(r[0]); b = __uint_as_float(r[1]); }
DI void sw16(float& a, float& b) { auto r = __builtin_amdgcn_permlane16_swap(__float_as_uint(a), __float_as_uint(b), false, false); a = __uint_as_float(r[0]); b = __uint_as_float(r[1]); }
DI int nxt9(int s) { return s == 8 ? 0 : s + 1; }
DI int nxtslot(int s) { return s == 2 * STG ? 0 : s + STG; }
DI __amdgpu_buffer_rsrc_t mkrs(const void* p, u32 n) { return __builtin_amdgcn_make_buffer_rsrc((void*)p, 0, n, 0x00020000); }
DI u32 wvoff(u32 ci) {
  const u32 pos = __umul24(ci, 7282u) >> 16, ch = ci - __umul24(pos, 9u);
  const u32 row = (pos & ~7u) | ((pos & 1u) << 2) | ((pos & 7u) >> 1);
  return __umul24(row, 3456u) + ch * 16u;
}

struct WoState { u32 vr[4], vt[4]; u32 dst; int kk; u32 base; };
DI void wo_blocks(__amdgpu_buffer_rsrc_t rs, WoState w, int v0, int n) {
  w.dst += v0 * 16384; w.kk += v0; w.kk = w.kk >= 9 ? w.kk - 9 : w.kk;
#pragma unroll 1
  for (int i = 0; i < n; ++i) {
    if (w.kk < 8) {
      const u32 so = w.base + (u32)(36 * w.kk - 4 * (w.kk & 1)) * 1152u;
      asm volatile("s_mov_b32 m0, %0\n\ts_waitcnt vmcnt(%7)\n\tbuffer_load_dwordx4 %1, %5, %6 offen lds\n\tbuffer_load_dwordx4 %2, %5, %6 offen offset:1024 lds\n\t"
                   "buffer_load_dwordx4 %3, %5, %6 offen offset:2048 lds\n\tbuffer_load_dwordx4 %4, %5, %6 offen offset:3072 lds"
                   :: "s"(w.dst), "v"(w.vr[0]), "v"(w.vr[1]), "v"(w.vr[2]), "v"(w.vr[3]), "s"(rs), "s"(so), "i"(WINO) : "memory");
      ++w.kk;
    } else {
      asm volatile("s_mov_b32 m0, %0\n\ts_waitcnt vmcnt(%7)\n\tbuffer_load_dwordx4 %1, %5, %6 offen lds\n\tbuffer_load_dwordx4 %2, %5, %6 offen offset:1024 lds\n\t"
                   "buffer_load_dwordx4 %3, %5, %6 offen offset:2048 lds\n\tbuffer_load_dwordx4 %4, %5, %6 offen offset:3072 lds"
                   :: "s"(w.dst), "v"(w.vt[0]), "v"(w.vt[1]), "v"(w.vt[2]), "v"(w.vt[3]), "s"(rs), "s"(w.base), "i"(WINO) : "memory");
      w.kk = 0;
    }
    w.dst += 16384;
  }
}

#define ORD 0
#define OWN 6
#define GL 8
constexpr int ord_ph(int n) { return n >= 14 ? 2 : (ORD == 0 ? (n < 5 ? 0 : 1) : ORD == 1 ? (n < 10 ? 1 - (n & 1) : 1) : (n < 9 ? 1 : 0)); }
constexpr int ord_k(int n) { return n >= 14 ? n - 14 : (ORD == 0 ? (n < 5 ? n : n - 5) : ORD == 1 ? (n < 10 ? n >> 1 : n - 5) : (n < 9 ? n : n - 9)); }
__global__ __launch_bounds__(512, 2) void fused(const u16* __restrict__ x, const u16* __restrict__ wqa, const u16* __restrict__ woa,
                                                const u16* __restrict__ wqb, const u16* __restrict__ wob, const u16* __restrict__ gw,
                                                u16* __restrict__ out, char* __restrict__ ws) {
  extern __shared__ __attribute__((aligned(16))) char lds[];
  const int tid = threadIdx.x, lane = tid & 63;
  const int wid = __builtin_amdgcn_readfirstlane(tid >> 6);
  STAMP_DECL; RSTAMP(0); STAMP(1);
  const int b = blockIdx.x, xcd = b & 7, bi = b >> 3;
  const int cp = 8 * (xcd & 1) + (bi & 7), hg = 4 * (xcd >> 1) + (bi >> 3);
  const int par = cp & 1, chunk = cp >> 1;
  const int hq = hg & 7;
  int s0 = 0;
  if (ROT == 1) { s0 = (bi & 7) + 2 * (bi >> 3); s0 = s0 >= 9 ? s0 - 9 : s0; }
  if (ROT == 2) s0 = 2 * (((bi & 7) + (bi >> 3)) & 3);
  if (ROT == 7) s0 = 2 * (((bi & 7) + (bi >> 3)) & 3) + (((xcd >> 1) + xcd) & 1);
  const int r8 = lane >> 3, sp8 = lane & 7;
  const u32 lds0 = (u32)(uintptr_t)(LAS char*)lds;
  {
    unsigned long long pc; asm volatile("s_getpc_b64 %0" : "=s"(pc));
    if (wid == 7 && lane < 5) {
      const unsigned long long pa = (pc & ~255ull) + min((unsigned)(bi * 5 + lane) * 128u, CODE_BYTES - 128u);
      asm volatile("s_mov_b32 m0, %0\n\ts_nop 0\n\tglobal_load_lds_dword %1, off" :: "s"(lds0 + 3 * STG + 1024), "v"(pa) : "memory");
    }
  }
  u32 voE = (u32)(2 * (128 * chunk + r8) + par) * 1152u + (u32)(sp8 ^ (r8 >> 1)) * 16u;
  int jh = 128 * chunk - 8 + r8; jh = jh < 0 ? 0 : jh;
  u32 voH = (u32)(2 * jh + par) * 1152u + (u32)(sp8 ^ (r8 & 6)) * 16u;
  const int u0 = 2 * wid, p0 = (u0 * 52) >> 8, i0 = u0 - 5 * p0, u1 = u0 + 1, p1 = (u1 * 52) >> 8, i1 = u1 - 5 * p1;
  u32 voO = voE ^ 64u, vw0 = wvoff(64 * i0 + lane), vw1 = wvoff(64 * i1 + lane);
  u32 sx = s0 * 128 + wid * 36864, dx = lds0 + wid * 2048;
  u32 sw0 = s0 * (64 * 3456) + p0 * 1152 + hq * 144, dw0 = lds0 + XSTG + p0 * WPART + i0 * 1024;
  u32 sw1 = s0 * (64 * 3456) + p1 * 1152 + hq * 144, dw1 = lds0 + XSTG + p1 * WPART + i1 * 1024;
  asm volatile("" : "+v"(voE), "+v"(voH), "+v"(voO), "+v"(vw0), "+v"(vw1), "+s"(sx), "+s"(dx), "+s"(sw0), "+s"(dw0), "+s"(sw1), "+s"(dw1));
  SBAR();
  const __amdgpu_buffer_rsrc_t rsX = mkrs(x, 2048 * 1152);
  const __amdgpu_buffer_rsrc_t rsW = mkrs((hg >> 3) ? wqb : wqa, 576 * 3456);
  {
    dma16(rsX, voE, sx, dx);
    dma16(rsX, voO, sx + 18432, dx + 1024);
    dma16(rsW, vw0, sw0, dw0);
    if (wid < 7) dma16(rsW, vw1, sw1, dw1);
    else {
      dma16(rsX, voH, s0 * 128, lds0 + 16384);
      const __amdgpu_buffer_rsrc_t rsG = mkrs(gw, 1152);
      dma16(rsG, lane * 16u, 0, lds0 + GWL); dma16(rsG, lane * 16u, 1024, lds0 + GWL + 1024);
    }
  }
  SBAR();
  STAMP(25);
  asm volatile("" :: "s"(woa), "s"(wob), "s"(gw), "s"(out), "s"(ws));
  const int sg = cp >> 2, mi = 16 * (hg >> 2) + 4 * (cp & 3) + (hg & 3);
  const int rg = (mi * 57) >> 9, ct = mi - 9 * rg;
  int rot2 = ROT2 ? ct + 2 * (rg & 1) + 4 * (sg & 1) : 0; rot2 = rot2 >= 9 ? rot2 - 9 : rot2;
  const int c = lane & 15, g = lane >> 4, q = (lane >> 2) & 3, p = lane & 3;
  const __amdgpu_buffer_rsrc_t rsS = mkrs(ws, WS_BYTES);
  const f32x4 z4 = {0.f, 0.f, 0.f, 0.f};
  u32 nonce = 0, vn = 0;
  const u32 rb = lds0 + XSTG + (8 * g + 2 * q) * WROW + 8 * p;

  if (wid >= 4) {
    __builtin_amdgcn_s_setprio(2);
    const int hw = wid - 4;
    u32 HB = lds0 + (128 + (c & 7)) * 128 + ((g ^ (c & 6)) * 16), HW = rb + WPART + 32 * hw;
    f32x4 hK = z4, hV = z4;
    const u32 ek = lds0 + XCH + (g * 8 + (c & 7)) * 8, ev = lds0 + XCH + 1280 + (lane & 31) * 8;
    WoState wo;
    const __amdgpu_buffer_rsrc_t rsO = mkrs((hw & 2) ? wob : woa, 576 * 1152);
    { const u32 woc = (u32)(ct * 128 + ((sp8 ^ (2 * ((r8 >> 1) & 3))) * 16));
      const u32 voR = (u32)r8 * 1152u + woc, voT = (u32)(64 + 72 * (r8 >> 2) + (r8 & 3)) * 1152u + woc;
#pragma unroll
      for (int m = 0; m < 4; ++m) { wo.vr[m] = voR + m * (9216 - 1024); wo.vt[m] = voT + (144 * (m & 1) + 4 * (m >> 1)) * 1152 - m * 1024; PIN(wo.vr[m]); PIN(wo.vt[m]); }
      wo.dst = lds0 + hw * 4096; wo.kk = rot2; wo.base = (hw & 1) ? 288 * 1152 : 0; }
    if (wid < 7) {
      const int a = hw;
      u32 vA = voE ^ (u32)((a & 1) * 64), vB = vA ^ 64u;
      u32 v5 = a == 0 ? vB + 15u * 18432u : (a == 1 ? voH : 0x80000000u);
      const u32 xso = a * 18432, xd = lds0 + a * 1024, xd5 = lds0 + (a == 0 ? 15 : 16) * 1024;
      u32 vw[9];
      const u32 wso = a * 1152 + hq * 144, wd = lds0 + XSTG + a * WPART;
#pragma unroll
      for (int i = 0; i < 9; ++i) { vw[i] = wvoff(64 * i + lane); PIN(vw[i]); }
      PIN(vA); PIN(vB); PIN(v5); PIN(HB); PIN(HW);
      u32x4 xh = {0u, 0u, 0u, 0u}, fk = xh, fv = xh;
      int st = s0, cs = 0;
      STAMP(2);
#pragma unroll 1
      for (int it = 0; it < 9; ++it) {
        const int sn = nxt9(st), sl = nxtslot(cs);
#pragma unroll
        for (int i = 5; i < 9; ++i) dma16w(rsW, vw[i], st * (64 * 3456) + wso, wd + cs + i * 1024);
        if (it < 8) {
          dma16w(rsX, vA, sn * 128 + xso, xd + sl); dma16w(rsX, vB, sn * 128 + xso + 3 * 18432, xd + sl + 3072); dma16w(rsX, vA, sn * 128 + xso + 6 * 18432, xd + sl + 6144);
        }
        hK = mfma(fk, xh, hK); hV = mfma(xh, fv, hV);
        if (it < 8) wait_vm<7>(); else wait_vm<4>();
        if (it == 0) STAMP(3);
        bar();
        if (it < 6) STAMP(19 + it);
        xh = lrd128(HB + cs); fk = trd2(HW + cs, WROW); fv = trd2(HW + cs + WPART, WROW);
        if (it < 8) {
#pragma unroll
          for (int i = 0; i < 5; ++i) dma16w(rsW, vw[i], sn * (64 * 3456) + wso, wd + sl + i * 1024);
          dma16w(rsX, vB, sn * 128 + xso + 9 * 18432, xd + sl + 9216); dma16w(rsX, vA, sn * 128 + xso + 12 * 18432, xd + sl + 12288);
          dma16w(rsX, v5, sn * 128, a == 2 ? lds0 + 3 * STG : xd5 + sl);
        } else wo_blocks(rsO, wo, 0, WOA);
        hK = mfma(fk, xh, hK); hV = mfma(xh, fv, hV);
        if (it < 8) wait_vm<11>(); else wait_vm<4 * WOA>();
        bar();
        xh = lrd128((HB ^ 64u) + cs); fk = trd2(HW + cs + 32 * WROW, WROW); fv = trd2(HW + cs + WPART + 32 * WROW, WROW);
        cs = sl; st = sn;
      }
      hK = mfma(fk, xh, hK); hV = mfma(xh, fv, hV);
      if (c >= 8) lwr64f(ek + hw * 256, hK);
      if (g >= 2) lwr64f(ev + hw * 256, hV);
    } else {
      const u32x2 inb0 = __builtin_amdgcn_raw_buffer_load_b64(rsS, INB_OFF + (u32)(sg * 64 + mi) * 512u + lane * 8u, 0, AUX_SC1);
      const u32 non0 = __builtin_amdgcn_raw_buffer_load_b32(rsS, NON_OFF + (u32)(sg * 64 + mi) * 4u, 0, AUX_SC1);
      const u32 BM = rb + (p >= 2 ? WPART + 112 : 128), BV = rb + 2 * WPART + 128;
      const u32 XG = lds0 + (16 * (hg >> 1) + c) * 128 + ((g ^ (c >> 1)) * 16), GG = lds0 + GWL + g * 16;
      f32x4 hM = z4, hV4 = z4, hG = z4;
      STAMP(2);
      wait_vm<2>();
      STAMP(3);
      int cs = 0, st = s0;
#pragma unroll 1
      for (int it = 0; it < 9; ++it) {
#pragma unroll
        for (int k2 = 0; k2 < 2; ++k2) {
          bar();
          const u32 ko = cs + k2 * 32 * WROW;
          const u32x4 xh = lrd128((HB ^ (u32)(64 * k2)) + cs), fk = trd2(HW + ko, WROW), fv = trd2(HW + ko + WPART, WROW), fm = trd2(BM + ko, WROW), f4 = trd2(BV + ko, WROW);
          const u32x4 xg = lrd128((XG ^ (u32)(64 * k2)) + cs), gg = lrd128(GG + st * 128 + k2 * 64);
          hK = mfma(fk, xh, hK); hV = mfma(xh, fv, hV); hM = mfma(fm, xh, hM); hV4 = mfma(xh, f4, hV4); hG = mfma(gg, xg, hG);
          if (k2 == 0 && it == 0) {
            nonce = __builtin_amdgcn_readfirstlane(non0) + 1u;
            while (__ballot(inb0[0] == nonce) != 0ull) ++nonce;
            if (lane == 0) __builtin_amdgcn_raw_buffer_store_b32(nonce, rsS, NON_OFF + (u32)(sg * 64 + mi) * 4u, 0, AUX_SC1);
          }
          if (k2 == 0 && it == 8) {
            vn = __builtin_amdgcn_raw_buffer_load_b32(rsS, NON_OFF + (u32)(sg * 64 + lane) * 4u, 0, AUX_SC1);
            wo_blocks(rsO, wo, 0, WOA);
          }
        }
        cs = nxtslot(cs); st = nxt9(st);
      }
      if (c >= 8) { lwr64f(ek + 3 * 256, hK); if (g >= 2) { lwr64f(ek + 4 * 256, hM); lwr64f(ek + 4 * 256 - 128, hM); } }
      if (g >= 2) { lwr64f(ev + 3 * 256, hV); lwr64f(ev + 4 * 256, hV4); }
      const float gt = __builtin_amdgcn_rcpf(1.f + __builtin_amdgcn_exp2f(-1.4426950408889634f * hG[0]));
      if (r8 == (hg & 1)) __builtin_amdgcn_raw_buffer_store_b32(__float_as_uint(gt), rsS, GATE_OFF + (u32)(cp * 128 + 16 * (hg >> 1) + lane) * 4u, 0, AUX_SC1);
    }
    wo_blocks(rsO, wo, WOA, WOB - WOA);
    lgkm0();
    STAMP(5);
    bar();
    wo_blocks(rsO, wo, WOB, 9 - WOB);
    __builtin_amdgcn_s_setprio(0);
  } else {
    const int w = wid;
    u32 B = rb, BM = rb + (p >= 2 ? WPART + 112 : 128), XB[2];
    XB[0] = lds0 + (32 * w + c) * 128 + ((g ^ (c >> 1)) * 16); XB[1] = XB[0] ^ 64;
    f32x4 bias[2]; u32 mq[5], mP0, mP1;
    {
      const float NINF = -__builtin_inff();
      const bool first = (chunk == 0 && w == 0);
#pragma unroll
      for (int r = 0; r < 4; ++r) {
        const int i = 4 * g + r;
        const bool okc = (i <= c && i >= c - 7), okp = i >= c + 9;
        bias[0][r] = (okc || (okp && !first)) ? 0.f : NINF; bias[1][r] = (okc || okp) ? 0.f : NINF;
      }
      mq[0] = ~0u; mq[1] = g == 0 ? ~0u : 0u; mq[2] = ~mq[1]; mq[3] = g <= 1 ? ~0u : 0u; mq[4] = ~mq[3];
      mP0 = c < 8 ? mq[4] : 0u; mP1 = ~mP0;
      PIN(bias[0]); PIN(bias[1]); PIN(mq[1]); PIN(mq[2]); PIN(mq[3]); PIN(mq[4]); PIN(mP0); PIN(mP1);
    }
    u32 ek = lds0 + XCH + w * XCB + (g * 8 + (c & 7)) * 8, ev = lds0 + XCH + w * XCB + 1280 + (lane & 31) * 8;
    PIN(ek); PIN(ev);
    u32 so[2], st[2]; u32x4 ones = {0x3f803f80u, 0x3f803f80u, 0x3f803f80u, 0x3f803f80u};
    { const u32 ob = (u32)((cp * 8 + 2 * w) * 36 + 9 * (hg >> 2)) * 1024u;
#pragma unroll
      for (int t = 0; t < 2; ++t) { so[t] = lane * 16u + ob + t * 36864u + (hg & 3) * 2048u; st[t] = ((c * 16u + g * 8u) | (mq[4] & 0x80000000u)) + ob + t * 36864u + 8192u + (hg & 3) * 256u; PIN(so[t]); PIN(st[t]); }
      PIN(ones); }
    f32x4 acc[2][14];
#pragma unroll
    for (int t = 0; t < 2; ++t)
#pragma unroll
      for (int i = 0; i < 14; ++i) { acc[t][i] = z4; PIN(acc[t][i]); }
    PIN(B); PIN(BM); PIN(XB[0]); PIN(XB[1]);
    __builtin_amdgcn_s_waitcnt(0xC07F);
    u32x2 wlo[7], whi[7]; u32x4 xf[2][2];
    auto faddr = [&](auto NC) -> u32 {
      constexpr int n = decltype(NC)::value; constexpr int f = n % 14, ko = (n / 14) * 32 * WROW;
      if constexpr (f < 4) return B + ko + WPART + 32 * f;
      else if constexpr (f == 4) return BM + ko;
      else if constexpr (f < 10) return B + ko + 2 * WPART + 32 * (f - 5);
      else return B + ko + 32 * (f - 10);
    };
    u32x10 Kp[3]; u32 Qp[2][10]; u32x6 Vp[5];
    auto pack = [&](auto FC, auto TC) {
      constexpr int f = decltype(FC)::value, t = decltype(TC)::value;
      u32 a0 = pk(acc[t][f][0], acc[t][f][1]), a1 = pk(acc[t][f][2], acc[t][f][3]);
      asm volatile("" : "+v"(a0), "+v"(a1));
      if constexpr (f < 4) { Kp[t + 1][2 * f] = a0; Kp[t + 1][2 * f + 1] = a1; }
      else if constexpr (f == 4) {
        auto r0 = __builtin_amdgcn_permlane32_swap(a0, a0, false, false); auto r1 = __builtin_amdgcn_permlane32_swap(a1, a1, false, false);
        Qp[t][8] = r0[0]; Qp[t][9] = r1[0]; Kp[t + 1][8] = r0[1]; Kp[t + 1][9] = r1[1];
      }
      else if constexpr (f < 10) { Vp[f - 5][2 * t + 2] = a0; Vp[f - 5][2 * t + 3] = a1; }
      else { Qp[t][2 * (f - 10)] = a0; Qp[t][2 * (f - 10) + 1] = a1; }
    };
    wait_vm<0>();
    STAMP(2);
    bar();
    STAMP(3);
    xf[0][0] = lrd128(XB[0]); xf[0][1] = lrd128(XB[0] + 2048);
    sfor<0, PD>([&](auto NC) { constexpr int n = decltype(NC)::value; const u32 a = faddr(NC); wlo[n % 7] = trd(a); whi[n % 7] = trd(a + WROW); SBAR(); });
    auto stage = [&](auto LC, int d) {
      constexpr bool LAST = decltype(LC)::value;
      sfor<0, 28>([&](auto NC) {
        constexpr int n = decltype(NC)::value;
        constexpr int f = n % 14, k2 = n / 14, nn = (n + PD) % 28, kn = nn / 14;
        constexpr bool RD = !(LAST && n + PD >= 28), BI = LAST && k2 == 1;
        constexpr int fx = f - 2;
        if constexpr ((nn == 0 && !LAST) || nn == 14) { SBAR(); bar(); SBAR(); }
        const u32x4 wv = {wlo[n % 7][0], wlo[n % 7][1], whi[n % 7][0], whi[n % 7][1]};
        const u32 a = faddr(IC<nn>{});
        if constexpr (f >= 5 && f < 10) mfma_acc<BI>(acc[0][f], xf[k2][0], wv); else mfma_acc<BI>(acc[0][f], wv, xf[k2][0]);
        SBAR();
        if constexpr (RD) wlo[nn % 7] = trd(a);
        if constexpr (BI && fx >= 0) pack(IC<(fx >= 0 ? fx : 0)>{}, IC<1>{});
        SBAR();
        if constexpr (f >= 5 && f < 10) mfma_acc<BI>(acc[1][f], xf[k2][1], wv); else mfma_acc<BI>(acc[1][f], wv, xf[k2][1]);
        SBAR();
        if constexpr (RD) whi[nn % 7] = trd(a + WROW);
        if constexpr (BI && !RD) pack(IC<2 * (f - 8)>{}, IC<0>{});
        if constexpr (!LAST) {
          if constexpr (nn == 27) { B += d; PIN(B); }
          if constexpr (nn == 19) { BM += d; PIN(BM); }
          if constexpr (nn == 7) { XB[0] += d; PIN(XB[0]); }
          if constexpr (nn == 20) { XB[1] += d; PIN(XB[1]); }
        }
        if constexpr (RD) {
          if constexpr (nn % 14 == 1) xf[kn][0] = lrd128(XB[kn]);
          if constexpr (nn % 14 == 3) xf[kn][1] = lrd128(XB[kn] + 2048);
        }
        SBAR();
      });
    };
    int sl = 0;
#pragma unroll 1
    for (int it = 0; it < 8; ++it) { const int sn = nxtslot(sl), d = sn - sl; sl = sn; stage(IC<0>{}, d); }
    STAMP(4);
    stage(IC<1>{}, 0);
    static_assert(PD == 6, "pack schedule");
    if (w < 3) {
      if (c >= 8) {
#pragma unroll
        for (int i = 0; i < 5; ++i) lwr64(ek + XCB + i * 256, Kp[2][2 * i], Kp[2][2 * i + 1]);
      }
      if (g >= 2) {
#pragma unroll
        for (int i = 0; i < 5; ++i) lwr64(ev + XCB + i * 256, Vp[i][4], Vp[i][5]);
      }
    }
    SBAR();
    pack(IC<12>{}, IC<1>{}); pack(IC<13>{}, IC<1>{});
    sfor<0, 6>([&](auto J) { pack(IC<2 * decltype(J)::value + 1>{}, IC<0>{}); });
    pack(IC<12>{}, IC<0>{}); pack(IC<13>{}, IC<0>{});
    lgkm0();
    STAMP(5);
    bar();
    STAMP(6);
#pragma unroll
    for (int i = 0; i < 5; ++i) {
      const u32x2 a = lrd64(ek + i * 256), v = lrd64(ev + i * 256);
      Kp[0][2 * i] = a[0]; Kp[0][2 * i + 1] = a[1]; Vp[i][0] = v[0]; Vp[i][1] = v[1];
    }
    PIN(Kp[1]); PIN(Kp[2]);
    SBAR();
    const bool selp = (c < 8) && (g >= 2);
    f32x4 sp[2][4], sc[2][4]; float mx[2][4];
    sfor<0, 8>([&](auto IC_) {
      constexpr int t = 1 - (decltype(IC_)::value >> 2), h = decltype(IC_)::value & 3;
      if constexpr (t == 0 && h == 0) PIN(Kp[0]);
      constexpr int ia0 = h == 0 ? 0 : (h == 3 ? 4 : 2), ia1 = h <= 1 ? 0 : 2, ib0 = h <= 1 ? 1 : 3, ib1 = h == 0 ? -1 : (h == 3 ? 3 : 1);
      u32x4 qo;
      qo[0] = Qp[t][2 * h] & mq[ia0]; qo[1] = Qp[t][2 * h + 1] & mq[ia1]; qo[2] = Qp[t][2 * h + 2] & mq[ib0];
      if constexpr (ib1 >= 0) qo[3] = Qp[t][2 * h + 3] & mq[ib1 >= 0 ? ib1 : 0]; else qo[3] = 0u;
      const u32x4 k0 = __builtin_shufflevector(Kp[t], Kp[t], 2 * h, 2 * h + 1, 2 * h + 2, 2 * h + 3);
      const u32x4 k1 = __builtin_shufflevector(Kp[t + 1], Kp[t + 1], 2 * h, 2 * h + 1, 2 * h + 2, 2 * h + 3);
      sp[t][h] = mfma(k0, qo, bias[t]); sc[t][h] = mfma(k1, qo, bias[t]);
    });
    __builtin_amdgcn_sched_group_barrier(0x2, 4, 0);
#pragma unroll
    for (int i = 0; i < 16; ++i) { __builtin_amdgcn_sched_group_barrier(0x8, 1, 0); __builtin_amdgcn_sched_group_barrier(0x2, 2, 0); }
    SBAR();
    if (selp) {
#pragma unroll
      for (int t = 1; t >= 0; --t)
#pragma unroll
        for (int h = 0; h < 4; ++h) { sc[t][h] = sp[t][h]; PIN(sc[t][h]); }
    }
    SBAR();
#pragma unroll
    for (int t = 0; t < 2; ++t)
#pragma unroll
      for (int h = 0; h < 4; ++h) mx[t][h] = fmx(fmx(fmx(sc[t][h][0], sc[t][h][1]), sc[t][h][2]), sc[t][h][3]);
#pragma unroll
    for (int t = 0; t < 2; ++t) {
      float* m = mx[t];
      sw32(m[0], m[1]); float m01 = fmx(m[0], m[1]); sw32(m[2], m[3]); float m23 = fmx(m[2], m[3]);
      sw16(m01, m23); float R = fmx(m01, m23) * -CEXP;
      float a = R, bb = R; sw16(a, bb); m[0] = a; m[1] = a; sw32(m[0], m[1]); m[2] = bb; m[3] = bb; sw32(m[2], m[3]);
    }
#pragma unroll
    for (int i = 0; i < 5; ++i) PIN(Vp[i]);
    SBAR();
    STAMP(18);
    f32x4 olo[2][4], ohi[2][4], sm[2][4];
    sfor<0, 8>([&](auto IC_) {
      constexpr int t = decltype(IC_)::value >> 2, h = decltype(IC_)::value & 3;
      const f32x2 cc = {CEXP, CEXP}, mm = {mx[t][h], mx[t][h]};
      const f32x2 e0 = __builtin_elementwise_fma((f32x2){sc[t][h][0], sc[t][h][1]}, cc, mm), e1 = __builtin_elementwise_fma((f32x2){sc[t][h][2], sc[t][h][3]}, cc, mm);
      const u32 p0 = pk(__builtin_amdgcn_exp2f(e0[0]), __builtin_amdgcn_exp2f(e0[1])), p1 = pk(__builtin_amdgcn_exp2f(e1[0]), __builtin_amdgcn_exp2f(e1[1]));
      const u32x4 po = {p0 & mP0, p1 & mP0, p0 & mP1, p1 & mP1};
      sm[t][h] = mfma(ones, po, z4);
      olo[t][h] = mfma(__builtin_shufflevector(Vp[h], Vp[h], 2 * t, 2 * t + 1, 2 * t + 2, 2 * t + 3), po, z4);
      ohi[t][h] = mfma(__builtin_shufflevector(Vp[h + 1], Vp[h + 1], 2 * t, 2 * t + 1, 2 * t + 2, 2 * t + 3), po, z4);
    });
    SBAR();
    f32x2 ov[2][10]; float inv[2][4];
#pragma unroll
    for (int t = 0; t < 2; ++t) {
#pragma unroll
      for (int h = 0; h < 4; ++h) inv[t][h] = __builtin_amdgcn_rcpf(sm[t][h][0]);
#pragma unroll
      for (int h = 0; h < 4; ++h) { ov[t][2 * h] = (f32x2){olo[t][h][0], olo[t][h][1]} * inv[t][h]; ov[t][2 * h + 1] = (f32x2){olo[t][h][2], olo[t][h][3]} * inv[t][h]; }
      ov[t][8] = (f32x2){ohi[t][3][0], ohi[t][3][1]} * inv[t][3]; ov[t][9] = (f32x2){ohi[t][3][2], ohi[t][3][3]} * inv[t][3];
    }
    if (g < 2) {
#pragma unroll
      for (int t = 0; t < 2; ++t) { ov[t][6] = (f32x2){ohi[t][2][0], ohi[t][2][1]} * inv[t][2]; PIN(ov[t][6]); }
      if (g == 0) {
#pragma unroll
        for (int t = 0; t < 2; ++t) {
          ov[t][2] = (f32x2){ohi[t][0][0], ohi[t][0][1]} * inv[t][0]; PIN(ov[t][2]);
          ov[t][4] = (f32x2){ohi[t][1][0], ohi[t][1][1]} * inv[t][1]; PIN(ov[t][4]);
          ov[t][5] = (f32x2){ohi[t][1][2], ohi[t][1][3]} * inv[t][1]; PIN(ov[t][5]);
          ov[t][7] = (f32x2){ohi[t][2][2], ohi[t][2][3]} * inv[t][2]; PIN(ov[t][7]);
        }
      }
    }
    STAMP(17);
#pragma unroll
    for (int t = 0; t < 2; ++t) {
      u32 op[10];
#pragma unroll
      for (int i = 0; i < 10; ++i) op[i] = pk(ov[t][i][0], ov[t][i][1]);
      __builtin_amdgcn_raw_buffer_store_b128((u32x4){op[0], op[1], op[2], op[3]}, rsS, so[t], 0, AUX_SC1);
      __builtin_amdgcn_raw_buffer_store_b128((u32x4){op[4], op[5], op[6], op[7]}, rsS, so[t] + 1024u, 0, AUX_SC1);
      __builtin_amdgcn_raw_buffer_store_b64((u32x2){op[8], op[9]}, rsS, st[t], 0, AUX_SC1);
    }
  }
  const int kq = wid & 3, th = wid >> 2;
  u32 WB[4];
  { const int pos = 4 * g + q, s = (pos >> 1) & 3;
#pragma unroll
    for (int i = 0; i < 4; ++i) { WB[i] = lds0 + (kq + (th ? 20 : 0)) * 4096 + pos * 128 + (((i ^ (2 * (kq & 1))) ^ s) * 32) + p * 8; PIN(WB[i]); } }
  u32 myin = INB_OFF + (u32)(sg * 64 + mi) * 512u + lane * 8u, dst = INB_OFF + (u32)(sg * 64 + lane) * 512u + mi * 8u;
  PIN(myin); PIN(dst);
  const int T0 = 32 * sg + 4 * rg + 2 * th + ((kq >> 1) ^ (FLIP & ct & 1)), TX = 32 * sg + 28 + rg;
  u32 l16 = lane * 16u; PIN(l16);
  u32 so_k, so_b[3];
  { int r = rot2 + (th ? 5 : 0); r = r >= 9 ? r - 9 : r; so_k = r * 1024u;
    so_b[0] = rg < 4 ? (u32)(TX * 36 + 9 * kq) * 1024u : 0xf0000000u;
    so_b[1] = (u32)((T0 ^ 1) * 36 + 9 * kq) * 1024u; so_b[2] = (u32)(T0 * 36 + 9 * kq) * 1024u;
    asm volatile("" : "+s"(so_k), "+s"(so_b[0]), "+s"(so_b[1]), "+s"(so_b[2])); }
  u32 RW[3];
#pragma unroll
  for (int j = 1; j < 4; ++j) { RW[j - 1] = lds0 + 32768 + ((th * 4 + (kq ^ j)) * 6 + (j - 1) * 2) * 1024 + lane * 16; PIN(RW[j - 1]); }
  STAMP(7);
  wait_vm<0>();
  STAMP(8);
  bar();
  STAMP(9);
  if (wid == 7) __builtin_amdgcn_raw_buffer_store_b64((u32x2){vn, nonce}, rsS, dst, 0, AUX_SC1);
  *(LAS u32*)(uintptr_t)(lds0 + CNT + wid * 4) = 0u;
  u32x4 wf[9][4];
  sfor<0, 9>([&](auto KC) { constexpr int k = decltype(KC)::value;
    if constexpr (k == 4) {
      const u32 d = th ? (u32)(4 - 9) * 16384u : 4u * 16384u;
#pragma unroll
      for (int i = 0; i < 4; ++i) { WB[i] += d; PIN(WB[i]); }
    }
    if constexpr (k == 8) {
#pragma unroll
      for (int i = 0; i < 4; ++i) { WB[i] += 4u * 16384u; PIN(WB[i]); }
    }
#pragma unroll
    for (int i = 0; i < 4; ++i) wf[k][i] = trd2(WB[i] + (k & 3) * 16384, 2048); });
  SBAR();
  bool allok = true;
  if (wid == 7) {
    const int cl = (lane >> 2) & 3;
    const u64 need = __ballot(cl == (rg >> 1) || (rg < 4 && cl == 3));
    u64 got = 0;
#pragma unroll 1
    for (int spin = 0; spin < (1 << 17); ++spin) {
      const u32x2 v = __builtin_amdgcn_raw_buffer_load_b64(rsS, myin, 0, AUX_SC1);
      const bool ok = v[0] == nonce;
      if (ok && v[1] != vn) { vn = v[1]; __builtin_amdgcn_raw_buffer_store_b64((u32x2){vn, nonce}, rsS, dst, 0, AUX_SC1); }
      got = __ballot(ok);
      if ((got & need) == need) break;
      __builtin_amdgcn_s_sleep(1);
    }
    allok = got == ~0ull;
  }
  lgkm0();
  STAMP(10);
  bar();
  STAMP(11);
  if (rg < 7) {
    constexpr int OW = OWN, NL = 23;
    u32x4 of[OW + 1];
    const u32 so_k0 = so_k;
    auto ldo = [&](auto NC) { constexpr int n = decltype(NC)::value; constexpr int ph = ord_ph(n), k = ord_k(n);
      if constexpr (k == 0) so_k = so_k0;
      of[n % (OW + 1)] = __builtin_amdgcn_raw_buffer_load_b128(rsS, l16, (ph == 0 && k == 4 && th) ? 0xf0000000u : so_b[ph] + so_k, AUX_SC1);
      if constexpr (n + 1 < NL && ord_k(n + 1) == k + 1) so_k = (so_k == 8192u) ? 0u : so_k + 1024u; };
    const int L = 32 * (wid & 1) + (lane >> 1);
    float gm = 0.f, gx = 0.f;
    auto ldg = [&]() {
      gm = __uint_as_float(__builtin_amdgcn_raw_buffer_load_b32(rsS, c * 4u, GATE_OFF + (u32)T0 * 64u, AUX_SC1));
      gx = __uint_as_float(__builtin_amdgcn_raw_buffer_load_b32(rsS, (L & 15) * 4u, GATE_OFF + (u32)(TX & 127) * 64u, AUX_SC1)); };
    if (GL == 0) ldg();
    sfor<0, OW>([&](auto NC) { ldo(NC); SBAR(); });
    f32x4 ac[3][4], T[2];
    const __amdgpu_buffer_rsrc_t rsD = mkrs(out, 2048 * 1152);
    const int row = 2 * (128 * (T0 >> 4) + 16 * (T0 & 7) + c) + ((T0 >> 3) & 1);
    u32 po = (u32)(row * 576 + ct * 64 + (kq & 1) * 32 + (g & 1) * 16 + (g >> 1) * 8) * 2u;
    const int rowx = 2 * (128 * (TX >> 4) + 16 * (TX & 7) + (L & 15)) + ((TX >> 3) & 1);
    u32 px = (u32)(rowx * 576 + ct * 64 + (wid >> 1) * 16 + (L >> 4) * 4 + (lane & 1) * 2) * 2u;
    u32 ra = lds0 + 32768 + wid * 6144 + l16, rx = lds0 + wid * 512 + lane * 8, wx = (lds0 + wid * 4096 + l16) ^ (u32)((kq & 1) * 2048), wx2 = wx ^ 2048u;
    u32 fw = lds0 + CNT + wid * 4, fg = lds0 + CNT + th * 16;
    PIN(po); PIN(px); PIN(ra); PIN(rx); PIN(wx); PIN(wx2); PIN(fw); PIN(fg);
    auto flag = [&](u32 v) { asm volatile("" ::: "memory"); *(volatile LAS u32*)(uintptr_t)fw = v; };
    float fm = 0.f;
    sfor<0, NL>([&](auto NC) {
      constexpr int n = decltype(NC)::value; constexpr int ph = ord_ph(n), k = ord_k(n);
      if constexpr (n + OW < NL) ldo(IC<n + OW>{});
      if constexpr (GL > 0 && n == GL) ldg();
      SBAR();
#pragma unroll
      for (int ii = 0; ii < 4; ++ii) { const int i = ii ^ 2; ac[ph][i] = mfma(wf[k][i], of[n % (OW + 1)], k == 0 ? z4 : ac[ph][i]); }
      SBAR();
      if constexpr (ORD < 2) {
        if constexpr (n >= 10 && n < 14) lwr128(((n - 10) & 2 ? wx2 : wx) + ((n - 10) & 1) * 1024, ac[0][n - 10]);
        if constexpr (n >= 14 && n < 16) { lwr128(RW[n - 13], ac[1][2 * (n - 14)]); lwr128(RW[n - 13] + 1024, ac[1][2 * (n - 14) + 1]); }
      } else {
        if constexpr (n >= 10 && n < 14) lwr128(RW[1 + ((n - 10) >> 1)] + ((n - 10) & 1) * 1024, ac[1][n - 10]);
        if constexpr (n >= 14 && n < 16) { lwr128((n == 14 ? wx : wx2), ac[0][2 * (n - 14)]); lwr128((n == 14 ? wx : wx2) + 1024, ac[0][2 * (n - 14) + 1]); }
      }
      if constexpr (n == 15) flag(2u);
      if constexpr (n == MID) {
#pragma unroll 1
        for (;;) {
          const u32x4 f = *(volatile LAS u32x4*)(uintptr_t)fg;
          asm volatile("" ::: "memory");
#pragma unroll
          for (int t = 0; t < 2; ++t) T[t] = *(LAS f32x4*)(uintptr_t)(ra + 2048 + t * 1024) + *(LAS f32x4*)(uintptr_t)(ra + 4096 + t * 1024);
          asm volatile("" ::: "memory");
          if (__builtin_amdgcn_readfirstlane(f[0] & f[1] & f[2] & f[3]) & 2u) break;
        }
        fm = (kq >> 1) ? 1.f - gm : gm;
        PIN(T[0]); PIN(T[1]); PIN(fm);
      }
      SBAR();
    });
    STAMP(12);
    lwr128(RW[0], ac[2][2]); lwr128(RW[0] + 1024, ac[2][3]);
    flag(3u);
    f32x4 D[2];
#pragma unroll
    for (int t = 0; t < 2; ++t) { D[t] = (ac[2][t] - T[t]) * fm + T[t]; PIN(D[t]); }
    {
      f32x4 r[2];
#pragma unroll 1
      for (;;) {
        const u32 f = *(volatile LAS u32*)(uintptr_t)(fw ^ 4u);
        asm volatile("" ::: "memory");
#pragma unroll
        for (int t = 0; t < 2; ++t) r[t] = *(LAS f32x4*)(uintptr_t)(ra + t * 1024);
        asm volatile("" ::: "memory");
        if (__builtin_amdgcn_readfirstlane(f) == 3u) break;
      }
      STAMP(13);
      f32x4 v[2];
#pragma unroll
      for (int t = 0; t < 2; ++t) v[t] = r[t] * fm + D[t];
      auto s0_ = __builtin_amdgcn_permlane16_swap(pk(v[0][0], v[0][1]), pk(v[1][0], v[1][1]), false, false);
      auto s1_ = __builtin_amdgcn_permlane16_swap(pk(v[0][2], v[0][3]), pk(v[1][2], v[1][3]), false, false);
      __builtin_amdgcn_raw_buffer_store_b128((u32x4){s0_[0], s1_[0], s0_[1], s1_[1]}, rsD, po, 0, 0);
    }
    if (rg < 4 && (XALL || th == 0)) {
#pragma unroll
      for (int hf = 0; hf < (XALL ? 1 : 2); ++hf) {
        f32x2 e[8];
#pragma unroll 1
        for (;;) {
          const u32x4 f = *(volatile LAS u32x4*)(uintptr_t)(fg ^ 16u);
          asm volatile("" ::: "memory");
#pragma unroll
          for (int j = 0; j < 8; ++j) e[j] = *(LAS f32x2*)(uintptr_t)(rx + j * 4096 + hf * 2048);
          asm volatile("" ::: "memory");
          if (__builtin_amdgcn_readfirstlane(f[0] & f[1] & f[2] & f[3]) & 2u) break;
        }
        const f32x2 A = (e[0] + e[1]) + (e[4] + e[5]), Bv = (e[2] + e[3]) + (e[6] + e[7]);
        const f32x2 r = (A - Bv) * gx + Bv;
        __builtin_amdgcn_raw_buffer_store_b32(pk(r[0], r[1]), rsD, px + hf * 64, 0, 0);
      }
    }
  }
  if (!allok) {
#pragma unroll 1
    for (int spin = 0; spin < (1 << 17); ++spin) {
      const u32x2 v = __builtin_amdgcn_raw_buffer_load_b64(rsS, myin, 0, AUX_SC1);
      const bool ok = v[0] == nonce;
      if (ok && v[1] != vn) { vn = v[1]; __builtin_amdgcn_raw_buffer_store_b64((u32x2){vn, nonce}, rsS, dst, 0, AUX_SC1); }
      if (__ballot(ok) == ~0ull) break;
      __builtin_amdgcn_s_sleep(1);
    }
  }
  STAMP(14);
  STAMP(15); RSTAMP(16);
  STAMP_OUT();
}

extern "C" void kernel_launch(void* const* d_in, const int* in_sizes, int n_in, void* d_out, int out_size, void* d_ws, size_t ws_size, hipStream_t stream) {
  const u16* x = (const u16*)d_in[0]; const u16* wqa = (const u16*)d_in[1]; const u16* woa = (const u16*)d_in[2];
  const u16* wqb = (const u16*)d_in[4]; const u16* wob = (const u16*)d_in[5]; const u16* gw = (const u16*)d_in[6];
  (void)hipFuncSetAttribute((const void*)fused, hipFuncAttributeMaxDynamicSharedMemorySize, LDS_BYTES);
  fused<<<256, 512, LDS_BYTES, stream>>>(x, wqa, woa, wqb, wob, gw, (u16*)d_out, (char*)d_ws);
}
```

```cpp
#include <hip/hip_runtime.h>
typedef unsigned short u16; typedef unsigned int u32; typedef unsigned long long u64;
typedef __attribute__((ext_vector_type(8))) __bf16 bf16x8;
typedef __attribute__((ext_vector_type(2))) __bf16 bf16x2;
typedef __attribute__((ext_vector_type(4))) float f32x4;
typedef __attribute__((ext_vector_type(2))) float f32x2;
typedef __attribute__((ext_vector_type(4))) short s16x4;
typedef __attribute__((ext_vector_type(4))) u32 u32x4;
typedef __attribute__((ext_vector_type(2))) u32 u32x2;
typedef __attribute__((ext_vector_type(6))) u32 u32x6;
typedef __attribute__((ext_vector_type(10))) u32 u32x10;
#define LAS __attribute__((address_space(3)))
#define DI __device__ __forceinline__
#define SBAR() __builtin_amdgcn_sched_barrier(0)
#define PIN(x) asm volatile("" : "+v"(x))
template <int V> struct IC { static constexpr int value = V; };
template <int A, int B, class F> __device__ __forceinline__ void sfor(F&& f) { if constexpr (A < B) { f(IC<A>{}); sfor<A + 1, B>(f); } }
#define ROT 7
#define ROT2 1
#define PD 6
#define PRE 4
#define DIAG 0
#define MID 19
#define XALL 0
#define WOA 1
#define WOB 3
#define FLIP 0
#define DEPTH 2

constexpr int WROW = 144, WPART = 9232, XSTG = 136 * 128, STG = 45184;
constexpr int LDS_BYTES = 163840;
constexpr int XCH = 147456, XCB = 2560, GWL = XCH + 4 * XCB, CNT = GWL + 2048, DUM = CNT + 256;
constexpr u32 OP_BYTES = 128u * 36u * 1024u;
constexpr u32 GATE_OFF = OP_BYTES;
constexpr u32 NON_OFF = GATE_OFF + 8192;
constexpr u32 INB_OFF = NON_OFF + 1024;
constexpr u32 WS_BYTES = INB_OFF + 256 * 512;
constexpr float CEXP = 0.23570226039551584f * 1.4426950408889634f;
constexpr int AUX_SC1 = 16;
constexpr u32 CODE_BYTES = 16640;

DI u32 pk(float a, float b) { f32x2 v = {a, b}; bf16x2 r = __builtin_convertvector(v, bf16x2); return __builtin_bit_cast(u32, r); }
DI f32x4 mfma(u32x4 a, u32x4 b, f32x4 c) { return __builtin_amdgcn_mfma_f32_16x16x32_bf16(__builtin_bit_cast(bf16x8, a), __builtin_bit_cast(bf16x8, b), c, 0, 0, 0); }
template <bool BI> DI void mfma_acc(f32x4& c, u32x4 a, u32x4 b) {
  if constexpr (BI) c = mfma(a, b, c);
  else asm volatile("v_mfma_f32_16x16x32_bf16 %0, %1, %2, %0" : "+v"(c) : "v"(a), "v"(b));
}
DI u32x2 trd(u32 a) { return __builtin_bit_cast(u32x2, __builtin_amdgcn_ds_read_tr16_b64_v4i16((LAS s16x4*)(uintptr_t)a)); }
DI u32x4 trd2(u32 a, int st) { const u32x2 x = trd(a), y = trd(a + st); return (u32x4){x[0], x[1], y[0], y[1]}; }
DI u32x4 lrd128(u32 a) { return *(LAS u32x4*)(uintptr_t)a; }
DI u32x2 lrd64(u32 a) { return *(LAS u32x2*)(uintptr_t)a; }
DI void lwr64(u32 a, u32 x, u32 y) { *(LAS u32x2*)(uintptr_t)a = (u32x2){x, y}; }
DI void lwr64f(u32 a, f32x4 v) { lwr64(a, pk(v[0], v[1]), pk(v[2], v[3])); }
DI void lwr128(u32 a, f32x4 v) { *(LAS f32x4*)(uintptr_t)a = v; }
#define NT 0
#define NT0 ""
#define NT1 ""
DI void dma16(__amdgpu_buffer_rsrc_t rs, u32 voff, u32 soff, u32 ldsdst) {
  asm volatile("s_mov_b32 m0, %0\n\ts_nop 0\n\tbuffer_load_dwordx4 %1, %2, %3 offen" NT0 " lds" :: "s"(ldsdst), "v"(voff), "s"(rs), "s"(soff) : "memory");
}
#define WIN 9
#define WINO 6
template <int WN = WIN> DI void dma16w(__amdgpu_buffer_rsrc_t rs, u32 voff, u32 soff, u32 ldsdst) {
  asm volatile("s_mov_b32 m0, %0\n\ts_waitcnt vmcnt(%4)\n\tbuffer_load_dwordx4 %1, %2, %3 offen" NT1 " lds" :: "s"(ldsdst), "v"(voff), "s"(rs), "s"(soff), "i"(WN) : "memory");
}
template <int N> DI void wait_vm() { asm volatile("s_waitcnt vmcnt(%0)" :: "i"(N) : "memory"); }
DI void bar() { asm volatile("s_barrier" ::: "memory"); }
DI void lgkm0() { asm volatile("s_waitcnt lgkmcnt(0)" ::: "memory"); }
#define STAMP(i)
#define RSTAMP(i)
#define STAMP_DECL
#define STAMP_OUT()
DI float dot2(u32 a, u32 b, float c) { return __builtin_amdgcn_fdot2_f32_bf16(__builtin_bit_cast(bf16x2, a), __builtin_bit_cast(bf16x2, b), c, false); }
DI float fmx(float a, float b) { return __builtin_elementwise_maximum(a, b); }
DI void sw32(float& a, float& b) { auto r = __builtin_amdgcn_permlane32_swap(__float_as_uint(a), __float_as_uint(b), false, false); a = __uint_as_float(r[0]); b = __uint_as_float(r[1]); }
DI void sw16(float& a, float& b) { auto r = __builtin_amdgcn_permlane16_swap(__float_as_uint(a), __float_as_uint(b), false, false); a = __uint_as_float(r[0]); b = __uint_as_float(r[1]); }
DI int nxt9(int s) { return s == 8 ? 0 : s + 1; }
DI int nxtslot(int s) { return s == 2 * STG ? 0 : s + STG; }
DI __amdgpu_buffer_rsrc_t mkrs(const void* p, u32 n) { return __builtin_amdgcn_make_buffer_rsrc((void*)p, 0, n, 0x00020000); }
DI u32 wvoff(u32 ci) {
  const u32 pos = __umul24(ci, 7282u) >> 16, ch = ci - __umul24(pos, 9u);
  const u32 row = (pos & ~7u) | ((pos & 1u) << 2) | ((pos & 7u) >> 1);
  return __umul24(row, 3456u) + ch * 16u;
}

struct WoState { u32 vr[4], vt[4]; u32 dst; int kk; u32 base; };
DI void wo_blocks(__amdgpu_buffer_rsrc_t rs, WoState w, int v0, int n) {
  w.dst += v0 * 16384; w.kk += v0; w.kk = w.kk >= 9 ? w.kk - 9 : w.kk;
#pragma unroll 1
  for (int i = 0; i < n; ++i) {
    if (w.kk < 8) {
      const u32 so = w.base + (u32)(36 * w.kk - 4 * (w.kk & 1)) * 1152u;
      asm volatile("s_mov_b32 m0, %0\n\ts_waitcnt vmcnt(%7)\n\tbuffer_load_dwordx4 %1, %5, %6 offen lds\n\tbuffer_load_dwordx4 %2, %5, %6 offen offset:1024 lds\n\t"
                   "buffer_load_dwordx4 %3, %5, %6 offen offset:2048 lds\n\tbuffer_load_dwordx4 %4, %5, %6 offen offset:3072 lds"
                   :: "s"(w.dst), "v"(w.vr[0]), "v"(w.vr[1]), "v"(w.vr[2]), "v"(w.vr[3]), "s"(rs), "s"(so), "i"(WINO) : "memory");
      ++w.kk;
    } else {
      asm volatile("s_mov_b32 m0, %0\n\ts_waitcnt vmcnt(%7)\n\tbuffer_load_dwordx4 %1, %5, %6 offen lds\n\tbuffer_load_dwordx4 %2, %5, %6 offen offset:1024 lds\n\t"
                   "buffer_load_dwordx4 %3, %5, %6 offen offset:2048 lds\n\tbuffer_load_dwordx4 %4, %5, %6 offen offset:3072 lds"
                   :: "s"(w.dst), "v"(w.vt[0]), "v"(w.vt[1]), "v"(w.vt[2]), "v"(w.vt[3]), "s"(rs), "s"(w.base), "i"(WINO) : "memory");
      w.kk = 0;
    }
    w.dst += 16384;
  }
}

#define FINJ 4
#define ORD 0
#define OWN 6
#define GL 8
constexpr int ord_ph(int n) { return n >= 14 ? 2 : (ORD == 0 ? (n < 5 ? 0 : 1) : ORD == 1 ? (n < 10 ? 1 - (n & 1) : 1) : (n < 9 ? 1 : 0)); }
constexpr int ord_k(int n) { return n >= 14 ? n - 14 : (ORD == 0 ? (n < 5 ? n : n - 5) : ORD == 1 ? (n < 10 ? n >> 1 : n - 5) : (n < 9 ? n : n - 9)); }
__global__ __launch_bounds__(512, 2) void fused(const u16* __restrict__ x, const u16* __restrict__ wqa, const u16* __restrict__ woa,
                                                const u16* __restrict__ wqb, const u16* __restrict__ wob, const u16* __restrict__ gw,
                                                u16* __restrict__ out, char* __restrict__ ws) {
  extern __shared__ __attribute__((aligned(16))) char lds[];
  const int tid = threadIdx.x, lane = tid & 63;
  const int wid = __builtin_amdgcn_readfirstlane(tid >> 6);
  STAMP_DECL; RSTAMP(0); STAMP(1);
  const int b = blockIdx.x, xcd = b & 7, bi = b >> 3;
  const int cp = 8 * (xcd & 1) + (bi & 7), hg = 4 * (xcd >> 1) + (bi >> 3);
  const int par = cp & 1, chunk = cp >> 1;
  const int hq = hg & 7;
  int s0 = 0;
  if (ROT == 1) { s0 = (bi & 7) + 2 * (bi >> 3); s0 = s0 >= 9 ? s0 - 9 : s0; }
  if (ROT == 2) s0 = 2 * (((bi & 7) + (bi >> 3)) & 3);
  if (ROT == 7) s0 = 2 * (((bi & 7) + (bi >> 3)) & 3) + (((xcd >> 1) + xcd) & 1);
  const int r8 = lane >> 3, sp8 = lane & 7;
  const u32 lds0 = (u32)(uintptr_t)(LAS char*)lds;
  {
    unsigned long long pc; asm volatile("s_getpc_b64 %0" : "=s"(pc));
    if (wid == 7 && lane < 5) {
      const unsigned long long pa = (pc & ~255ull) + min((unsigned)(bi * 5 + lane) * 128u, CODE_BYTES - 128u);
      asm volatile("s_mov_b32 m0, %0\n\ts_nop 0\n\tglobal_load_lds_dword %1, off" :: "s"(lds0 + 3 * STG + 1024), "v"(pa) : "memory");
    }
  }
  u32 voE = (u32)(2 * (128 * chunk + r8) + par) * 1152u + (u32)(sp8 ^ (r8 >> 1)) * 16u;
  int jh = 128 * chunk - 8 + r8; jh = jh < 0 ? 0 : jh;
  u32 voH = (u32)(2 * jh + par) * 1152u + (u32)(sp8 ^ (r8 & 6)) * 16u;
  const int u0 = 2 * wid, p0 = (u0 * 52) >> 8, i0 = u0 - 5 * p0, u1 = u0 + 1, p1 = (u1 * 52) >> 8, i1 = u1 - 5 * p1;
  u32 voO = voE ^ 64u, vw0 = wvoff(64 * i0 + lane), vw1 = wvoff(64 * i1 + lane);
  u32 sx = s0 * 128 + wid * 36864, dx = lds0 + wid * 2048;
  u32 sw0 = s0 * (64 * 3456) + p0 * 1152 + hq * 144, dw0 = lds0 + XSTG + p0 * WPART + i0 * 1024;
  u32 sw1 = s0 * (64 * 3456) + p1 * 1152 + hq * 144, dw1 = lds0 + XSTG + p1 * WPART + i1 * 1024;
  asm volatile("" : "+v"(voE), "+v"(voH), "+v"(voO), "+v"(vw0), "+v"(vw1), "+s"(sx), "+s"(dx), "+s"(sw0), "+s"(dw0), "+s"(sw1), "+s"(dw1));
  SBAR();
  const __amdgpu_buffer_rsrc_t rsX = mkrs(x, 2048 * 1152);
  const __amdgpu_buffer_rsrc_t rsW = mkrs((hg >> 3) ? wqb : wqa, 576 * 3456);
  {
    dma16(rsX, voE, sx, dx);
    dma16(rsX, voO, sx + 18432, dx + 1024);
    dma16(rsW, vw0, sw0, dw0);
    if (wid < 7) dma16(rsW, vw1, sw1, dw1);
    else {
      dma16(rsX, voH, s0 * 128, lds0 + 16384);
      const __amdgpu_buffer_rsrc_t rsG = mkrs(gw, 1152);
      dma16(rsG, lane * 16u, 0, lds0 + GWL); dma16(rsG, lane * 16u, 1024, lds0 + GWL + 1024);
    }
  }
  SBAR();
  STAMP(25);
  asm volatile("" :: "s"(woa), "s"(wob), "s"(gw), "s"(out), "s"(ws));
  const int sg = cp >> 2, mi = 16 * (hg >> 2) + 4 * (cp & 3) + (hg & 3);
  const int rg = (mi * 57) >> 9, ct = mi - 9 * rg;
  int rot2 = ROT2 ? ct + 2 * (rg & 1) + 4 * (sg & 1) : 0; rot2 = rot2 >= 9 ? rot2 - 9 : rot2;
  const int c = lane & 15, g = lane >> 4, q = (lane >> 2) & 3, p = lane & 3;
  const __amdgpu_buffer_rsrc_t rsS = mkrs(ws, WS_BYTES);
  const f32x4 z4 = {0.f, 0.f, 0.f, 0.f};
  u32 nonce = 0, vn = 0;
  const u32 rb = lds0 + XSTG + (8 * g + 2 * q) * WROW + 8 * p;

  if (wid >= 4) {
    __builtin_amdgcn_s_setprio(2);
    const int hw = wid - 4;
    u32 HB = lds0 + (128 + (c & 7)) * 128 + ((g ^ (c & 6)) * 16), HW = rb + WPART + 32 * hw;
    f32x4 hK = z4, hV = z4;
    const u32 ek = lds0 + XCH + (g * 8 + (c & 7)) * 8, ev = lds0 + XCH + 1280 + (lane & 31) * 8;
    WoState wo;
    const __amdgpu_buffer_rsrc_t rsO = mkrs((hw & 2) ? wob : woa, DIAG == 9 ? 0 : 576 * 1152);
    { const u32 woc = (u32)(ct * 128 + ((sp8 ^ (2 * ((r8 >> 1) & 3))) * 16));
      const u32 voR = (u32)r8 * 1152u + woc, voT = (u32)(64 + 72 * (r8 >> 2) + (r8 & 3)) * 1152u + woc;
#pragma unroll
      for (int m = 0; m < 4; ++m) { wo.vr[m] = voR + m * (9216 - 1024); wo.vt[m] = voT + (144 * (m & 1) + 4 * (m >> 1)) * 1152 - m * 1024; PIN(wo.vr[m]); PIN(wo.vt[m]); }
      wo.dst = lds0 + hw * 4096; wo.kk = rot2; wo.base = (hw & 1) ? 288 * 1152 : 0; }
    if (wid < 7) {
      const int a = hw;
      u32 vA = voE ^ (u32)((a & 1) * 64), vB = vA ^ 64u;
      u32 v5 = a == 0 ? vB + 15u * 18432u : (a == 1 ? voH : 0x80000000u);
      const u32 xso = a * 18432, xd = lds0 + a * 1024, xd5 = lds0 + (a == 0 ? 15 : 16) * 1024;
      u32 vw[9];
      const u32 wso = a * 1152 + hq * 144, wd = lds0 + XSTG + a * WPART;
#pragma unroll
      for (int i = 0; i < 9; ++i) { vw[i] = wvoff(64 * i + lane); PIN(vw[i]); }
      PIN(vA); PIN(vB); PIN(v5); PIN(HB); PIN(HW);
      u32x4 xh = {0u, 0u, 0u, 0u}, fk = xh, fv = xh;
      int st = s0, cs = 0;
      STAMP(2);
#pragma unroll 1
      for (int it = 0; it < 9; ++it) {
        const int sn = nxt9(st), sl = nxtslot(cs);
#pragma unroll
        for (int i = 5; i < 9; ++i) dma16w(rsW, vw[i], st * (64 * 3456) + wso, wd + cs + i * 1024);
        if (it < 8) {
          dma16w(rsX, vA, sn * 128 + xso, xd + sl); dma16w(rsX, vB, sn * 128 + xso + 3 * 18432, xd + sl + 3072); dma16w(rsX, vA, sn * 128 + xso + 6 * 18432, xd + sl + 6144);
        }
        hK = mfma(fk, xh, hK); hV = mfma(xh, fv, hV);
        if (it < 8) wait_vm<7>(); else wait_vm<4>();
        if (it == 0) STAMP(3);
        bar();
        if (it < 6) STAMP(19 + it);
        xh = lrd128(HB + cs); fk = trd2(HW + cs, WROW); fv = trd2(HW + cs + WPART, WROW);
        if (it < 8) {
#pragma unroll
          for (int i = 0; i < 5; ++i) dma16w(rsW, vw[i], sn * (64 * 3456) + wso, wd + sl + i * 1024);
          dma16w(rsX, vB, sn * 128 + xso + 9 * 18432, xd + sl + 9216); dma16w(rsX, vA, sn * 128 + xso + 12 * 18432, xd + sl + 12288);
          dma16w(rsX, v5, sn * 128, a == 2 ? lds0 + 3 * STG : xd5 + sl);
        } else wo_blocks(rsO, wo, 0, WOA);
        hK = mfma(fk, xh, hK); hV = mfma(xh, fv, hV);
        if (it < 8) wait_vm<11>(); else wait_vm<4 * WOA>();
        bar();
        xh = lrd128((HB ^ 64u) + cs); fk = trd2(HW + cs + 32 * WROW, WROW); fv = trd2(HW + cs + WPART + 32 * WROW, WROW);
        cs = sl; st = sn;
      }
      hK = mfma(fk, xh, hK); hV = mfma(xh, fv, hV);
      if (c >= 8) lwr64f(ek + hw * 256, hK);
      if (g >= 2) lwr64f(ev + hw * 256, hV);
    } else {
      const u32x2 inb0 = __builtin_amdgcn_raw_buffer_load_b64(rsS, INB_OFF + (u32)(sg * 64 + mi) * 512u + lane * 8u, 0, AUX_SC1);
      const u32 non0 = __builtin_amdgcn_raw_buffer_load_b32(rsS, NON_OFF + (u32)(sg * 64 + mi) * 4u, 0, AUX_SC1);
      const u32 BM = rb + (p >= 2 ? WPART + 112 : 128), BV = rb + 2 * WPART + 128;
      const u32 XG = lds0 + (16 * (hg >> 1) + c) * 128 + ((g ^ (c >> 1)) * 16), GG = lds0 + GWL + g * 16;
      f32x4 hM = z4, hV4 = z4, hG = z4;
      STAMP(2);
      wait_vm<2>();
      STAMP(3);
      int cs = 0, st = s0;
#pragma unroll 1
      for (int it = 0; it < 9; ++it) {
#pragma unroll
        for (int k2 = 0; k2 < 2; ++k2) {
          bar();
          const u32 ko = cs + k2 * 32 * WROW;
          const u32x4 xh = lrd128((HB ^ (u32)(64 * k2)) + cs), fk = trd2(HW + ko, WROW), fv = trd2(HW + ko + WPART, WROW), fm = trd2(BM + ko, WROW), f4 = trd2(BV + ko, WROW);
          const u32x4 xg = lrd128((XG ^ (u32)(64 * k2)) + cs), gg = lrd128(GG + st * 128 + k2 * 64);
          hK = mfma(fk, xh, hK); hV = mfma(xh, fv, hV); hM = mfma(fm, xh, hM); hV4 = mfma(xh, f4, hV4); hG = mfma(gg, xg, hG);
          if (k2 == 0 && it == 0) {
            nonce = __builtin_amdgcn_readfirstlane(non0) + 1u;
            while (__ballot(inb0[0] == nonce) != 0ull) ++nonce;
            if (lane == 0) __builtin_amdgcn_raw_buffer_store_b32(nonce, rsS, NON_OFF + (u32)(sg * 64 + mi) * 4u, 0, AUX_SC1);
          }
          if (k2 == 0 && it == 8) {
            vn = __builtin_amdgcn_raw_buffer_load_b32(rsS, NON_OFF + (u32)(sg * 64 + lane) * 4u, 0, AUX_SC1);
            wo_blocks(rsO, wo, 0, WOA);
          }
        }
        cs = nxtslot(cs); st = nxt9(st);
      }
      if (c >= 8) { lwr64f(ek + 3 * 256, hK); if (g >= 2) { lwr64f(ek + 4 * 256, hM); lwr64f(ek + 4 * 256 - 128, hM); } }
      if (g >= 2) { lwr64f(ev + 3 * 256, hV); lwr64f(ev + 4 * 256, hV4); }
      const float gt = __builtin_amdgcn_rcpf(1.f + __builtin_amdgcn_exp2f(-1.4426950408889634f * hG[0]));
      if (r8 == (hg & 1)) __builtin_amdgcn_raw_buffer_store_b32(__float_as_uint(gt), rsS, GATE_OFF + (u32)(cp * 128 + 16 * (hg >> 1) + lane) * 4u, 0, AUX_SC1);
    }
    wo_blocks(rsO, wo, WOA, WOB - WOA);
    lgkm0();
    STAMP(5);
    bar();
    wo_blocks(rsO, wo, WOB, 9 - WOB);
    __builtin_amdgcn_s_setprio(0);
  } else {
    const int w = wid;
    u32 B = rb, BM = rb + (p >= 2 ? WPART + 112 : 128), XB[2];
    XB[0] = lds0 + (32 * w + c) * 128 + ((g ^ (c >> 1)) * 16); XB[1] = XB[0] ^ 64;
    f32x4 bias[2]; u32 mq[5], mP0, mP1;
    {
      const float NINF = -__builtin_inff();
      const bool first = (chunk == 0 && w == 0);
#pragma unroll
      for (int r = 0; r < 4; ++r) {
        const int i = 4 * g + r;
        const bool okc = (i <= c && i >= c - 7), okp = i >= c + 9;
        bias[0][r] = (okc || (okp && !first)) ? 0.f : NINF; bias[1][r] = (okc || okp) ? 0.f : NINF;
      }
      mq[0] = ~0u; mq[1] = g == 0 ? ~0u : 0u; mq[2] = ~mq[1]; mq[3] = g <= 1 ? ~0u : 0u; mq[4] = ~mq[3];
      mP0 = c < 8 ? mq[4] : 0u; mP1 = ~mP0;
      PIN(bias[0]); PIN(bias[1]); PIN(mq[1]); PIN(mq[2]); PIN(mq[3]); PIN(mq[4]); PIN(mP0); PIN(mP1);
    }
    u32 ek = lds0 + XCH + w * XCB + (g * 8 + (c & 7)) * 8, ev = lds0 + XCH + w * XCB + 1280 + (lane & 31) * 8;
    u32 wk = (w < 3 && c >= 8) ? ek + XCB : lds0 + DUM + (lane & 31) * 8, wv_ = (w < 3 && g >= 2) ? ev + XCB : lds0 + DUM + (lane & 31) * 8;
    PIN(ek); PIN(ev); PIN(wk); PIN(wv_);
    u32 so[2], st[2]; u32x4 ones = {0x3f803f80u, 0x3f803f80u, 0x3f803f80u, 0x3f803f80u};
    { const u32 ob = (u32)((cp * 8 + 2 * w) * 36 + 9 * (hg >> 2)) * 1024u;
#pragma unroll
      for (int t = 0; t < 2; ++t) { so[t] = lane * 16u + ob + t * 36864u + (hg & 3) * 2048u; st[t] = ((c * 16u + g * 8u) | (mq[4] & 0x80000000u)) + ob + t * 36864u + 8192u + (hg & 3) * 256u; PIN(so[t]); PIN(st[t]); }
      PIN(ones); }
    f32x4 acc[2][14];
#pragma unroll
    for (int t = 0; t < 2; ++t)
#pragma unroll
      for (int i = 0; i < 14; ++i) { acc[t][i] = z4; PIN(acc[t][i]); }
    PIN(B); PIN(BM); PIN(XB[0]); PIN(XB[1]);
    __builtin_amdgcn_s_waitcnt(0xC07F);
    u32x2 wlo[7], whi[7]; u32x4 xf[2][2];
    auto faddr = [&](auto NC) -> u32 {
      constexpr int n = decltype(NC)::value; constexpr int f = n % 14, ko = (n / 14) * 32 * WROW;
      if constexpr (f < 4) return B + ko + WPART + 32 * f;
      else if constexpr (f == 4) return BM + ko;
      else if constexpr (f < 10) return B + ko + 2 * WPART + 32 * (f - 5);
      else return B + ko + 32 * (f - 10);
    };
    u32x10 Kp[3]; u32 Qp[2][10]; u32x6 Vp[5];
    u32 zr[4] = {0u, 0u, 0u, 0u};
#pragma unroll
    for (int i = 0; i < 4; ++i) PIN(zr[i]);
    auto pack = [&](auto FC, auto TC) {
      constexpr int f = decltype(FC)::value, t = decltype(TC)::value;
      u32 a0 = pk(acc[t][f][0], acc[t][f][1]), a1 = pk(acc[t][f][2], acc[t][f][3]);
      asm volatile("" : "+v"(a0), "+v"(a1));
      if constexpr (f < 4) { Kp[t + 1][2 * f] = a0; Kp[t + 1][2 * f + 1] = a1; }
      else if constexpr (f == 4) {
        auto r0 = __builtin_amdgcn_permlane32_swap(a0, zr[2 * t], false, false); auto r1 = __builtin_amdgcn_permlane32_swap(a1, zr[2 * t + 1], false, false);
        Qp[t][8] = r0[0]; Qp[t][9] = r1[0]; Kp[t + 1][8] = r0[1]; Kp[t + 1][9] = r1[1];
      }
      else if constexpr (f < 10) { Vp[f - 5][2 * t + 2] = a0; Vp[f - 5][2 * t + 3] = a1; }
      else { Qp[t][2 * (f - 10)] = a0; Qp[t][2 * (f - 10) + 1] = a1; }
    };
    wait_vm<0>();
    STAMP(2);
    bar();
    STAMP(3);
    xf[0][0] = lrd128(XB[0]); xf[0][1] = lrd128(XB[0] + 2048);
    sfor<0, PD>([&](auto NC) { constexpr int n = decltype(NC)::value; const u32 a = faddr(NC); wlo[n % 7] = trd(a); whi[n % 7] = trd(a + WROW); SBAR(); });
    auto stage = [&](auto LC, int d) {
      constexpr bool LAST = decltype(LC)::value;
      sfor<0, 28>([&](auto NC) {
        constexpr int n = decltype(NC)::value;
        constexpr int f = n % 14, k2 = n / 14, nn = (n + PD) % 28, kn = nn / 14;
        constexpr bool RD = !(LAST && n + PD >= 28), BI = LAST && k2 == 1;
        constexpr int fx = f - 2;
        if constexpr ((nn == 0 && !LAST) || nn == 14) { SBAR(); bar(); SBAR(); }
        const u32x4 wv = {wlo[n % 7][0], wlo[n % 7][1], whi[n % 7][0], whi[n % 7][1]};
        const u32 a = faddr(IC<nn>{});
        if constexpr (f >= 5 && f < 10) mfma_acc<BI>(acc[0][f], xf[k2][0], wv); else mfma_acc<BI>(acc[0][f], wv, xf[k2][0]);
        SBAR();
        if constexpr (RD) wlo[nn % 7] = trd(a);
        if constexpr (BI && fx >= 0) pack(IC<(fx >= 0 ? fx : 0)>{}, IC<1>{});
        SBAR();
        if constexpr (f >= 5 && f < 10) mfma_acc<BI>(acc[1][f], xf[k2][1], wv); else mfma_acc<BI>(acc[1][f], wv, xf[k2][1]);
        SBAR();
        if constexpr (RD) whi[nn % 7] = trd(a + WROW);
        if constexpr (BI && !RD) pack(IC<2 * (f - 8)>{}, IC<0>{});
        if constexpr (!LAST) {
          if constexpr (nn == 27) { B += d; PIN(B); }
          if constexpr (nn == 19) { BM += d; PIN(BM); }
          if constexpr (nn == 7) { XB[0] += d; PIN(XB[0]); }
          if constexpr (nn == 20) { XB[1] += d; PIN(XB[1]); }
        }
        if constexpr (RD) {
          if constexpr (nn % 14 == 1) xf[kn][0] = lrd128(XB[kn]);
          if constexpr (nn % 14 == 3) xf[kn][1] = lrd128(XB[kn] + 2048);
        }
        SBAR();
      });
    };
    int sl = 0;
#pragma unroll 1
    for (int it = 0; it < 8; ++it) { const int sn = nxtslot(sl), d = sn - sl; sl = sn; stage(IC<0>{}, d); }
    STAMP(4);
    stage(IC<1>{}, 0);
    static_assert(PD == 6, "pack schedule");
#pragma unroll
    for (int i = 0; i < 5; ++i) lwr64(wk + i * 256, Kp[2][2 * i], Kp[2][2 * i + 1]);
#pragma unroll
    for (int i = 0; i < 5; ++i) lwr64(wv_ + i * 256, Vp[i][4], Vp[i][5]);
    SBAR();
    pack(IC<1>{}, IC<0>{}); pack(IC<3>{}, IC<0>{});
    pack(IC<12>{}, IC<1>{}); pack(IC<13>{}, IC<1>{});
    lgkm0();
    STAMP(5);
    bar();
    STAMP(6);
#pragma unroll
    for (int i = 0; i < 5; ++i) { const u32x2 a = lrd64(ek + i * 256); Kp[0][2 * i] = a[0]; Kp[0][2 * i + 1] = a[1]; }
#pragma unroll
    for (int i = 0; i < 5; ++i) { const u32x2 v = *(volatile LAS u32x2*)(uintptr_t)(ev + i * 256); Vp[i][0] = v[0]; Vp[i][1] = v[1]; }
    PIN(Kp[1]); PIN(Kp[2]);
    SBAR();
    const bool selp = (c < 8) && (g >= 2);
    f32x4 sp[2][4], sc[2][4]; float mx[2][4];
    u32x4 qo[2][4];
    auto mkq = [&](auto TC, auto HC) {
      constexpr int t = decltype(TC)::value, h = decltype(HC)::value;
      constexpr int ia0 = h == 0 ? 0 : (h == 3 ? 4 : 2), ia1 = h <= 1 ? 0 : 2, ib0 = h <= 1 ? 1 : (h == 3 ? 0 : 3), ib1 = h == 0 ? -1 : (h == 3 ? 0 : 1);
      qo[t][h][0] = Qp[t][2 * h] & mq[ia0]; qo[t][h][1] = Qp[t][2 * h + 1] & mq[ia1]; qo[t][h][2] = Qp[t][2 * h + 2] & mq[ib0];
      if constexpr (ib1 >= 0) qo[t][h][3] = Qp[t][2 * h + 3] & mq[ib1 >= 0 ? ib1 : 0]; else qo[t][h][3] = 0u;
      PIN(qo[t][h]);
    };
    mkq(IC<1>{}, IC<0>{});
    SBAR();
    sfor<0, 8>([&](auto IC_) {
      constexpr int j = decltype(IC_)::value, t = 1 - (j >> 2), h = j & 3;
      if constexpr (j == 4) { PIN(Kp[0]); SBAR(); }
      const u32x4 k0 = __builtin_shufflevector(Kp[t], Kp[t], 2 * h, 2 * h + 1, 2 * h + 2, 2 * h + 3);
      const u32x4 k1 = __builtin_shufflevector(Kp[t + 1], Kp[t + 1], 2 * h, 2 * h + 1, 2 * h + 2, 2 * h + 3);
      sc[t][h] = mfma(k1, qo[t][h], bias[t]);
      SBAR();
      if constexpr (j < 7) mkq(IC<1 - ((j + 1) >> 2)>{}, IC<(j + 1) & 3>{});
      SBAR();
      sp[t][h] = mfma(k0, qo[t][h], bias[t]);
      SBAR();
      if constexpr (j < 3) pack(IC<11 + j>{}, IC<0>{});
      else if constexpr (j < 6) pack(IC<5 + 2 * (j - 3)>{}, IC<0>{});
      SBAR();
    });
    if (selp) {
#pragma unroll
      for (int t = 1; t >= 0; --t)
#pragma unroll
        for (int h = 0; h < 4; ++h) { sc[t][h] = sp[t][h]; PIN(sc[t][h]); }
    }
    SBAR();
#pragma unroll
    for (int t = 0; t < 2; ++t)
#pragma unroll
      for (int h = 0; h < 4; ++h) mx[t][h] = fmx(fmx(fmx(sc[t][h][0], sc[t][h][1]), sc[t][h][2]), sc[t][h][3]);
#pragma unroll
    for (int t = 0; t < 2; ++t) {
      float* m = mx[t];
      sw32(m[0], m[1]); float m01 = fmx(m[0], m[1]); sw32(m[2], m[3]); float m23 = fmx(m[2], m[3]);
      sw16(m01, m23); float R = fmx(m01, m23) * -CEXP;
      float a = R, bb = R; sw16(a, bb); m[0] = a; m[1] = a; sw32(m[0], m[1]); m[2] = bb; m[3] = bb; sw32(m[2], m[3]);
    }
    f32x2 ex[2][4][2];
#pragma unroll
    for (int t = 0; t < 2; ++t)
#pragma unroll
      for (int h = 0; h < 4; ++h) {
        const f32x2 cc = {CEXP, CEXP}, mm = {mx[t][h], mx[t][h]};
        ex[t][h][0] = __builtin_elementwise_fma((f32x2){sc[t][h][0], sc[t][h][1]}, cc, mm); ex[t][h][1] = __builtin_elementwise_fma((f32x2){sc[t][h][2], sc[t][h][3]}, cc, mm);
      }
    asm volatile("" : "+v"(Vp[0]), "+v"(Vp[1]), "+v"(Vp[2]), "+v"(Vp[3]), "+v"(Vp[4]));
    SBAR();
    STAMP(18);
    f32x4 olo[2][4], ohi[2][4], sm[2][4];
    float ee[4]; u32 pq[2]; u32x4 po[2], oa[2], ob_[2]; u32x2 oc[2];
    auto eA = [&](auto JC) { constexpr int j = decltype(JC)::value, t = j >> 2, h = j & 3;
      ee[0] = __builtin_amdgcn_exp2f(ex[t][h][0][0]); ee[1] = __builtin_amdgcn_exp2f(ex[t][h][0][1]); ee[2] = __builtin_amdgcn_exp2f(ex[t][h][1][0]);
      asm volatile("" : "+v"(ee[0]), "+v"(ee[1]), "+v"(ee[2])); };
    auto eB = [&](auto JC) { constexpr int j = decltype(JC)::value, t = j >> 2, h = j & 3;
      ee[3] = __builtin_amdgcn_exp2f(ex[t][h][1][1]); pq[0] = pk(ee[0], ee[1]); SBAR(); po[j & 1][0] = pq[0] & mP0; pq[1] = pk(ee[2], ee[3]);
      asm volatile("" : "+v"(pq[0]), "+v"(pq[1])); };
    auto eC = [&](auto JC) { constexpr int j = decltype(JC)::value;
      po[j & 1][2] = pq[0] & mP1; po[j & 1][1] = pq[1] & mP0; po[j & 1][3] = pq[1] & mP1; PIN(po[j & 1]); };
    auto fin = [&](auto TC) {
      constexpr int t = decltype(TC)::value;
      asm volatile("" :: "v"(sm[t][0]), "v"(sm[t][1]), "v"(sm[t][2]), "v"(ohi[t][0]));
      f32x2 ov[10]; float iv[4];
#pragma unroll
      for (int h = 0; h < 4; ++h) iv[h] = __builtin_amdgcn_rcpf(sm[t][h][0]);
#pragma unroll
      for (int h = 0; h < 4; ++h) { ov[2 * h] = (f32x2){olo[t][h][0], olo[t][h][1]} * iv[h]; ov[2 * h + 1] = (f32x2){olo[t][h][2], olo[t][h][3]} * iv[h]; }
      ov[8] = (f32x2){ohi[t][3][0], ohi[t][3][1]} * iv[3]; ov[9] = (f32x2){ohi[t][3][2], ohi[t][3][3]} * iv[3];
#pragma unroll
      for (int i = 0; i < 10; ++i) PIN(ov[i]);
      SBAR();
      if (g < 2) {
        PIN(iv[2]); ov[6] = (f32x2){ohi[t][2][0], ohi[t][2][1]} * iv[2]; PIN(ov[6]);
        if (g == 0) {
          PIN(iv[0]); PIN(iv[1]); PIN(iv[2]);
          ov[2] = (f32x2){ohi[t][0][0], ohi[t][0][1]} * iv[0]; PIN(ov[2]);
          ov[4] = (f32x2){ohi[t][1][0], ohi[t][1][1]} * iv[1]; PIN(ov[4]);
          ov[5] = (f32x2){ohi[t][1][2], ohi[t][1][3]} * iv[1]; PIN(ov[5]);
          ov[7] = (f32x2){ohi[t][2][2], ohi[t][2][3]} * iv[2]; PIN(ov[7]);
        }
      }
      oa[t] = (u32x4){pk(ov[0][0], ov[0][1]), pk(ov[1][0], ov[1][1]), pk(ov[2][0], ov[2][1]), pk(ov[3][0], ov[3][1])};
      __builtin_amdgcn_raw_buffer_store_b128(oa[t], rsS, so[t], 0, AUX_SC1);
      SBAR();
      ob_[t] = (u32x4){pk(ov[4][0], ov[4][1]), pk(ov[5][0], ov[5][1]), pk(ov[6][0], ov[6][1]), pk(ov[7][0], ov[7][1])};
      __builtin_amdgcn_raw_buffer_store_b128(ob_[t], rsS, so[t] + 1024u, 0, AUX_SC1);
      SBAR();
      oc[t] = (u32x2){pk(ov[8][0], ov[8][1]), pk(ov[9][0], ov[9][1])};
      __builtin_amdgcn_raw_buffer_store_b64(oc[t], rsS, st[t], 0, AUX_SC1);
      SBAR();
    };
    eA(IC<0>{}); eB(IC<0>{}); eC(IC<0>{});
    SBAR();
    sfor<0, 8>([&](auto JC) {
      constexpr int j = decltype(JC)::value, t = j >> 2, h = j & 3;
      if constexpr (j < 7) eA(IC<(j < 7 ? j + 1 : 0)>{});
      SBAR();
      sm[t][h] = mfma(ones, po[j & 1], z4);
      SBAR();
      if constexpr (j < 7) eB(IC<(j < 7 ? j + 1 : 0)>{});
      SBAR();
      olo[t][h] = mfma(__builtin_shufflevector(Vp[h], Vp[h], 2 * t, 2 * t + 1, 2 * t + 2, 2 * t + 3), po[j & 1], z4);
      SBAR();
      if constexpr (j < 7) eC(IC<(j < 7 ? j + 1 : 0)>{});
      SBAR();
      ohi[t][h] = mfma(__builtin_shufflevector(Vp[h + 1], Vp[h + 1], 2 * t, 2 * t + 1, 2 * t + 2, 2 * t + 3), po[j & 1], z4);
      SBAR();
      if constexpr (j == FINJ) { asm volatile("" :: "v"(sm[0][3])); fin(IC<0>{}); }
    });
    STAMP(17);
    fin(IC<1>{});
    asm volatile("" :: "v"(oa[0]), "v"(ob_[0]), "v"(oc[0]), "v"(oa[1]), "v"(ob_[1]));
    STAMP(26);
    STAMP(27);
  }
  const int kq = wid & 3, th = wid >> 2;
  u32 WB[4];
  { const int pos = 4 * g + q, s = (pos >> 1) & 3;
#pragma unroll
    for (int i = 0; i < 4; ++i) { WB[i] = lds0 + (kq + (th ? 20 : 0)) * 4096 + pos * 128 + (((i ^ (2 * (kq & 1))) ^ s) * 32) + p * 8; PIN(WB[i]); } }
  u32 myin = INB_OFF + (u32)(sg * 64 + mi) * 512u + lane * 8u, dst = INB_OFF + (u32)(sg * 64 + lane) * 512u + mi * 8u;
  PIN(myin); PIN(dst);
  const int T0 = 32 * sg + 4 * rg + 2 * th + ((kq >> 1) ^ (FLIP & ct & 1)), TX = 32 * sg + 28 + rg;
  u32 l16 = lane * 16u; PIN(l16);
  u32 so_k, so_b[3];
  { int r = rot2 + (th ? 5 : 0); r = r >= 9 ? r - 9 : r; so_k = r * 1024u;
    so_b[0] = rg < 4 ? (u32)(TX * 36 + 9 * kq) * 1024u : 0xf0000000u;
    so_b[1] = (u32)((T0 ^ 1) * 36 + 9 * kq) * 1024u; so_b[2] = (u32)(T0 * 36 + 9 * kq) * 1024u;
    asm volatile("" : "+s"(so_k), "+s"(so_b[0]), "+s"(so_b[1]), "+s"(so_b[2])); }
  u32 RW[3];
#pragma unroll
  for (int j = 1; j < 4; ++j) { RW[j - 1] = lds0 + 32768 + ((th * 4 + (kq ^ j)) * 6 + (j - 1) * 2) * 1024 + lane * 16; PIN(RW[j - 1]); }
  STAMP(7);
  wait_vm<0>();
  STAMP(8);
  bar();
  STAMP(9);
  if (wid == 7) __builtin_amdgcn_raw_buffer_store_b64((u32x2){vn, nonce}, rsS, dst, 0, AUX_SC1);
  *(LAS u32*)(uintptr_t)(lds0 + CNT + wid * 4) = 0u;
  u32x4 wf[9][4];
  sfor<0, 9>([&](auto KC) { constexpr int k = decltype(KC)::value;
    if constexpr (k == 4) {
      const u32 d = th ? (u32)(4 - 9) * 16384u : 4u * 16384u;
#pragma unroll
      for (int i = 0; i < 4; ++i) { WB[i] += d; PIN(WB[i]); }
    }
    if constexpr (k == 8) {
#pragma unroll
      for (int i = 0; i < 4; ++i) { WB[i] += 4u * 16384u; PIN(WB[i]); }
    }
#pragma unroll
    for (int i = 0; i < 4; ++i) wf[k][i] = trd2(WB[i] + (k & 3) * 16384, 2048); });
  SBAR();
  bool allok = true;
  if (wid == 7) {
    const int cl = (lane >> 2) & 3;
    const u64 need = __ballot(cl == (rg >> 1) || (rg < 4 && cl == 3));
    u64 got = 0;
#pragma unroll 1
    for (int spin = 0; spin < (1 << 17); ++spin) {
      const u32x2 v = __builtin_amdgcn_raw_buffer_load_b64(rsS, myin, 0, AUX_SC1);
      const bool ok = v[0] == nonce;
      if (ok && v[1] != vn) { vn = v[1]; __builtin_amdgcn_raw_buffer_store_b64((u32x2){vn, nonce}, rsS, dst, 0, AUX_SC1); }
      got = __ballot(ok);
      if ((got & need) == need) break;
      __builtin_amdgcn_s_sleep(1);
    }
    allok = got == ~0ull;
  }
  lgkm0();
  STAMP(10);
  bar();
  STAMP(11);
  if (rg < 7) {
    constexpr int OW = OWN, NL = 23;
    u32x4 of[OW + 1];
    const u32 so_k0 = so_k;
    auto ldo = [&](auto NC) { constexpr int n = decltype(NC)::value; constexpr int ph = ord_ph(n), k = ord_k(n);
      if constexpr (k == 0) so_k = so_k0;
      of[n % (OW + 1)] = __builtin_amdgcn_raw_buffer_load_b128(rsS, l16, (ph == 0 && k == 4 && th) ? 0xf0000000u : so_b[ph] + so_k, AUX_SC1);
      if constexpr (n + 1 < NL && ord_k(n + 1) == k + 1) so_k = (so_k == 8192u) ? 0u : so_k + 1024u; };
    const int L = 32 * (wid & 1) + (lane >> 1);
    float gm = 0.f, gx = 0.f;
    auto ldg = [&]() {
      gm = __uint_as_float(__builtin_amdgcn_raw_buffer_load_b32(rsS, c * 4u, GATE_OFF + (u32)T0 * 64u, AUX_SC1));
      gx = __uint_as_float(__builtin_amdgcn_raw_buffer_load_b32(rsS, (L & 15) * 4u, GATE_OFF + (u32)(TX & 127) * 64u, AUX_SC1)); };
    if (GL == 0) ldg();
    sfor<0, OW>([&](auto NC) { ldo(NC); SBAR(); });
    f32x4 ac[3][4], T[2];
    const __amdgpu_buffer_rsrc_t rsD = mkrs(out, 2048 * 1152);
    const int row = 2 * (128 * (T0 >> 4) + 16 * (T0 & 7) + c) + ((T0 >> 3) & 1);
    u32 po = (u32)(row * 576 + ct * 64 + (kq & 1) * 32 + (g & 1) * 16 + (g >> 1) * 8) * 2u;
    const int rowx = 2 * (128 * (TX >> 4) + 16 * (TX & 7) + (L & 15)) + ((TX >> 3) & 1);
    u32 px = (u32)(rowx * 576 + ct * 64 + (wid >> 1) * 16 + (L >> 4) * 4 + (lane & 1) * 2) * 2u;
    u32 ra = lds0 + 32768 + wid * 6144 + l16, rx = lds0 + wid * 512 + lane * 8, wx = (lds0 + wid * 4096 + l16) ^ (u32)((kq & 1) * 2048), wx2 = wx ^ 2048u;
    u32 fw = lds0 + CNT + wid * 4, fg = lds0 + CNT + th * 16;
    PIN(po); PIN(px); PIN(ra); PIN(rx); PIN(wx); PIN(wx2); PIN(fw); PIN(fg);
    auto flag = [&](u32 v) { asm volatile("" ::: "memory"); *(volatile LAS u32*)(uintptr_t)fw = v; };
    float fm = 0.f;
    sfor<0, NL>([&](auto NC) {
      constexpr int n = decltype(NC)::value; constexpr int ph = ord_ph(n), k = ord_k(n);
      if constexpr (n + OW < NL) ldo(IC<n + OW>{});
      if constexpr (GL > 0 && n == GL) ldg();
      SBAR();
#pragma unroll
      for (int ii = 0; ii < 4; ++ii) { const int i = ii ^ 2; ac[ph][i] = mfma(wf[k][i], of[n % (OW + 1)], k == 0 ? z4 : ac[ph][i]); }
      SBAR();
      if constexpr (ORD < 2) {
        if constexpr (n >= 10 && n < 14) lwr128(((n - 10) & 2 ? wx2 : wx) + ((n - 10) & 1) * 1024, ac[0][n - 10]);
        if constexpr (n >= 14 && n < 16) { lwr128(RW[n - 13], ac[1][2 * (n - 14)]); lwr128(RW[n - 13] + 1024, ac[1][2 * (n - 14) + 1]); }
      } else {
        if constexpr (n >= 10 && n < 14) lwr128(RW[1 + ((n - 10) >> 1)] + ((n - 10) & 1) * 1024, ac[1][n - 10]);
        if constexpr (n >= 14 && n < 16) { lwr128((n == 14 ? wx : wx2), ac[0][2 * (n - 14)]); lwr128((n == 14 ? wx : wx2) + 1024, ac[0][2 * (n - 14) + 1]); }
      }
      if constexpr (n == 15) flag(2u);
      if constexpr (n == MID) {
#pragma unroll 1
        for (;;) {
          const u32x4 f = *(volatile LAS u32x4*)(uintptr_t)fg;
          asm volatile("" ::: "memory");
#pragma unroll
          for (int t = 0; t < 2; ++t) T[t] = *(LAS f32x4*)(uintptr_t)(ra + 2048 + t * 1024) + *(LAS f32x4*)(uintptr_t)(ra + 4096 + t * 1024);
          asm volatile("" ::: "memory");
          if (__builtin_amdgcn_readfirstlane(f[0] & f[1] & f[2] & f[3]) & 2u) break;
        }
        fm = (kq >> 1) ? 1.f - gm : gm;
        PIN(T[0]); PIN(T[1]); PIN(fm);
      }
      SBAR();
    });
    STAMP(12);
    lwr128(RW[0], ac[2][2]); lwr128(RW[0] + 1024, ac[2][3]);
    flag(3u);
    f32x4 D[2];
#pragma unroll
    for (int t = 0; t < 2; ++t) { D[t] = (ac[2][t] - T[t]) * fm + T[t]; PIN(D[t]); }
    {
      f32x4 r[2];
#pragma unroll 1
      for (;;) {
        const u32 f = *(volatile LAS u32*)(uintptr_t)(fw ^ 4u);
        asm volatile("" ::: "memory");
#pragma unroll
        for (int t = 0; t < 2; ++t) r[t] = *(LAS f32x4*)(uintptr_t)(ra + t * 1024);
        asm volatile("" ::: "memory");
        if (__builtin_amdgcn_readfirstlane(f) == 3u) break;
      }
      STAMP(13);
      f32x4 v[2];
#pragma unroll
      for (int t = 0; t < 2; ++t) v[t] = r[t] * fm + D[t];
      auto s0_ = __builtin_amdgcn_permlane16_swap(pk(v[0][0], v[0][1]), pk(v[1][0], v[1][1]), false, false);
      auto s1_ = __builtin_amdgcn_permlane16_swap(pk(v[0][2], v[0][3]), pk(v[1][2], v[1][3]), false, false);
      __builtin_amdgcn_raw_buffer_store_b128((u32x4){s0_[0], s1_[0], s0_[1], s1_[1]}, rsD, po, 0, 0);
    }
    if (rg < 4 && (XALL || th == 0)) {
#pragma unroll
      for (int hf = 0; hf < (XALL ? 1 : 2); ++hf) {
        f32x2 e[8];
#pragma unroll 1
        for (;;) {
          const u32x4 f = *(volatile LAS u32x4*)(uintptr_t)(fg ^ 16u);
          asm volatile("" ::: "memory");
#pragma unroll
          for (int j = 0; j < 8; ++j) e[j] = *(LAS f32x2*)(uintptr_t)(rx + j * 4096 + hf * 2048);
          asm volatile("" ::: "memory");
          if (__builtin_amdgcn_readfirstlane(f[0] & f[1] & f[2] & f[3]) & 2u) break;
        }
        const f32x2 A = (e[0] + e[1]) + (e[4] + e[5]), Bv = (e[2] + e[3]) + (e[6] + e[7]);
        const f32x2 r = (A - Bv) * gx + Bv;
        __builtin_amdgcn_raw_buffer_store_b32(pk(r[0], r[1]), rsD, px + hf * 64, 0, 0);
      }
    }
  }
  if (!allok) {
#pragma unroll 1
    for (int spin = 0; spin < (1 << 17); ++spin) {
      const u32x2 v = __builtin_amdgcn_raw_buffer_load_b64(rsS, myin, 0, AUX_SC1);
      const bool ok = v[0] == nonce;
      if (ok && v[1] != vn) { vn = v[1]; __builtin_amdgcn_raw_buffer_store_b64((u32x2){vn, nonce}, rsS, dst, 0, AUX_SC1); }
      if (__ballot(ok) == ~0ull) break;
      __builtin_amdgcn_s_sleep(1);
    }
  }
  STAMP(14);
  STAMP(15); RSTAMP(16);
  STAMP_OUT();
}

extern "C" void kernel_launch(void* const* d_in, const int* in_sizes, int n_in, void* d_out, int out_size, void* d_ws, size_t ws_size, hipStream_t stream) {
  const u16* x = (const u16*)d_in[0]; const u16* wqa = (const u16*)d_in[1]; const u16* woa = (const u16*)d_in[2];
  const u16* wqb = (const u16*)d_in[4]; const u16* wob = (const u16*)d_in[5]; const u16* gw = (const u16*)d_in[6];
  (void)hipFuncSetAttribute((const void*)fused, hipFuncAttributeMaxDynamicSharedMemorySize, LDS_BYTES);
  fused<<<256, 512, LDS_BYTES, stream>>>(x, wqa, woa, wqb, wob, gw, (u16*)d_out, (char*)d_ws);
}
```

```cpp
#include <hip/hip_runtime.h>
typedef unsigned short u16; typedef unsigned int u32; typedef unsigned long long u64;
typedef __attribute__((ext_vector_type(8))) __bf16 bf16x8;
typedef __attribute__((ext_vector_type(2))) __bf16 bf16x2;
typedef __attribute__((ext_vector_type(4))) float f32x4;
typedef __attribute__((ext_vector_type(2))) float f32x2;
typedef __attribute__((ext_vector_type(4))) short s16x4;
typedef __attribute__((ext_vector_type(4))) u32 u32x4;
typedef __attribute__((ext_vector_type(2))) u32 u32x2;
typedef __attribute__((ext_vector_type(6))) u32 u32x6;
typedef __attribute__((ext_vector_type(10))) u32 u32x10;
#define LAS __attribute__((address_space(3)))
#define DI __device__ __forceinline__
#define SBAR() __builtin_amdgcn_sched_barrier(0)
#define PIN(x) asm volatile("" : "+v"(x))
template <int V> struct IC { static constexpr int value = V; };
template <int A, int B, class F> __device__ __forceinline__ void sfor(F&& f) { if constexpr (A < B) { f(IC<A>{}); sfor<A + 1, B>(f); } }

constexpr int WROW = 144, WPART = 9232, XSTG = 136 * 128, STG = 45184;
constexpr int LDS_BYTES = 163840;
constexpr int XCH = 147456, XCB = 2560, GWL = XCH + 4 * XCB, CNT = GWL + 2048, DUM = CNT + 256;
constexpr u32 OP_BYTES = 128u * 36u * 1024u;
constexpr u32 GATE_OFF = OP_BYTES;
constexpr u32 NON_OFF = GATE_OFF + 8192;
constexpr u32 INB_OFF = NON_OFF + 1024;
constexpr u32 WS_BYTES = INB_OFF + 256 * 512;
constexpr float CEXP = 0.23570226039551584f * 1.4426950408889634f;
constexpr int AUX_SC1 = 16;
constexpr int PD = 6;
constexpr int WIN = 9;
constexpr int WINO = 6;
constexpr int WOA = 1, WOB = 3;
constexpr int OW = 6, NL = 23, MID = 19, GL = 8;

DI u32 pk(float a, float b) { f32x2 v = {a, b}; bf16x2 r = __builtin_convertvector(v, bf16x2); return __builtin_bit_cast(u32, r); }
DI f32x4 mfma(u32x4 a, u32x4 b, f32x4 c) { return __builtin_amdgcn_mfma_f32_16x16x32_bf16(__builtin_bit_cast(bf16x8, a), __builtin_bit_cast(bf16x8, b), c, 0, 0, 0); }
template <bool BI> DI void mfma_acc(f32x4& c, u32x4 a, u32x4 b) {
  if constexpr (BI) c = mfma(a, b, c);
  else asm volatile("v_mfma_f32_16x16x32_bf16 %0, %1, %2, %0" : "+v"(c) : "v"(a), "v"(b));
}
DI u32x2 trd(u32 a) { return __builtin_bit_cast(u32x2, __builtin_amdgcn_ds_read_tr16_b64_v4i16((LAS s16x4*)(uintptr_t)a)); }
DI u32x4 trd2(u32 a, int st) { const u32x2 x = trd(a), y = trd(a + st); return (u32x4){x[0], x[1], y[0], y[1]}; }
DI u32x4 lrd128(u32 a) { return *(LAS u32x4*)(uintptr_t)a; }
DI u32x2 lrd64(u32 a) { return *(LAS u32x2*)(uintptr_t)a; }
DI void lwr64(u32 a, u32 x, u32 y) { *(LAS u32x2*)(uintptr_t)a = (u32x2){x, y}; }
DI void lwr64f(u32 a, f32x4 v) { lwr64(a, pk(v[0], v[1]), pk(v[2], v[3])); }
DI void lwr128(u32 a, f32x4 v) { *(LAS f32x4*)(uintptr_t)a = v; }
DI void dma16(__amdgpu_buffer_rsrc_t rs, u32 voff, u32 soff, u32 ldsdst) {
  asm volatile("s_mov_b32 m0, %0\n\ts_nop 0\n\tbuffer_load_dwordx4 %1, %2, %3 offen lds" :: "s"(ldsdst), "v"(voff), "s"(rs), "s"(soff) : "memory");
}
DI void dma16w(__amdgpu_buffer_rsrc_t rs, u32 voff, u32 soff, u32 ldsdst) {
  asm volatile("s_mov_b32 m0, %0\n\ts_waitcnt vmcnt(%4)\n\tbuffer_load_dwordx4 %1, %2, %3 offen lds" :: "s"(ldsdst), "v"(voff), "s"(rs), "s"(soff), "i"(WIN) : "memory");
}
template <int N> DI void wait_vm() { asm volatile("s_waitcnt vmcnt(%0)" :: "i"(N) : "memory"); }
DI void bar() { asm volatile("s_barrier" ::: "memory"); }
DI void lgkm0() { asm volatile("s_waitcnt lgkmcnt(0)" ::: "memory"); }
DI float fmx(float a, float b) { return __builtin_elementwise_maximum(a, b); }
DI void sw32(float& a, float& b) { auto r = __builtin_amdgcn_permlane32_swap(__float_as_uint(a), __float_as_uint(b), false, false); a = __uint_as_float(r[0]); b = __uint_as_float(r[1]); }
DI void sw16(float& a, float& b) { auto r = __builtin_amdgcn_permlane16_swap(__float_as_uint(a), __float_as_uint(b), false, false); a = __uint_as_float(r[0]); b = __uint_as_float(r[1]); }
DI int nxt9(int s) { return s == 8 ? 0 : s + 1; }
DI int nxtslot(int s) { return s == 2 * STG ? 0 : s + STG; }
DI __amdgpu_buffer_rsrc_t mkrs(const void* p, u32 n) { return __builtin_amdgcn_make_buffer_rsrc((void*)p, 0, n, 0x00020000); }
DI u32 wvoff(u32 ci) {
  const u32 pos = __umul24(ci, 7282u) >> 16, ch = ci - __umul24(pos, 9u);
  const u32 row = (pos & ~7u) | ((pos & 1u) << 2) | ((pos & 7u) >> 1);
  return __umul24(row, 3456u) + ch * 16u;
}

struct WoState { u32 vr[4], vt[4]; u32 dst; int kk; u32 base; };
DI void wo_blocks(__amdgpu_buffer_rsrc_t rs, WoState w, int v0, int n) {
  w.dst += v0 * 16384; w.kk += v0; w.kk = w.kk >= 9 ? w.kk - 9 : w.kk;
#pragma unroll 1
  for (int i = 0; i < n; ++i) {
    if (w.kk < 8) {
      const u32 so = w.base + (u32)(36 * w.kk - 4 * (w.kk & 1)) * 1152u;
      asm volatile("s_mov_b32 m0, %0\n\ts_waitcnt vmcnt(%7)\n\tbuffer_load_dwordx4 %1, %5, %6 offen lds\n\tbuffer_load_dwordx4 %2, %5, %6 offen offset:1024 lds\n\t"
                   "buffer_load_dwordx4 %3, %5, %6 offen offset:2048 lds\n\tbuffer_load_dwordx4 %4, %5, %6 offen offset:3072 lds"
                   :: "s"(w.dst), "v"(w.vr[0]), "v"(w.vr[1]), "v"(w.vr[2]), "v"(w.vr[3]), "s"(rs), "s"(so), "i"(WINO) : "memory");
      ++w.kk;
    } else {
      asm volatile("s_mov_b32 m0, %0\n\ts_waitcnt vmcnt(%7)\n\tbuffer_load_dwordx4 %1, %5, %6 offen lds\n\tbuffer_load_dwordx4 %2, %5, %6 offen offset:1024 lds\n\t"
                   "buffer_load_dwordx4 %3, %5, %6 offen offset:2048 lds\n\tbuffer_load_dwordx4 %4, %5, %6 offen offset:3072 lds"
                   :: "s"(w.dst), "v"(w.vt[0]), "v"(w.vt[1]), "v"(w.vt[2]), "v"(w.vt[3]), "s"(rs), "s"(w.base), "i"(WINO) : "memory");
      w.kk = 0;
    }
    w.dst += 16384;
  }
}

constexpr int ord_ph(int n) { return n < 5 ? 0 : (n < 14 ? 1 : 2); }
constexpr int ord_k(int n) { return n < 5 ? n : (n < 14 ? n - 5 : n - 14); }

__global__ __launch_bounds__(512, 2) void fused(const u16* __restrict__ x, const u16* __restrict__ wqa, const u16* __restrict__ woa,
                                                const u16* __restrict__ wqb, const u16* __restrict__ wob, const u16* __restrict__ gw,
                                                u16* __restrict__ out, char* __restrict__ ws) {
  extern __shared__ __attribute__((aligned(16))) char lds[];
  const int tid = threadIdx.x, lane = tid & 63;
  const int wid = __builtin_amdgcn_readfirstlane(tid >> 6);
  const int b = blockIdx.x, xcd = b & 7, bi = b >> 3;
  const int cp = 8 * (xcd & 1) + (bi & 7), hg = 4 * (xcd >> 1) + (bi >> 3);
  const int par = cp & 1, chunk = cp >> 1;
  const int hq = hg & 7;
  const int s0 = 2 * (((bi & 7) + (bi >> 3)) & 3) + (((xcd >> 1) + xcd) & 1);
  const int r8 = lane >> 3, sp8 = lane & 7;
  const u32 lds0 = (u32)(uintptr_t)(LAS char*)lds;
  {
    unsigned long long pc; u32 len;
    asm volatile("s_getpc_b64 %0\n.Lgm_pc:\n\ts_mov_b32 %1, .Lgm_end-.Lgm_pc" : "=s"(pc), "=s"(len));
    const u32 off = min((u32)(wid * 32 + bi) * 128u, len & ~3u);
    asm volatile("s_mov_b32 m0, %0\n\ts_nop 0\n\tglobal_load_lds_dword %1, %2" :: "s"(lds0 + 3 * STG + 1024), "v"(off), "s"(pc) : "memory");
  }
  u32 voE = (u32)(2 * (128 * chunk + r8) + par) * 1152u + (u32)(sp8 ^ (r8 >> 1)) * 16u;
  int jh = 128 * chunk - 8 + r8; jh = jh < 0 ? 0 : jh;
  u32 voH = (u32)(2 * jh + par) * 1152u + (u32)(sp8 ^ (r8 & 6)) * 16u;
  const int u0 = 2 * wid, p0 = (u0 * 52) >> 8, i0 = u0 - 5 * p0, u1 = u0 + 1, p1 = (u1 * 52) >> 8, i1 = u1 - 5 * p1;
  u32 voO = voE ^ 64u, vw0 = wvoff(64 * i0 + lane), vw1 = wvoff(64 * i1 + lane);
  u32 sx = s0 * 128 + wid * 36864, dx = lds0 + wid * 2048;
  u32 sw0 = s0 * (64 * 3456) + p0 * 1152 + hq * 144, dw0 = lds0 + XSTG + p0 * WPART + i0 * 1024;
  u32 sw1 = s0 * (64 * 3456) + p1 * 1152 + hq * 144, dw1 = lds0 + XSTG + p1 * WPART + i1 * 1024;
  asm volatile("" : "+v"(voE), "+v"(voH), "+v"(voO), "+v"(vw0), "+v"(vw1), "+s"(sx), "+s"(dx), "+s"(sw0), "+s"(dw0), "+s"(sw1), "+s"(dw1));
  SBAR();
  const __amdgpu_buffer_rsrc_t rsX = mkrs(x, 2048 * 1152);
  const __amdgpu_buffer_rsrc_t rsW = mkrs((hg >> 3) ? wqb : wqa, 576 * 3456);
  {
    dma16(rsX, voE, sx, dx);
    dma16(rsX, voO, sx + 18432, dx + 1024);
    dma16(rsW, vw0, sw0, dw0);
    if (wid < 7) dma16(rsW, vw1, sw1, dw1);
    else {
      dma16(rsX, voH, s0 * 128, lds0 + 16384);
      const __amdgpu_buffer_rsrc_t rsG = mkrs(gw, 1152);
      dma16(rsG, lane * 16u, 0, lds0 + GWL); dma16(rsG, lane * 16u, 1024, lds0 + GWL + 1024);
    }
  }
  SBAR();
  asm volatile("" :: "s"(woa), "s"(wob), "s"(gw), "s"(out), "s"(ws));
  const int sg = cp >> 2, mi = 16 * (hg >> 2) + 4 * (cp & 3) + (hg & 3);
  const int rg = (mi * 57) >> 9, ct = mi - 9 * rg;
  int rot2 = ct + 2 * (rg & 1) + 4 * (sg & 1); rot2 = rot2 >= 9 ? rot2 - 9 : rot2;
  const int c = lane & 15, g = lane >> 4, q = (lane >> 2) & 3, p = lane & 3;
  const __amdgpu_buffer_rsrc_t rsS = mkrs(ws, WS_BYTES);
  const f32x4 z4 = {0.f, 0.f, 0.f, 0.f};
  u32 nonce = 0, vn = 0;
  const u32 rb = lds0 + XSTG + (8 * g + 2 * q) * WROW + 8 * p;

  if (wid >= 4) {
    __builtin_amdgcn_s_setprio(2);
    const int hw = wid - 4;
    u32 HB = lds0 + (128 + (c & 7)) * 128 + ((g ^ (c & 6)) * 16), HW = rb + WPART + 32 * hw;
    f32x4 hK = z4, hV = z4;
    const u32 ek = lds0 + XCH + (g * 8 + (c & 7)) * 8, ev = lds0 + XCH + 1280 + (lane & 31) * 8;
    WoState wo;
    const __amdgpu_buffer_rsrc_t rsO = mkrs((hw & 2) ? wob : woa, 576 * 1152);
    { const u32 woc = (u32)(ct * 128 + ((sp8 ^ (2 * ((r8 >> 1) & 3))) * 16));
      const u32 voR = (u32)r8 * 1152u + woc, voT = (u32)(64 + 72 * (r8 >> 2) + (r8 & 3)) * 1152u + woc;
#pragma unroll
      for (int m = 0; m < 4; ++m) { wo.vr[m] = voR + m * (9216 - 1024); wo.vt[m] = voT + (144 * (m & 1) + 4 * (m >> 1)) * 1152 - m * 1024; PIN(wo.vr[m]); PIN(wo.vt[m]); }
      wo.dst = lds0 + hw * 4096; wo.kk = rot2; wo.base = (hw & 1) ? 288 * 1152 : 0; }
    if (wid < 7) {
      const int a = hw;
      u32 vA = voE ^ (u32)((a & 1) * 64), vB = vA ^ 64u;
      u32 v5 = a == 0 ? vB + 15u * 18432u : (a == 1 ? voH : 0x80000000u);
      const u32 xso = a * 18432, xd = lds0 + a * 1024, xd5 = lds0 + (a == 0 ? 15 : 16) * 1024;
      u32 vw[9];
      const u32 wso = a * 1152 + hq * 144, wd = lds0 + XSTG + a * WPART;
#pragma unroll
      for (int i = 0; i < 9; ++i) { vw[i] = wvoff(64 * i + lane); PIN(vw[i]); }
      PIN(vA); PIN(vB); PIN(v5); PIN(HB); PIN(HW);
      u32x4 xh = {0u, 0u, 0u, 0u}, fk = xh, fv = xh;
      int st = s0, cs = 0;
#pragma unroll 1
      for (int it = 0; it < 9; ++it) {
        const int sn = nxt9(st), sl = nxtslot(cs);
#pragma unroll
        for (int i = 5; i < 9; ++i) dma16w(rsW, vw[i], st * (64 * 3456) + wso, wd + cs + i * 1024);
        if (it < 8) {
          dma16w(rsX, vA, sn * 128 + xso, xd + sl); dma16w(rsX, vB, sn * 128 + xso + 3 * 18432, xd + sl + 3072); dma16w(rsX, vA, sn * 128 + xso + 6 * 18432, xd + sl + 6144);
        }
        hK = mfma(fk, xh, hK); hV = mfma(xh, fv, hV);
        if (it < 8) wait_vm<7>(); else wait_vm<4>();
        bar();
        xh = lrd128(HB + cs); fk = trd2(HW + cs, WROW); fv = trd2(HW + cs + WPART, WROW);
        if (it < 8) {
#pragma unroll
          for (int i = 0; i < 5; ++i) dma16w(rsW, vw[i], sn * (64 * 3456) + wso, wd + sl + i * 1024);
          dma16w(rsX, vB, sn * 128 + xso + 9 * 18432, xd + sl + 9216); dma16w(rsX, vA, sn * 128 + xso + 12 * 18432, xd + sl + 12288);
          dma16w(rsX, v5, sn * 128, a == 2 ? lds0 + 3 * STG : xd5 + sl);
        } else wo_blocks(rsO, wo, 0, WOA);
        hK = mfma(fk, xh, hK); hV = mfma(xh, fv, hV);
        if (it < 8) wait_vm<11>(); else wait_vm<4 * WOA>();
        bar();
        xh = lrd128((HB ^ 64u) + cs); fk = trd2(HW + cs + 32 * WROW, WROW); fv = trd2(HW + cs + WPART + 32 * WROW, WROW);
        cs = sl; st = sn;
      }
      hK = mfma(fk, xh, hK); hV = mfma(xh, fv, hV);
      if (c >= 8) lwr64f(ek + hw * 256, hK);
      if (g >= 2) lwr64f(ev + hw * 256, hV);
    } else {
      const u32x2 inb0 = __builtin_amdgcn_raw_buffer_load_b64(rsS, INB_OFF + (u32)(sg * 64 + mi) * 512u + lane * 8u, 0, AUX_SC1);
      const u32 non0 = __builtin_amdgcn_raw_buffer_load_b32(rsS, NON_OFF + (u32)(sg * 64 + mi) * 4u, 0, AUX_SC1);
      const u32 BM = rb + (p >= 2 ? WPART + 112 : 128), BV = rb + 2 * WPART + 128;
      const u32 XG = lds0 + (16 * (hg >> 1) + c) * 128 + ((g ^ (c >> 1)) * 16), GG = lds0 + GWL + g * 16;
      f32x4 hM = z4, hV4 = z4, hG = z4;
      wait_vm<2>();
      int cs = 0, st = s0;
#pragma unroll 1
      for (int it = 0; it < 9; ++it) {
#pragma unroll
        for (int k2 = 0; k2 < 2; ++k2) {
          bar();
          const u32 ko = cs + k2 * 32 * WROW;
          const u32x4 xh = lrd128((HB ^ (u32)(64 * k2)) + cs), fk = trd2(HW + ko, WROW), fv = trd2(HW + ko + WPART, WROW), fm = trd2(BM + ko, WROW), f4 = trd2(BV + ko, WROW);
          const u32x4 xg = lrd128((XG ^ (u32)(64 * k2)) + cs), gg = lrd128(GG + st * 128 + k2 * 64);
          hK = mfma(fk, xh, hK); hV = mfma(xh, fv, hV); hM = mfma(fm, xh, hM); hV4 = mfma(xh, f4, hV4); hG = mfma(gg, xg, hG);
          if (k2 == 0 && it == 0) {
            nonce = __builtin_amdgcn_readfirstlane(non0) + 1u;
            while (__ballot(inb0[0] == nonce) != 0ull) ++nonce;
            if (lane == 0) __builtin_amdgcn_raw_buffer_store_b32(nonce, rsS, NON_OFF + (u32)(sg * 64 + mi) * 4u, 0, AUX_SC1);
          }
          if (k2 == 0 && it == 8) {
            vn = __builtin_amdgcn_raw_buffer_load_b32(rsS, NON_OFF + (u32)(sg * 64 + lane) * 4u, 0, AUX_SC1);
            wo_blocks(rsO, wo, 0, WOA);
          }
        }
        cs = nxtslot(cs); st = nxt9(st);
      }
      if (c >= 8) { lwr64f(ek + 3 * 256, hK); if (g >= 2) { lwr64f(ek + 4 * 256, hM); lwr64f(ek + 4 * 256 - 128, hM); } }
      if (g >= 2) { lwr64f(ev + 3 * 256, hV); lwr64f(ev + 4 * 256, hV4); }
      const float gt = __builtin_amdgcn_rcpf(1.f + __builtin_amdgcn_exp2f(-1.4426950408889634f * hG[0]));
      if (r8 == (hg & 1)) __builtin_amdgcn_raw_buffer_store_b32(__float_as_uint(gt), rsS, GATE_OFF + (u32)(cp * 128 + 16 * (hg >> 1) + lane) * 4u, 0, AUX_SC1);
    }
    wo_blocks(rsO, wo, WOA, WOB - WOA);
    lgkm0();
    bar();
    wo_blocks(rsO, wo, WOB, 9 - WOB);
    __builtin_amdgcn_s_setprio(0);
  } else {
    const int w = wid;
    u32 B = rb, BM = rb + (p >= 2 ? WPART + 112 : 128), XB[2];
    XB[0] = lds0 + (32 * w + c) * 128 + ((g ^ (c >> 1)) * 16); XB[1] = XB[0] ^ 64;
    f32x4 bias[2]; u32 mq[5], mP0, mP1;
    {
      const float NINF = -__builtin_inff();
      const bool first = (chunk == 0 && w == 0);
#pragma unroll
      for (int r = 0; r < 4; ++r) {
        const int i = 4 * g + r;
        const bool okc = (i <= c && i >= c - 7), okp = i >= c + 9;
        bias[0][r] = (okc || (okp && !first)) ? 0.f : NINF; bias[1][r] = (okc || okp) ? 0.f : NINF;
      }
      mq[0] = ~0u; mq[1] = g == 0 ? ~0u : 0u; mq[2] = ~mq[1]; mq[3] = g <= 1 ? ~0u : 0u; mq[4] = ~mq[3];
      mP0 = c < 8 ? mq[4] : 0u; mP1 = ~mP0;
      PIN(bias[0]); PIN(bias[1]); PIN(mq[1]); PIN(mq[2]); PIN(mq[3]); PIN(mq[4]); PIN(mP0); PIN(mP1);
    }
    u32 ek = lds0 + XCH + w * XCB + (g * 8 + (c & 7)) * 8, ev = lds0 + XCH + w * XCB + 1280 + (lane & 31) * 8;
    u32 wk = (w < 3 && c >= 8) ? ek + XCB : lds0 + DUM + (lane & 31) * 8, wv_ = (w < 3 && g >= 2) ? ev + XCB : lds0 + DUM + (lane & 31) * 8;
    PIN(ek); PIN(ev); PIN(wk); PIN(wv_);
    u32 so[2], st[2]; u32x4 ones = {0x3f803f80u, 0x3f803f80u, 0x3f803f80u, 0x3f803f80u};
    { const u32 ob = (u32)((cp * 8 + 2 * w) * 36 + 9 * (hg >> 2)) * 1024u;
#pragma unroll
      for (int t = 0; t < 2; ++t) { so[t] = lane * 16u + ob + t * 36864u + (hg & 3) * 2048u; st[t] = ((c * 16u + g * 8u) | (mq[4] & 0x80000000u)) + ob + t * 36864u + 8192u + (hg & 3) * 256u; PIN(so[t]); PIN(st[t]); }
      PIN(ones); }
    f32x4 acc[2][14];
#pragma unroll
    for (int t = 0; t < 2; ++t)
#pragma unroll
      for (int i = 0; i < 14; ++i) { acc[t][i] = z4; PIN(acc[t][i]); }
    PIN(B); PIN(BM); PIN(XB[0]); PIN(XB[1]);
    __builtin_amdgcn_s_waitcnt(0xC07F);
    u32x2 wlo[7], whi[7]; u32x4 xf[2][2];
    auto faddr = [&](auto NC) -> u32 {
      constexpr int n = decltype(NC)::value; constexpr int f = n % 14, ko = (n / 14) * 32 * WROW;
      if constexpr (f < 4) return B + ko + WPART + 32 * f;
      else if constexpr (f == 4) return BM + ko;
      else if constexpr (f < 10) return B + ko + 2 * WPART + 32 * (f - 5);
      else return B + ko + 32 * (f - 10);
    };
    u32x10 Kp[3]; u32 Qp[2][10]; u32x6 Vp[5];
    u32 zr[4] = {0u, 0u, 0u, 0u};
#pragma unroll
    for (int i = 0; i < 4; ++i) PIN(zr[i]);
    auto pack = [&](auto FC, auto TC) {
      constexpr int f = decltype(FC)::value, t = decltype(TC)::value;
      u32 a0 = pk(acc[t][f][0], acc[t][f][1]), a1 = pk(acc[t][f][2], acc[t][f][3]);
      asm volatile("" : "+v"(a0), "+v"(a1));
      if constexpr (f < 4) { Kp[t + 1][2 * f] = a0; Kp[t + 1][2 * f + 1] = a1; }
      else if constexpr (f == 4) {
        auto r0 = __builtin_amdgcn_permlane32_swap(a0, zr[2 * t], false, false); auto r1 = __builtin_amdgcn_permlane32_swap(a1, zr[2 * t + 1], false, false);
        Qp[t][8] = r0[0]; Qp[t][9] = r1[0]; Kp[t + 1][8] = r0[1]; Kp[t + 1][9] = r1[1];
      }
      else if constexpr (f < 10) { Vp[f - 5][2 * t + 2] = a0; Vp[f - 5][2 * t + 3] = a1; }
      else { Qp[t][2 * (f - 10)] = a0; Qp[t][2 * (f - 10) + 1] = a1; }
    };
    wait_vm<0>();
    bar();
    xf[0][0] = lrd128(XB[0]); xf[0][1] = lrd128(XB[0] + 2048);
    sfor<0, PD>([&](auto NC) { constexpr int n = decltype(NC)::value; const u32 a = faddr(NC); wlo[n % 7] = trd(a); whi[n % 7] = trd(a + WROW); SBAR(); });
    auto stage = [&](auto LC, int d) {
      constexpr bool LAST = decltype(LC)::value;
      sfor<0, 28>([&](auto NC) {
        constexpr int n = decltype(NC)::value;
        constexpr int f = n % 14, k2 = n / 14, nn = (n + PD) % 28, kn = nn / 14;
        constexpr bool RD = !(LAST && n + PD >= 28), BI = LAST && k2 == 1;
        constexpr int fx = f - 2;
        if constexpr ((nn == 0 && !LAST) || nn == 14) { SBAR(); bar(); SBAR(); }
        const u32x4 wv = {wlo[n % 7][0], wlo[n % 7][1], whi[n % 7][0], whi[n % 7][1]};
        const u32 a = faddr(IC<nn>{});
        if constexpr (f >= 5 && f < 10) mfma_acc<BI>(acc[0][f], xf[k2][0], wv); else mfma_acc<BI>(acc[0][f], wv, xf[k2][0]);
        SBAR();
        if constexpr (RD) wlo[nn % 7] = trd(a);
        if constexpr (BI && fx >= 0) pack(IC<(fx >= 0 ? fx : 0)>{}, IC<1>{});
        SBAR();
        if constexpr (f >= 5 && f < 10) mfma_acc<BI>(acc[1][f], xf[k2][1], wv); else mfma_acc<BI>(acc[1][f], wv, xf[k2][1]);
        SBAR();
        if constexpr (RD) whi[nn % 7] = trd(a + WROW);
        if constexpr (BI && !RD) pack(IC<2 * (f - 8)>{}, IC<0>{});
        if constexpr (!LAST) {
          if constexpr (nn == 27) { B += d; PIN(B); }
          if constexpr (nn == 19) { BM += d; PIN(BM); }
          if constexpr (nn == 7) { XB[0] += d; PIN(XB[0]); }
          if constexpr (nn == 20) { XB[1] += d; PIN(XB[1]); }
        }
        if constexpr (RD) {
          if constexpr (nn % 14 == 1) xf[kn][0] = lrd128(XB[kn]);
          if constexpr (nn % 14 == 3) xf[kn][1] = lrd128(XB[kn] + 2048);
        }
        SBAR();
      });
    };
    int sl = 0;
#pragma unroll 1
    for (int it = 0; it < 8; ++it) { const int sn = nxtslot(sl), d = sn - sl; sl = sn; stage(IC<0>{}, d); }
    stage(IC<1>{}, 0);
    static_assert(PD == 6, "pack schedule");
#pragma unroll
    for (int i = 0; i < 5; ++i) lwr64(wk + i * 256, Kp[2][2 * i], Kp[2][2 * i + 1]);
#pragma unroll
    for (int i = 0; i < 5; ++i) lwr64(wv_ + i * 256, Vp[i][4], Vp[i][5]);
    SBAR();
    pack(IC<1>{}, IC<0>{}); pack(IC<3>{}, IC<0>{});
    pack(IC<12>{}, IC<1>{}); pack(IC<13>{}, IC<1>{});
    lgkm0();
    bar();
#pragma unroll
    for (int i = 0; i < 5; ++i) { const u32x2 a = lrd64(ek + i * 256); Kp[0][2 * i] = a[0]; Kp[0][2 * i + 1] = a[1]; }
#pragma unroll
    for (int i = 0; i < 5; ++i) { const u32x2 v = *(volatile LAS u32x2*)(uintptr_t)(ev + i * 256); Vp[i][0] = v[0]; Vp[i][1] = v[1]; }
    PIN(Kp[1]); PIN(Kp[2]);
    SBAR();
    const bool selp = (c < 8) && (g >= 2);
    f32x4 sp[2][4], sc[2][4]; float mx[2][4];
    u32x4 qo[2][4];
    auto mkq = [&](auto TC, auto HC) {
      constexpr int t = decltype(TC)::value, h = decltype(HC)::value;
      constexpr int ia0 = h == 0 ? 0 : (h == 3 ? 4 : 2), ia1 = h <= 1 ? 0 : 2, ib0 = h <= 1 ? 1 : (h == 3 ? 0 : 3), ib1 = h == 0 ? -1 : (h == 3 ? 0 : 1);
      qo[t][h][0] = Qp[t][2 * h] & mq[ia0]; qo[t][h][1] = Qp[t][2 * h + 1] & mq[ia1]; qo[t][h][2] = Qp[t][2 * h + 2] & mq[ib0];
      if constexpr (ib1 >= 0) qo[t][h][3] = Qp[t][2 * h + 3] & mq[ib1 >= 0 ? ib1 : 0]; else qo[t][h][3] = 0u;
      PIN(qo[t][h]);
    };
    mkq(IC<1>{}, IC<0>{});
    SBAR();
    sfor<0, 8>([&](auto IC_) {
      constexpr int j = decltype(IC_)::value, t = 1 - (j >> 2), h = j & 3;
      if constexpr (j == 4) { PIN(Kp[0]); SBAR(); }
      const u32x4 k0 = __builtin_shufflevector(Kp[t], Kp[t], 2 * h, 2 * h + 1, 2 * h + 2, 2 * h + 3);
      const u32x4 k1 = __builtin_shufflevector(Kp[t + 1], Kp[t + 1], 2 * h, 2 * h + 1, 2 * h + 2, 2 * h + 3);
      sc[t][h] = mfma(k1, qo[t][h], bias[t]);
      SBAR();
      if constexpr (j < 7) mkq(IC<1 - ((j + 1) >> 2)>{}, IC<(j + 1) & 3>{});
      SBAR();
      sp[t][h] = mfma(k0, qo[t][h], bias[t]);
      SBAR();
      if constexpr (j < 3) pack(IC<11 + j>{}, IC<0>{});
      else if constexpr (j < 6) pack(IC<5 + 2 * (j - 3)>{}, IC<0>{});
      SBAR();
    });
    if (selp) {
#pragma unroll
      for (int t = 1; t >= 0; --t)
#pragma unroll
        for (int h = 0; h < 4; ++h) { sc[t][h] = sp[t][h]; PIN(sc[t][h]); }
    }
    SBAR();
#pragma unroll
    for (int t = 0; t < 2; ++t)
#pragma unroll
      for (int h = 0; h < 4; ++h) mx[t][h] = fmx(fmx(fmx(sc[t][h][0], sc[t][h][1]), sc[t][h][2]), sc[t][h][3]);
#pragma unroll
    for (int t = 0; t < 2; ++t) {
      float* m = mx[t];
      sw32(m[0], m[1]); float m01 = fmx(m[0], m[1]); sw32(m[2], m[3]); float m23 = fmx(m[2], m[3]);
      sw16(m01, m23); float R = fmx(m01, m23) * -CEXP;
      float a = R, bb = R; sw16(a, bb); m[0] = a; m[1] = a; sw32(m[0], m[1]); m[2] = bb; m[3] = bb; sw32(m[2], m[3]);
    }
    f32x2 ex[2][4][2];
#pragma unroll
    for (int t = 0; t < 2; ++t)
#pragma unroll
      for (int h = 0; h < 4; ++h) {
        const f32x2 cc = {CEXP, CEXP}, mm = {mx[t][h], mx[t][h]};
        ex[t][h][0] = __builtin_elementwise_fma((f32x2){sc[t][h][0], sc[t][h][1]}, cc, mm); ex[t][h][1] = __builtin_elementwise_fma((f32x2){sc[t][h][2], sc[t][h][3]}, cc, mm);
      }
    asm volatile("" : "+v"(Vp[0]), "+v"(Vp[1]), "+v"(Vp[2]), "+v"(Vp[3]), "+v"(Vp[4]));
    SBAR();
    f32x4 olo[2][4], ohi[2][4], sm[2][4];
    float ee[4]; u32 pq[2]; u32x4 po[2], oa[2], ob_[2]; u32x2 oc[2];
    auto eA = [&](auto JC) { constexpr int j = decltype(JC)::value, t = j >> 2, h = j & 3;
      ee[0] = __builtin_amdgcn_exp2f(ex[t][h][0][0]); ee[1] = __builtin_amdgcn_exp2f(ex[t][h][0][1]); ee[2] = __builtin_amdgcn_exp2f(ex[t][h][1][0]);
      asm volatile("" : "+v"(ee[0]), "+v"(ee[1]), "+v"(ee[2])); };
    auto eB = [&](auto JC) { constexpr int j = decltype(JC)::value, t = j >> 2, h = j & 3;
      ee[3] = __builtin_amdgcn_exp2f(ex[t][h][1][1]); pq[0] = pk(ee[0], ee[1]); SBAR(); po[j & 1][0] = pq[0] & mP0; pq[1] = pk(ee[2], ee[3]);
      asm volatile("" : "+v"(pq[0]), "+v"(pq[1])); };
    auto eC = [&](auto JC) { constexpr int j = decltype(JC)::value;
      po[j & 1][2] = pq[0] & mP1; po[j & 1][1] = pq[1] & mP0; po[j & 1][3] = pq[1] & mP1; PIN(po[j & 1]); };
    auto fin = [&](auto TC) {
      constexpr int t = decltype(TC)::value;
      asm volatile("" :: "v"(sm[t][0]), "v"(sm[t][1]), "v"(sm[t][2]), "v"(ohi[t][0]));
      f32x2 ov[10]; float iv[4];
#pragma unroll
      for (int h = 0; h < 4; ++h) iv[h] = __builtin_amdgcn_rcpf(sm[t][h][0]);
#pragma unroll
      for (int h = 0; h < 4; ++h) { ov[2 * h] = (f32x2){olo[t][h][0], olo[t][h][1]} * iv[h]; ov[2 * h + 1] = (f32x2){olo[t][h][2], olo[t][h][3]} * iv[h]; }
      ov[8] = (f32x2){ohi[t][3][0], ohi[t][3][1]} * iv[3]; ov[9] = (f32x2){ohi[t][3][2], ohi[t][3][3]} * iv[3];
#pragma unroll
      for (int i = 0; i < 10; ++i) PIN(ov[i]);
      SBAR();
      if (g < 2) {
        PIN(iv[2]); ov[6] = (f32x2){ohi[t][2][0], ohi[t][2][1]} * iv[2]; PIN(ov[6]);
        if (g == 0) {
          PIN(iv[0]); PIN(iv[1]); PIN(iv[2]);
          ov[2] = (f32x2){ohi[t][0][0], ohi[t][0][1]} * iv[0]; PIN(ov[2]);
          ov[4] = (f32x2){ohi[t][1][0], ohi[t][1][1]} * iv[1]; PIN(ov[4]);
          ov[5] = (f32x2){ohi[t][1][2], ohi[t][1][3]} * iv[1]; PIN(ov[5]);
          ov[7] = (f32x2){ohi[t][2][2], ohi[t][2][3]} * iv[2]; PIN(ov[7]);
        }
      }
      oa[t] = (u32x4){pk(ov[0][0], ov[0][1]), pk(ov[1][0], ov[1][1]), pk(ov[2][0], ov[2][1]), pk(ov[3][0], ov[3][1])};
      __builtin_amdgcn_raw_buffer_store_b128(oa[t], rsS, so[t], 0, AUX_SC1);
      SBAR();
      ob_[t] = (u32x4){pk(ov[4][0], ov[4][1]), pk(ov[5][0], ov[5][1]), pk(ov[6][0], ov[6][1]), pk(ov[7][0], ov[7][1])};
      __builtin_amdgcn_raw_buffer_store_b128(ob_[t], rsS, so[t] + 1024u, 0, AUX_SC1);
      SBAR();
      oc[t] = (u32x2){pk(ov[8][0], ov[8][1]), pk(ov[9][0], ov[9][1])};
      __builtin_amdgcn_raw_buffer_store_b64(oc[t], rsS, st[t], 0, AUX_SC1);
      SBAR();
    };
    eA(IC<0>{}); eB(IC<0>{}); eC(IC<0>{});
    SBAR();
    sfor<0, 8>([&](auto JC) {
      constexpr int j = decltype(JC)::value, t = j >> 2, h = j & 3;
      if constexpr (j < 7) eA(IC<(j < 7 ? j + 1 : 0)>{});
      SBAR();
      sm[t][h] = mfma(ones, po[j & 1], z4);
      SBAR();
      if constexpr (j < 7) eB(IC<(j < 7 ? j + 1 : 0)>{});
      SBAR();
      olo[t][h] = mfma(__builtin_shufflevector(Vp[h], Vp[h], 2 * t, 2 * t + 1, 2 * t + 2, 2 * t + 3), po[j & 1], z4);
      SBAR();
      if constexpr (j < 7) eC(IC<(j < 7 ? j + 1 : 0)>{});
      SBAR();
      ohi[t][h] = mfma(__builtin_shufflevector(Vp[h + 1], Vp[h + 1], 2 * t, 2 * t + 1, 2 * t + 2, 2 * t + 3), po[j & 1], z4);
      SBAR();
      if constexpr (j == 4) { asm volatile("" :: "v"(sm[0][3])); fin(IC<0>{}); }
    });
    fin(IC<1>{});
    asm volatile("" :: "v"(oa[0]), "v"(ob_[0]), "v"(oc[0]), "v"(oa[1]), "v"(ob_[1]));
  }
  const int kq = wid & 3, th = wid >> 2;
  u32 WB[4];
  { const int pos = 4 * g + q, s = (pos >> 1) & 3;
#pragma unroll
    for (int i = 0; i < 4; ++i) { WB[i] = lds0 + (kq + (th ? 20 : 0)) * 4096 + pos * 128 + (((i ^ (2 * (kq & 1))) ^ s) * 32) + p * 8; PIN(WB[i]); } }
  u32 myin = INB_OFF + (u32)(sg * 64 + mi) * 512u + lane * 8u, dst = INB_OFF + (u32)(sg * 64 + lane) * 512u + mi * 8u;
  PIN(myin); PIN(dst);
  const int T0 = 32 * sg + 4 * rg + 2 * th + (kq >> 1), TX = 32 * sg + 28 + rg;
  u32 l16 = lane * 16u; PIN(l16);
  u32 so_k, so_b[3];
  { int r = rot2 + (th ? 5 : 0); r = r >= 9 ? r - 9 : r; so_k = r * 1024u;
    so_b[0] = rg < 4 ? (u32)(TX * 36 + 9 * kq) * 1024u : 0xf0000000u;
    so_b[1] = (u32)((T0 ^ 1) * 36 + 9 * kq) * 1024u; so_b[2] = (u32)(T0 * 36 + 9 * kq) * 1024u;
    asm volatile("" : "+s"(so_k), "+s"(so_b[0]), "+s"(so_b[1]), "+s"(so_b[2])); }
  u32 RW[3];
#pragma unroll
  for (int j = 1; j < 4; ++j) { RW[j - 1] = lds0 + 32768 + ((th * 4 + (kq ^ j)) * 6 + (j - 1) * 2) * 1024 + lane * 16; PIN(RW[j - 1]); }
  wait_vm<0>();
  bar();
  if (wid == 7) __builtin_amdgcn_raw_buffer_store_b64((u32x2){vn, nonce}, rsS, dst, 0, AUX_SC1);
  *(LAS u32*)(uintptr_t)(lds0 + CNT + wid * 4) = 0u;
  u32x4 wf[9][4];
  sfor<0, 9>([&](auto KC) { constexpr int k = decltype(KC)::value;
    if constexpr (k == 4) {
      const u32 d = th ? (u32)(4 - 9) * 16384u : 4u * 16384u;
#pragma unroll
      for (int i = 0; i < 4; ++i) { WB[i] += d; PIN(WB[i]); }
    }
    if constexpr (k == 8) {
#pragma unroll
      for (int i = 0; i < 4; ++i) { WB[i] += 4u * 16384u; PIN(WB[i]); }
    }
#pragma unroll
    for (int i = 0; i < 4; ++i) wf[k][i] = trd2(WB[i] + (k & 3) * 16384, 2048); });
  SBAR();
  bool allok = true;
  if (wid == 7) {
    const int cl = (lane >> 2) & 3;
    const u64 need = __ballot(cl == (rg >> 1) || (rg < 4 && cl == 3));
    u64 got = 0;
#pragma unroll 1
    for (int spin = 0; spin < (1 << 20); ++spin) {
      const u32x2 v = __builtin_amdgcn_raw_buffer_load_b64(rsS, myin, 0, AUX_SC1);
      const bool ok = v[0] == nonce;
      if (ok && v[1] != vn) { vn = v[1]; __builtin_amdgcn_raw_buffer_store_b64((u32x2){vn, nonce}, rsS, dst, 0, AUX_SC1); }
      got = __ballot(ok);
      if ((got & need) == need) break;
      __builtin_amdgcn_s_sleep(1);
    }
    allok = got == ~0ull;
  }
  lgkm0();
  bar();
  if (rg < 7) {
    u32x4 of[OW + 1];
    const u32 so_k0 = so_k;
    auto ldo = [&](auto NC) { constexpr int n = decltype(NC)::value; constexpr int ph = ord_ph(n), k = ord_k(n);
      if constexpr (k == 0) so_k = so_k0;
      of[n % (OW + 1)] = __builtin_amdgcn_raw_buffer_load_b128(rsS, l16, (ph == 0 && k == 4 && th) ? 0xf0000000u : so_b[ph] + so_k, AUX_SC1);
      if constexpr (n + 1 < NL && ord_k(n + 1) == k + 1) so_k = (so_k == 8192u) ? 0u : so_k + 1024u; };
    const int L = 32 * (wid & 1) + (lane >> 1);
    float gm = 0.f, gx = 0.f;
    auto ldg = [&]() {
      gm = __uint_as_float(__builtin_amdgcn_raw_buffer_load_b32(rsS, c * 4u, GATE_OFF + (u32)T0 * 64u, AUX_SC1));
      gx = __uint_as_float(__builtin_amdgcn_raw_buffer_load_b32(rsS, (L & 15) * 4u, GATE_OFF + (u32)(TX & 127) * 64u, AUX_SC1)); };
    sfor<0, OW>([&](auto NC) { ldo(NC); SBAR(); });
    f32x4 ac[3][4], T[2];
    const __amdgpu_buffer_rsrc_t rsD = mkrs(out, 2048 * 1152);
    const int row = 2 * (128 * (T0 >> 4) + 16 * (T0 & 7) + c) + ((T0 >> 3) & 1);
    u32 po = (u32)(row * 576 + ct * 64 + (kq & 1) * 32 + (g & 1) * 16 + (g >> 1) * 8) * 2u;
    const int rowx = 2 * (128 * (TX >> 4) + 16 * (TX & 7) + (L & 15)) + ((TX >> 3) & 1);
    u32 px = (u32)(rowx * 576 + ct * 64 + (wid >> 1) * 16 + (L >> 4) * 4 + (lane & 1) * 2) * 2u;
    u32 ra = lds0 + 32768 + wid * 6144 + l16, rx = lds0 + wid * 512 + lane * 8, wx = (lds0 + wid * 4096 + l16) ^ (u32)((kq & 1) * 2048), wx2 = wx ^ 2048u;
    u32 fw = lds0 + CNT + wid * 4, fg = lds0 + CNT + th * 16;
    PIN(po); PIN(px); PIN(ra); PIN(rx); PIN(wx); PIN(wx2); PIN(fw); PIN(fg);
    auto flag = [&](u32 v) { asm volatile("" ::: "memory"); *(volatile LAS u32*)(uintptr_t)fw = v; };
    float fm = 0.f;
    sfor<0, NL>([&](auto NC) {
      constexpr int n = decltype(NC)::value; constexpr int ph = ord_ph(n), k = ord_k(n);
      if constexpr (n + OW < NL) ldo(IC<n + OW>{});
      if constexpr (n == GL) ldg();
      SBAR();
#pragma unroll
      for (int ii = 0; ii < 4; ++ii) { const int i = ii ^ 2; ac[ph][i] = mfma(wf[k][i], of[n % (OW + 1)], k == 0 ? z4 : ac[ph][i]); }
      SBAR();
      if constexpr (n >= 10 && n < 14) lwr128(((n - 10) & 2 ? wx2 : wx) + ((n - 10) & 1) * 1024, ac[0][n - 10]);
      if constexpr (n >= 14 && n < 16) { lwr128(RW[n - 13], ac[1][2 * (n - 14)]); lwr128(RW[n - 13] + 1024, ac[1][2 * (n - 14) + 1]); }
      if constexpr (n == 15) flag(2u);
      if constexpr (n == MID) {
#pragma unroll 1
        for (;;) {
          const u32x4 f = *(volatile LAS u32x4*)(uintptr_t)fg;
          asm volatile("" ::: "memory");
#pragma unroll
          for (int t = 0; t < 2; ++t) T[t] = *(LAS f32x4*)(uintptr_t)(ra + 2048 + t * 1024) + *(LAS f32x4*)(uintptr_t)(ra + 4096 + t * 1024);
          asm volatile("" ::: "memory");
          if (__builtin_amdgcn_readfirstlane(f[0] & f[1] & f[2] & f[3]) & 2u) break;
        }
        fm = (kq >> 1) ? 1.f - gm : gm;
        PIN(T[0]); PIN(T[1]); PIN(fm);
      }
      SBAR();
    });
    lwr128(RW[0], ac[2][2]); lwr128(RW[0] + 1024, ac[2][3]);
    flag(3u);
    f32x4 D[2];
#pragma unroll
    for (int t = 0; t < 2; ++t) { D[t] = (ac[2][t] - T[t]) * fm + T[t]; PIN(D[t]); }
    {
      f32x4 r[2];
#pragma unroll 1
      for (;;) {
        const u32 f = *(volatile LAS u32*)(uintptr_t)(fw ^ 4u);
        asm volatile("" ::: "memory");
#pragma unroll
        for (int t = 0; t < 2; ++t) r[t] = *(LAS f32x4*)(uintptr_t)(ra + t * 1024);
        asm volatile("" ::: "memory");
        if (__builtin_amdgcn_readfirstlane(f) == 3u) break;
      }
      f32x4 v[2];
#pragma unroll
      for (int t = 0; t < 2; ++t) v[t] = r[t] * fm + D[t];
      auto s0_ = __builtin_amdgcn_permlane16_swap(pk(v[0][0], v[0][1]), pk(v[1][0], v[1][1]), false, false);
      auto s1_ = __builtin_amdgcn_permlane16_swap(pk(v[0][2], v[0][3]), pk(v[1][2], v[1][3]), false, false);
      __builtin_amdgcn_raw_buffer_store_b128((u32x4){s0_[0], s1_[0], s0_[1], s1_[1]}, rsD, po, 0, 0);
    }
    if (rg < 4 && th == 0) {
#pragma unroll
      for (int hf = 0; hf < 2; ++hf) {
        f32x2 e[8];
#pragma unroll 1
        for (;;) {
          const u32x4 f = *(volatile LAS u32x4*)(uintptr_t)(fg ^ 16u);
          asm volatile("" ::: "memory");
#pragma unroll
          for (int j = 0; j < 8; ++j) e[j] = *(LAS f32x2*)(uintptr_t)(rx + j * 4096 + hf * 2048);
          asm volatile("" ::: "memory");
          if (__builtin_amdgcn_readfirstlane(f[0] & f[1] & f[2] & f[3]) & 2u) break;
        }
        const f32x2 A = (e[0] + e[1]) + (e[4] + e[5]), Bv = (e[2] + e[3]) + (e[6] + e[7]);
        const f32x2 r = (A - Bv) * gx + Bv;
        __builtin_amdgcn_raw_buffer_store_b32(pk(r[0], r[1]), rsD, px + hf * 64, 0, 0);
      }
    }
  }
  if (!allok) {
#pragma unroll 1
    for (int spin = 0; spin < (1 << 20); ++spin) {
      const u32x2 v = __builtin_amdgcn_raw_buffer_load_b64(rsS, myin, 0, AUX_SC1);
      const bool ok = v[0] == nonce;
      if (ok && v[1] != vn) { vn = v[1]; __builtin_amdgcn_raw_buffer_store_b64((u32x2){vn, nonce}, rsS, dst, 0, AUX_SC1); }
      if (__ballot(ok) == ~0ull) break;
      __builtin_amdgcn_s_sleep(1);
    }
  }
  asm volatile(".Lgm_end:");
}

extern "C" void kernel_launch(void* const* d_in, const int* in_sizes, int n_in, void* d_out, int out_size, void* d_ws, size_t ws_size, hipStream_t stream) {
  const u16* x = (const u16*)d_in[0]; const u16* wqa = (const u16*)d_in[1]; const u16* woa = (const u16*)d_in[2];
  const u16* wqb = (const u16*)d_in[4]; const u16* wob = (const u16*)d_in[5]; const u16* gw = (const u16*)d_in[6];
  (void)hipFuncSetAttribute((const void*)fused, hipFuncAttributeMaxDynamicSharedMemorySize, LDS_BYTES);
  fused<<<256, 512, LDS_BYTES, stream>>>(x, wqa, woa, wqb, wob, gw, (u16*)d_out, (char*)d_ws);
}
```

```cpp
#include <hip/hip_runtime.h>
typedef unsigned short u16; typedef unsigned int u32; typedef unsigned long long u64;
typedef __attribute__((ext_vector_type(8))) __bf16 bf16x8;
typedef __attribute__((ext_vector_type(2))) __bf16 bf16x2;
typedef __attribute__((ext_vector_type(4))) float f32x4;
typedef __attribute__((ext_vector_type(2))) float f32x2;
typedef __attribute__((ext_vector_type(4))) short s16x4;
typedef __attribute__((ext_vector_type(4))) u32 u32x4;
typedef __attribute__((ext_vector_type(2))) u32 u32x2;
typedef __attribute__((ext_vector_type(6))) u32 u32x6;
typedef __attribute__((ext_vector_type(10))) u32 u32x10;
#define LAS __attribute__((address_space(3)))
#define DI __device__ __forceinline__
#define SBAR() __builtin_amdgcn_sched_barrier(0)
#define PIN(x) asm volatile("" : "+v"(x))
template <int V> struct IC { static constexpr int value = V; };
template <int A, int B, class F> __device__ __forceinline__ void sfor(F&& f) { if constexpr (A < B) { f(IC<A>{}); sfor<A + 1, B>(f); } }

constexpr int WROW = 144, WPART = 9232, XSTG = 136 * 128, STG = 45184;
constexpr int LDS_BYTES = 163840;
constexpr int XCH = 147456, XCB = 2560, GWL = XCH + 4 * XCB, CNT = GWL + 2048, DUM = CNT + 256;
constexpr u32 OP_BYTES = 128u * 36u * 1024u;
constexpr u32 GATE_OFF = OP_BYTES;
constexpr u32 NON_OFF = GATE_OFF + 8192;
constexpr u32 INB_OFF = NON_OFF + 1024;
constexpr u32 WS_BYTES = INB_OFF + 256 * 512;
constexpr float CEXP = 0.23570226039551584f * 1.4426950408889634f;
constexpr int AUX_SC1 = 16;
constexpr int PD = 6;
constexpr int WIN = 9;
constexpr int WINO = 6;
constexpr int WOA = 1, WOB = 3;
constexpr int OW = 6, NL = 23, MID = 19, GL = 8;

DI u32 pk(float a, float b) { f32x2 v = {a, b}; bf16x2 r = __builtin_convertvector(v, bf16x2); return __builtin_bit_cast(u32, r); }
DI f32x4 mfma(u32x4 a, u32x4 b, f32x4 c) { return __builtin_amdgcn_mfma_f32_16x16x32_bf16(__builtin_bit_cast(bf16x8, a), __builtin_bit_cast(bf16x8, b), c, 0, 0, 0); }
template <bool BI> DI void mfma_acc(f32x4& c, u32x4 a, u32x4 b) {
  if constexpr (BI) c = mfma(a, b, c);
  else asm volatile("v_mfma_f32_16x16x32_bf16 %0, %1, %2, %0" : "+v"(c) : "v"(a), "v"(b));
}
DI u32x2 trd(u32 a) { return __builtin_bit_cast(u32x2, __builtin_amdgcn_ds_read_tr16_b64_v4i16((LAS s16x4*)(uintptr_t)a)); }
DI u32x4 trd2(u32 a, int st) { const u32x2 x = trd(a), y = trd(a + st); return (u32x4){x[0], x[1], y[0], y[1]}; }
DI u32x4 lrd128(u32 a) { return *(LAS u32x4*)(uintptr_t)a; }
DI u32x2 lrd64(u32 a) { return *(LAS u32x2*)(uintptr_t)a; }
DI void lwr64(u32 a, u32 x, u32 y) { *(LAS u32x2*)(uintptr_t)a = (u32x2){x, y}; }
DI void lwr64f(u32 a, f32x4 v) { lwr64(a, pk(v[0], v[1]), pk(v[2], v[3])); }
DI void lwr128(u32 a, f32x4 v) { *(LAS f32x4*)(uintptr_t)a = v; }
DI void dma16(__amdgpu_buffer_rsrc_t rs, u32 voff, u32 soff, u32 ldsdst) {
  asm volatile("s_mov_b32 m0, %0\n\ts_nop 0\n\tbuffer_load_dwordx4 %1, %2, %3 offen lds" :: "s"(ldsdst), "v"(voff), "s"(rs), "s"(soff) : "memory");
}
DI void dma16w(__amdgpu_buffer_rsrc_t rs, u32 voff, u32 soff, u32 ldsdst) {
  asm volatile("s_mov_b32 m0, %0\n\ts_waitcnt vmcnt(%4)\n\tbuffer_load_dwordx4 %1, %2, %3 offen lds" :: "s"(ldsdst), "v"(voff), "s"(rs), "s"(soff), "i"(WIN) : "memory");
}
template <int N> DI void wait_vm() { asm volatile("s_waitcnt vmcnt(%0)" :: "i"(N) : "memory"); }
DI void bar() { asm volatile("s_barrier" ::: "memory"); }
DI void lgkm0() { asm volatile("s_waitcnt lgkmcnt(0)" ::: "memory"); }
DI float fmx(float a, float b) { return __builtin_elementwise_maximum(a, b); }
DI void sw32(float& a, float& b) { auto r = __builtin_amdgcn_permlane32_swap(__float_as_uint(a), __float_as_uint(b), false, false); a = __uint_as_float(r[0]); b = __uint_as_float(r[1]); }
DI void sw16(float& a, float& b) { auto r = __builtin_amdgcn_permlane16_swap(__float_as_uint(a), __float_as_uint(b), false, false); a = __uint_as_float(r[0]); b = __uint_as_float(r[1]); }
DI int nxt9(int s) { return s == 8 ? 0 : s + 1; }
DI int nxtslot(int s) { return s == 2 * STG ? 0 : s + STG; }
DI __amdgpu_buffer_rsrc_t mkrs(const void* p, u32 n) { return __builtin_amdgcn_make_buffer_rsrc((void*)p, 0, n, 0x00020000); }
DI u32 wvoff(u32 ci) {
  const u32 pos = __umul24(ci, 7282u) >> 16, ch = ci - __umul24(pos, 9u);
  const u32 row = (pos & ~7u) | ((pos & 1u) << 2) | ((pos & 7u) >> 1);
  return __umul24(row, 3456u) + ch * 16u;
}

struct WoState { u32 vr[4], vt[4]; u32 dst; int kk; u32 base; };
DI void wo_blocks(__amdgpu_buffer_rsrc_t rs, WoState w, int v0, int n) {
  w.dst += v0 * 16384; w.kk += v0; w.kk = w.kk >= 9 ? w.kk - 9 : w.kk;
#pragma unroll 1
  for (int i = 0; i < n; ++i) {
    if (w.kk < 8) {
      const u32 so = w.base + (u32)(36 * w.kk - 4 * (w.kk & 1)) * 1152u;
      asm volatile("s_mov_b32 m0, %0\n\ts_waitcnt vmcnt(%7)\n\tbuffer_load_dwordx4 %1, %5, %6 offen lds\n\tbuffer_load_dwordx4 %2, %5, %6 offen offset:1024 lds\n\t"
                   "buffer_load_dwordx4 %3, %5, %6 offen offset:2048 lds\n\tbuffer_load_dwordx4 %4, %5, %6 offen offset:3072 lds"
                   :: "s"(w.dst), "v"(w.vr[0]), "v"(w.vr[1]), "v"(w.vr[2]), "v"(w.vr[3]), "s"(rs), "s"(so), "i"(WINO) : "memory");
      ++w.kk;
    } else {
      asm volatile("s_mov_b32 m0, %0\n\ts_waitcnt vmcnt(%7)\n\tbuffer_load_dwordx4 %1, %5, %6 offen lds\n\tbuffer_load_dwordx4 %2, %5, %6 offen offset:1024 lds\n\t"
                   "buffer_load_dwordx4 %3, %5, %6 offen offset:2048 lds\n\tbuffer_load_dwordx4 %4, %5, %6 offen offset:3072 lds"
                   :: "s"(w.dst), "v"(w.vt[0]), "v"(w.vt[1]), "v"(w.vt[2]), "v"(w.vt[3]), "s"(rs), "s"(w.base), "i"(WINO) : "memory");
      w.kk = 0;
    }
    w.dst += 16384;
  }
}

constexpr int ord_ph(int n) { return n < 5 ? 0 : (n < 14 ? 1 : 2); }
constexpr int ord_k(int n) { return n < 5 ? n : (n < 14 ? n - 5 : n - 14); }

__global__ __launch_bounds__(512, 2) void fused(const u16* __restrict__ x, const u16* __restrict__ wqa, const u16* __restrict__ woa,
                                                const u16* __restrict__ wqb, const u16* __restrict__ wob, const u16* __restrict__ gw,
                                                u16* __restrict__ out, char* __restrict__ ws) {
  extern __shared__ __attribute__((aligned(16))) char lds[];
  const int tid = threadIdx.x, lane = tid & 63;
  const int wid = __builtin_amdgcn_readfirstlane(tid >> 6);
  const int b = blockIdx.x, xcd = b & 7, bi = b >> 3;
  const int cp = 8 * (xcd & 1) + (bi & 7), hg = 4 * (xcd >> 1) + (bi >> 3);
  const int par = cp & 1, chunk = cp >> 1;
  const int hq = hg & 7;
  const int s0 = 2 * (((bi & 7) + (bi >> 3)) & 3) + (((xcd >> 1) + xcd) & 1);
  const int r8 = lane >> 3, sp8 = lane & 7;
  const u32 lds0 = (u32)(uintptr_t)(LAS char*)lds;
  {
    unsigned long long pc; u32 len;
    asm volatile("s_getpc_b64 %0\n.Lgm_pc:\n\ts_mov_b32 %1, .Lgm_end-.Lgm_pc" : "=s"(pc), "=s"(len));
    const u32 off = min((u32)(wid * 32 + bi) * 128u, len & ~3u);
    asm volatile("s_mov_b32 m0, %0\n\ts_nop 0\n\tglobal_load_lds_dword %1, %2" :: "s"(lds0 + 3 * STG + 1024), "v"(off), "s"(pc) : "memory");
  }
  u32 voE = (u32)(2 * (128 * chunk + r8) + par) * 1152u + (u32)(sp8 ^ (r8 >> 1)) * 16u;
  int jh = 128 * chunk - 8 + r8; jh = jh < 0 ? 0 : jh;
  u32 voH = (u32)(2 * jh + par) * 1152u + (u32)(sp8 ^ (r8 & 6)) * 16u;
  const int u0 = 2 * wid, p0 = (u0 * 52) >> 8, i0 = u0 - 5 * p0, u1 = u0 + 1, p1 = (u1 * 52) >> 8, i1 = u1 - 5 * p1;
  u32 voO = voE ^ 64u, vw0 = wvoff(64 * i0 + lane), vw1 = wvoff(64 * i1 + lane);
  u32 sx = s0 * 128 + wid * 36864, dx = lds0 + wid * 2048;
  u32 sw0 = s0 * (64 * 3456) + p0 * 1152 + hq * 144, dw0 = lds0 + XSTG + p0 * WPART + i0 * 1024;
  u32 sw1 = s0 * (64 * 3456) + p1 * 1152 + hq * 144, dw1 = lds0 + XSTG + p1 * WPART + i1 * 1024;
  asm volatile("" : "+v"(voE), "+v"(voH), "+v"(voO), "+v"(vw0), "+v"(vw1), "+s"(sx), "+s"(dx), "+s"(sw0), "+s"(dw0), "+s"(sw1), "+s"(dw1));
  SBAR();
  const __amdgpu_buffer_rsrc_t rsX = mkrs(x, 2048 * 1152);
  const __amdgpu_buffer_rsrc_t rsW = mkrs((hg >> 3) ? wqb : wqa, 576 * 3456);
  {
    dma16(rsX, voE, sx, dx);
    dma16(rsX, voO, sx + 18432, dx + 1024);
    dma16(rsW, vw0, sw0, dw0);
    if (wid < 7) dma16(rsW, vw1, sw1, dw1);
    else {
      dma16(rsX, voH, s0 * 128, lds0 + 16384);
      const __amdgpu_buffer_rsrc_t rsG = mkrs(gw, 1152);
      dma16(rsG, lane * 16u, 0, lds0 + GWL); dma16(rsG, lane * 16u, 1024, lds0 + GWL + 1024);
    }
  }
  SBAR();
  asm volatile("" :: "s"(woa), "s"(wob), "s"(gw), "s"(out), "s"(ws));
  const int sg = cp >> 2, mi = 16 * (hg >> 2) + 4 * (cp & 3) + (hg & 3);
  const int rg = (mi * 57) >> 9, ct = mi - 9 * rg;
  int rot2 = ct + 2 * (rg & 1) + 4 * (sg & 1); rot2 = rot2 >= 9 ? rot2 - 9 : rot2;
  const int c = lane & 15, g = lane >> 4, q = (lane >> 2) & 3, p = lane & 3;
  const __amdgpu_buffer_rsrc_t rsS = mkrs(ws, WS_BYTES);
  const f32x4 z4 = {0.f, 0.f, 0.f, 0.f};
  u32 nonce = 0, vn = 0;
  const u32 rb = lds0 + XSTG + (8 * g + 2 * q) * WROW + 8 * p;

  if (wid >= 4) {
    __builtin_amdgcn_s_setprio(2);
    const int hw = wid - 4;
    u32 HB = lds0 + (128 + (c & 7)) * 128 + ((g ^ (c & 6)) * 16), HW = rb + WPART + 32 * hw;
    f32x4 hK = z4, hV = z4;
    const u32 ek = lds0 + XCH + (g * 8 + (c & 7)) * 8, ev = lds0 + XCH + 1280 + (lane & 31) * 8;
    WoState wo;
    const __amdgpu_buffer_rsrc_t rsO = mkrs((hw & 2) ? wob : woa, 576 * 1152);
    { const u32 woc = (u32)(ct * 128 + ((sp8 ^ (2 * ((r8 >> 1) & 3))) * 16));
      const u32 voR = (u32)r8 * 1152u + woc, voT = (u32)(64 + 72 * (r8 >> 2) + (r8 & 3)) * 1152u + woc;
#pragma unroll
      for (int m = 0; m < 4; ++m) { wo.vr[m] = voR + m * (9216 - 1024); wo.vt[m] = voT + (144 * (m & 1) + 4 * (m >> 1)) * 1152 - m * 1024; PIN(wo.vr[m]); PIN(wo.vt[m]); }
      wo.dst = lds0 + hw * 4096; wo.kk = rot2; wo.base = (hw & 1) ? 288 * 1152 : 0; }
    if (wid < 7) {
      const int a = hw;
      u32 vA = voE ^ (u32)((a & 1) * 64), vB = vA ^ 64u;
      u32 v5 = a == 0 ? vB + 15u * 18432u : (a == 1 ? voH : 0x80000000u);
      const u32 xso = a * 18432, xd = lds0 + a * 1024, xd5 = lds0 + (a == 0 ? 15 : 16) * 1024;
      u32 vw[9];
      const u32 wso = a * 1152 + hq * 144, wd = lds0 + XSTG + a * WPART;
#pragma unroll
      for (int i = 0; i < 9; ++i) { vw[i] = wvoff(64 * i + lane); PIN(vw[i]); }
      PIN(vA); PIN(vB); PIN(v5); PIN(HB); PIN(HW);
      u32x4 xh = {0u, 0u, 0u, 0u}, fk = xh, fv = xh;
      int st = s0, cs = 0;
#pragma unroll 1
      for (int it = 0; it < 9; ++it) {
        const int sn = nxt9(st), sl = nxtslot(cs);
#pragma unroll
        for (int i = 5; i < 9; ++i) dma16w(rsW, vw[i], st * (64 * 3456) + wso, wd + cs + i * 1024);
        if (it < 8) {
          dma16w(rsX, vA, sn * 128 + xso, xd + sl); dma16w(rsX, vB, sn * 128 + xso + 3 * 18432, xd + sl + 3072); dma16w(rsX, vA, sn * 128 + xso + 6 * 18432, xd + sl + 6144);
        }
        hK = mfma(fk, xh, hK); hV = mfma(xh, fv, hV);
        if (it < 8) wait_vm<7>(); else wait_vm<4>();
        bar();
        xh = lrd128(HB + cs); fk = trd2(HW + cs, WROW); fv = trd2(HW + cs + WPART, WROW);
        if (it < 8) {
#pragma unroll
          for (int i = 0; i < 5; ++i) dma16w(rsW, vw[i], sn * (64 * 3456) + wso, wd + sl + i * 1024);
          dma16w(rsX, vB, sn * 128 + xso + 9 * 18432, xd + sl + 9216); dma16w(rsX, vA, sn * 128 + xso + 12 * 18432, xd + sl + 12288);
          dma16w(rsX, v5, sn * 128, a == 2 ? lds0 + 3 * STG : xd5 + sl);
        } else wo_blocks(rsO, wo, 0, WOA);
        hK = mfma(fk, xh, hK); hV = mfma(xh, fv, hV);
        if (it < 8) wait_vm<11>(); else wait_vm<4 * WOA>();
        bar();
        xh = lrd128((HB ^ 64u) + cs); fk = trd2(HW + cs + 32 * WROW, WROW); fv = trd2(HW + cs + WPART + 32 * WROW, WROW);
        cs = sl; st = sn;
      }
      hK = mfma(fk, xh, hK); hV = mfma(xh, fv, hV);
      if (c >= 8) lwr64f(ek + hw * 256, hK);
      if (g >= 2) lwr64f(ev + hw * 256, hV);
    } else {
      const u32x2 inb0 = __builtin_amdgcn_raw_buffer_load_b64(rsS, INB_OFF + (u32)(sg * 64 + mi) * 512u + lane * 8u, 0, AUX_SC1);
      const u32 non0 = __builtin_amdgcn_raw_buffer_load_b32(rsS, NON_OFF + (u32)(sg * 64 + mi) * 4u, 0, AUX_SC1);
      const u32 BM = rb + (p >= 2 ? WPART + 112 : 128), BV = rb + 2 * WPART + 128;
      const u32 XG = lds0 + (16 * (hg >> 1) + c) * 128 + ((g ^ (c >> 1)) * 16), GG = lds0 + GWL + g * 16;
      f32x4 hM = z4, hV4 = z4, hG = z4;
      wait_vm<2>();
      int cs = 0, st = s0;
#pragma unroll 1
      for (int it = 0; it < 9; ++it) {
#pragma unroll
        for (int k2 = 0; k2 < 2; ++k2) {
          bar();
          const u32 ko = cs + k2 * 32 * WROW;
          const u32x4 xh = lrd128((HB ^ (u32)(64 * k2)) + cs), fk = trd2(HW + ko, WROW), fv = trd2(HW + ko + WPART, WROW), fm = trd2(BM + ko, WROW), f4 = trd2(BV + ko, WROW);
          const u32x4 xg = lrd128((XG ^ (u32)(64 * k2)) + cs), gg = lrd128(GG + st * 128 + k2 * 64);
          hK = mfma(fk, xh, hK); hV = mfma(xh, fv, hV); hM = mfma(fm, xh, hM); hV4 = mfma(xh, f4, hV4); hG = mfma(gg, xg, hG);
          if (k2 == 0 && it == 0) {
            nonce = __builtin_amdgcn_readfirstlane(non0) + 1u;
            while (__ballot(inb0[0] == nonce) != 0ull) ++nonce;
            if (lane == 0) __builtin_amdgcn_raw_buffer_store_b32(nonce, rsS, NON_OFF + (u32)(sg * 64 + mi) * 4u, 0, AUX_SC1);
          }
          if (k2 == 0 && it == 8) {
            vn = __builtin_amdgcn_raw_buffer_load_b32(rsS, NON_OFF + (u32)(sg * 64 + lane) * 4u, 0, AUX_SC1);
            wo_blocks(rsO, wo, 0, WOA);
          }
        }
        cs = nxtslot(cs); st = nxt9(st);
      }
      if (c >= 8) { lwr64f(ek + 3 * 256, hK); if (g >= 2) { lwr64f(ek + 4 * 256, hM); lwr64f(ek + 4 * 256 - 128, hM); } }
      if (g >= 2) { lwr64f(ev + 3 * 256, hV); lwr64f(ev + 4 * 256, hV4); }
      const float gt = __builtin_amdgcn_rcpf(1.f + __builtin_amdgcn_exp2f(-1.4426950408889634f * hG[0]));
      if (r8 == (hg & 1)) __builtin_amdgcn_raw_buffer_store_b32(__float_as_uint(gt), rsS, GATE_OFF + (u32)(cp * 128 + 16 * (hg >> 1) + lane) * 4u, 0, AUX_SC1);
    }
    wo_blocks(rsO, wo, WOA, WOB - WOA);
    lgkm0();
    bar();
    wo_blocks(rsO, wo, WOB, 9 - WOB);
    __builtin_amdgcn_s_setprio(0);
  } else {
    const int w = wid;
    u32 B = rb, BM = rb + (p >= 2 ? WPART + 112 : 128), XB[2];
    XB[0] = lds0 + (32 * w + c) * 128 + ((g ^ (c >> 1)) * 16); XB[1] = XB[0] ^ 64;
    f32x4 bias[2]; u32 mq[5], mP0, mP1;
    {
      const float NINF = -__builtin_inff();
      const bool first = (chunk == 0 && w == 0);
#pragma unroll
      for (int r = 0; r < 4; ++r) {
        const int i = 4 * g + r;
        const bool okc = (i <= c && i >= c - 7), okp = i >= c + 9;
        bias[0][r] = (okc || (okp && !first)) ? 0.f : NINF; bias[1][r] = (okc || okp) ? 0.f : NINF;
      }
      mq[0] = ~0u; mq[1] = g == 0 ? ~0u : 0u; mq[2] = ~mq[1]; mq[3] = g <= 1 ? ~0u : 0u; mq[4] = ~mq[3];
      mP0 = c < 8 ? mq[4] : 0u; mP1 = ~mP0;
      PIN(bias[0]); PIN(bias[1]); PIN(mq[1]); PIN(mq[2]); PIN(mq[3]); PIN(mq[4]); PIN(mP0); PIN(mP1);
    }
    u32 ek = lds0 + XCH + w * XCB + (g * 8 + (c & 7)) * 8, ev = lds0 + XCH + w * XCB + 1280 + (lane & 31) * 8;
    u32 wk = (w < 3 && c >= 8) ? ek + XCB : lds0 + DUM + (lane & 31) * 8, wv_ = (w < 3 && g >= 2) ? ev + XCB : lds0 + DUM + (lane & 31) * 8;
    PIN(ek); PIN(ev); PIN(wk); PIN(wv_);
    u32 so[2], st[2]; u32x4 ones = {0x3f803f80u, 0x3f803f80u, 0x3f803f80u, 0x3f803f80u};
    { const u32 ob = (u32)((cp * 8 + 2 * w) * 36 + 9 * (hg >> 2)) * 1024u;
#pragma unroll
      for (int t = 0; t < 2; ++t) { so[t] = lane * 16u + ob + t * 36864u + (hg & 3) * 2048u; st[t] = ((c * 16u + g * 8u) | (mq[4] & 0x80000000u)) + ob + t * 36864u + 8192u + (hg & 3) * 256u; PIN(so[t]); PIN(st[t]); }
      PIN(ones); }
    f32x4 acc[2][14];
#pragma unroll
    for (int t = 0; t < 2; ++t)
#pragma unroll
      for (int i = 0; i < 14; ++i) { acc[t][i] = z4; PIN(acc[t][i]); }
    PIN(B); PIN(BM); PIN(XB[0]); PIN(XB[1]);
    __builtin_amdgcn_s_waitcnt(0xC07F);
    u32x2 wlo[7], whi[7]; u32x4 xf[2][2];
    auto faddr = [&](auto NC) -> u32 {
      constexpr int n = decltype(NC)::value; constexpr int f = n % 14, ko = (n / 14) * 32 * WROW;
      if constexpr (f < 4) return B + ko + WPART + 32 * f;
      else if constexpr (f == 4) return BM + ko;
      else if constexpr (f < 10) return B + ko + 2 * WPART + 32 * (f - 5);
      else return B + ko + 32 * (f - 10);
    };
    u32x10 Kp[3]; u32 Qp[2][10]; u32x6 Vp[5];
    u32 zr[4] = {0u, 0u, 0u, 0u};
#pragma unroll
    for (int i = 0; i < 4; ++i) PIN(zr[i]);
    auto pack = [&](auto FC, auto TC) {
      constexpr int f = decltype(FC)::value, t = decltype(TC)::value;
      u32 a0 = pk(acc[t][f][0], acc[t][f][1]), a1 = pk(acc[t][f][2], acc[t][f][3]);
      asm volatile("" : "+v"(a0), "+v"(a1));
      if constexpr (f < 4) { Kp[t + 1][2 * f] = a0; Kp[t + 1][2 * f + 1] = a1; }
      else if constexpr (f == 4) {
        auto r0 = __builtin_amdgcn_permlane32_swap(a0, zr[2 * t], false, false); auto r1 = __builtin_amdgcn_permlane32_swap(a1, zr[2 * t + 1], false, false);
        Qp[t][8] = r0[0]; Qp[t][9] = r1[0]; Kp[t + 1][8] = r0[1]; Kp[t + 1][9] = r1[1];
      }
      else if constexpr (f < 10) { Vp[f - 5][2 * t + 2] = a0; Vp[f - 5][2 * t + 3] = a1; }
      else { Qp[t][2 * (f - 10)] = a0; Qp[t][2 * (f - 10) + 1] = a1; }
    };
    wait_vm<0>();
    bar();
    xf[0][0] = lrd128(XB[0]); xf[0][1] = lrd128(XB[0] + 2048);
    sfor<0, PD>([&](auto NC) { constexpr int n = decltype(NC)::value; const u32 a = faddr(NC); wlo[n % 7] = trd(a); whi[n % 7] = trd(a + WROW); SBAR(); });
    auto stage = [&](auto LC, int d) {
      constexpr bool LAST = decltype(LC)::value;
      sfor<0, 28>([&](auto NC) {
        constexpr int n = decltype(NC)::value;
        constexpr int f = n % 14, k2 = n / 14, nn = (n + PD) % 28, kn = nn / 14;
        constexpr bool RD = !(LAST && n + PD >= 28), BI = LAST && k2 == 1;
        constexpr int fx = f - 2;
        if constexpr ((nn == 0 && !LAST) || nn == 14) { SBAR(); bar(); SBAR(); }
        const u32x4 wv = {wlo[n % 7][0], wlo[n % 7][1], whi[n % 7][0], whi[n % 7][1]};
        const u32 a = faddr(IC<nn>{});
        if constexpr (f >= 5 && f < 10) mfma_acc<BI>(acc[0][f], xf[k2][0], wv); else mfma_acc<BI>(acc[0][f], wv, xf[k2][0]);
        SBAR();
        if constexpr (RD) wlo[nn % 7] = trd(a);
        if constexpr (BI && fx >= 0) pack(IC<(fx >= 0 ? fx : 0)>{}, IC<1>{});
        SBAR();
        if constexpr (f >= 5 && f < 10) mfma_acc<BI>(acc[1][f], xf[k2][1], wv); else mfma_acc<BI>(acc[1][f], wv, xf[k2][1]);
        SBAR();
        if constexpr (RD) whi[nn % 7] = trd(a + WROW);
        if constexpr (BI && !RD) pack(IC<2 * (f - 8)>{}, IC<0>{});
        if constexpr (!LAST) {
          if constexpr (nn == 27) { B += d; PIN(B); }
          if constexpr (nn == 19) { BM += d; PIN(BM); }
          if constexpr (nn == 7) { XB[0] += d; PIN(XB[0]); }
          if constexpr (nn == 20) { XB[1] += d; PIN(XB[1]); }
        }
        if constexpr (RD) {
          if constexpr (nn % 14 == 1) xf[kn][0] = lrd128(XB[kn]);
          if constexpr (nn % 14 == 3) xf[kn][1] = lrd128(XB[kn] + 2048);
        }
        SBAR();
      });
    };
    int sl = 0;
#pragma unroll 1
    for (int it = 0; it < 8; ++it) { const int sn = nxtslot(sl), d = sn - sl; sl = sn; stage(IC<0>{}, d); }
    stage(IC<1>{}, 0);
#pragma unroll
    for (int i = 0; i < 5; ++i) lwr64(wk + i * 256, Kp[2][2 * i], Kp[2][2 * i + 1]);
#pragma unroll
    for (int i = 0; i < 5; ++i) lwr64(wv_ + i * 256, Vp[i][4], Vp[i][5]);
    SBAR();
    pack(IC<1>{}, IC<0>{}); pack(IC<3>{}, IC<0>{});
    pack(IC<12>{}, IC<1>{}); pack(IC<13>{}, IC<1>{});
    lgkm0();
    bar();
#pragma unroll
    for (int i = 0; i < 5; ++i) { const u32x2 a = lrd64(ek + i * 256); Kp[0][2 * i] = a[0]; Kp[0][2 * i + 1] = a[1]; }
#pragma unroll
    for (int i = 0; i < 5; ++i) { const u32x2 v = *(volatile LAS u32x2*)(uintptr_t)(ev + i * 256); Vp[i][0] = v[0]; Vp[i][1] = v[1]; }
    PIN(Kp[1]); PIN(Kp[2]);
    SBAR();
    const bool selp = (c < 8) && (g >= 2);
    f32x4 sp[2][4], sc[2][4]; float mx[2][4];
    u32x4 qo[2][4];
    auto mkq = [&](auto TC, auto HC) {
      constexpr int t = decltype(TC)::value, h = decltype(HC)::value;
      constexpr int ia0 = h == 0 ? 0 : (h == 3 ? 4 : 2), ia1 = h <= 1 ? 0 : 2, ib0 = h <= 1 ? 1 : (h == 3 ? 0 : 3), ib1 = h == 0 ? -1 : (h == 3 ? 0 : 1);
      qo[t][h][0] = Qp[t][2 * h] & mq[ia0]; qo[t][h][1] = Qp[t][2 * h + 1] & mq[ia1]; qo[t][h][2] = Qp[t][2 * h + 2] & mq[ib0];
      if constexpr (ib1 >= 0) qo[t][h][3] = Qp[t][2 * h + 3] & mq[ib1 >= 0 ? ib1 : 0]; else qo[t][h][3] = 0u;
      PIN(qo[t][h]);
    };
    mkq(IC<1>{}, IC<0>{});
    SBAR();
    sfor<0, 8>([&](auto IC_) {
      constexpr int j = decltype(IC_)::value, t = 1 - (j >> 2), h = j & 3;
      if constexpr (j == 4) { PIN(Kp[0]); SBAR(); }
      const u32x4 k0 = __builtin_shufflevector(Kp[t], Kp[t], 2 * h, 2 * h + 1, 2 * h + 2, 2 * h + 3);
      const u32x4 k1 = __builtin_shufflevector(Kp[t + 1], Kp[t + 1], 2 * h, 2 * h + 1, 2 * h + 2, 2 * h + 3);
      sc[t][h] = mfma(k1, qo[t][h], bias[t]);
      SBAR();
      if constexpr (j < 7) mkq(IC<1 - ((j + 1) >> 2)>{}, IC<(j + 1) & 3>{});
      SBAR();
      sp[t][h] = mfma(k0, qo[t][h], bias[t]);
      SBAR();
      if constexpr (j < 3) pack(IC<11 + j>{}, IC<0>{});
      else if constexpr (j < 6) pack(IC<5 + 2 * (j - 3)>{}, IC<0>{});
      SBAR();
    });
    if (selp) {
#pragma unroll
      for (int t = 1; t >= 0; --t)
#pragma unroll
        for (int h = 0; h < 4; ++h) { sc[t][h] = sp[t][h]; PIN(sc[t][h]); }
    }
    SBAR();
#pragma unroll
    for (int t = 0; t < 2; ++t)
#pragma unroll
      for (int h = 0; h < 4; ++h) mx[t][h] = fmx(fmx(fmx(sc[t][h][0], sc[t][h][1]), sc[t][h][2]), sc[t][h][3]);
#pragma unroll
    for (int t = 0; t < 2; ++t) {
      float* m = mx[t];
      sw32(m[0], m[1]); float m01 = fmx(m[0], m[1]); sw32(m[2], m[3]); float m23 = fmx(m[2], m[3]);
      sw16(m01, m23); float R = fmx(m01, m23) * -CEXP;
      float a = R, bb = R; sw16(a, bb); m[0] = a; m[1] = a; sw32(m[0], m[1]); m[2] = bb; m[3] = bb; sw32(m[2], m[3]);
    }
    f32x2 ex[2][4][2];
#pragma unroll
    for (int t = 0; t < 2; ++t)
#pragma unroll
      for (int h = 0; h < 4; ++h) {
        const f32x2 cc = {CEXP, CEXP}, mm = {mx[t][h], mx[t][h]};
        ex[t][h][0] = __builtin_elementwise_fma((f32x2){sc[t][h][0], sc[t][h][1]}, cc, mm); ex[t][h][1] = __builtin_elementwise_fma((f32x2){sc[t][h][2], sc[t][h][3]}, cc, mm);
      }
    asm volatile("" : "+v"(Vp[0]), "+v"(Vp[1]), "+v"(Vp[2]), "+v"(Vp[3]), "+v"(Vp[4]));
    SBAR();
    f32x4 olo[2][4], ohi[2][4], sm[2][4];
    float ee[4]; u32 pq[2]; u32x4 po[2], oa[2], ob_[2]; u32x2 oc[2];
    auto eA = [&](auto JC) { constexpr int j = decltype(JC)::value, t = j >> 2, h = j & 3;
      ee[0] = __builtin_amdgcn_exp2f(ex[t][h][0][0]); ee[1] = __builtin_amdgcn_exp2f(ex[t][h][0][1]); ee[2] = __builtin_amdgcn_exp2f(ex[t][h][1][0]);
      asm volatile("" : "+v"(ee[0]), "+v"(ee[1]), "+v"(ee[2])); };
    auto eB = [&](auto JC) { constexpr int j = decltype(JC)::value, t = j >> 2, h = j & 3;
      ee[3] = __builtin_amdgcn_exp2f(ex[t][h][1][1]); pq[0] = pk(ee[0], ee[1]); SBAR(); po[j & 1][0] = pq[0] & mP0; pq[1] = pk(ee[2], ee[3]);
      asm volatile("" : "+v"(pq[0]), "+v"(pq[1])); };
    auto eC = [&](auto JC) { constexpr int j = decltype(JC)::value;
      po[j & 1][2] = pq[0] & mP1; po[j & 1][1] = pq[1] & mP0; po[j & 1][3] = pq[1] & mP1; PIN(po[j & 1]); };
    auto fin = [&](auto TC) {
      constexpr int t = decltype(TC)::value;
      asm volatile("" :: "v"(sm[t][0]), "v"(sm[t][1]), "v"(sm[t][2]), "v"(ohi[t][0]));
      f32x2 ov[10]; float iv[4];
#pragma unroll
      for (int h = 0; h < 4; ++h) iv[h] = __builtin_amdgcn_rcpf(sm[t][h][0]);
#pragma unroll
      for (int h = 0; h < 4; ++h) { ov[2 * h] = (f32x2){olo[t][h][0], olo[t][h][1]} * iv[h]; ov[2 * h + 1] = (f32x2){olo[t][h][2], olo[t][h][3]} * iv[h]; }
      ov[8] = (f32x2){ohi[t][3][0], ohi[t][3][1]} * iv[3]; ov[9] = (f32x2){ohi[t][3][2], ohi[t][3][3]} * iv[3];
#pragma unroll
      for (int i = 0; i < 10; ++i) PIN(ov[i]);
      SBAR();
      if (g < 2) {
        PIN(iv[2]); ov[6] = (f32x2){ohi[t][2][0], ohi[t][2][1]} * iv[2]; PIN(ov[6]);
        if (g == 0) {
          PIN(iv[0]); PIN(iv[1]); PIN(iv[2]);
          ov[2] = (f32x2){ohi[t][0][0], ohi[t][0][1]} * iv[0]; PIN(ov[2]);
          ov[4] = (f32x2){ohi[t][1][0], ohi[t][1][1]} * iv[1]; PIN(ov[4]);
          ov[5] = (f32x2){ohi[t][1][2], ohi[t][1][3]} * iv[1]; PIN(ov[5]);
          ov[7] = (f32x2){ohi[t][2][2], ohi[t][2][3]} * iv[2]; PIN(ov[7]);
        }
      }
      oa[t] = (u32x4){pk(ov[0][0], ov[0][1]), pk(ov[1][0], ov[1][1]), pk(ov[2][0], ov[2][1]), pk(ov[3][0], ov[3][1])};
      __builtin_amdgcn_raw_buffer_store_b128(oa[t], rsS, so[t], 0, AUX_SC1);
      SBAR();
      ob_[t] = (u32x4){pk(ov[4][0], ov[4][1]), pk(ov[5][0], ov[5][1]), pk(ov[6][0], ov[6][1]), pk(ov[7][0], ov[7][1])};
      __builtin_amdgcn_raw_buffer_store_b128(ob_[t], rsS, so[t] + 1024u, 0, AUX_SC1);
      SBAR();
      oc[t] = (u32x2){pk(ov[8][0], ov[8][1]), pk(ov[9][0], ov[9][1])};
      __builtin_amdgcn_raw_buffer_store_b64(oc[t], rsS, st[t], 0, AUX_SC1);
      SBAR();
    };
    eA(IC<0>{}); eB(IC<0>{}); eC(IC<0>{});
    SBAR();
    sfor<0, 8>([&](auto JC) {
      constexpr int j = decltype(JC)::value, t = j >> 2, h = j & 3;
      if constexpr (j < 7) eA(IC<(j < 7 ? j + 1 : 0)>{});
      SBAR();
      sm[t][h] = mfma(ones, po[j & 1], z4);
      SBAR();
      if constexpr (j < 7) eB(IC<(j < 7 ? j + 1 : 0)>{});
      SBAR();
      olo[t][h] = mfma(__builtin_shufflevector(Vp[h], Vp[h], 2 * t, 2 * t + 1, 2 * t + 2, 2 * t + 3), po[j & 1], z4);
      SBAR();
      if constexpr (j < 7) eC(IC<(j < 7 ? j + 1 : 0)>{});
      SBAR();
      ohi[t][h] = mfma(__builtin_shufflevector(Vp[h + 1], Vp[h + 1], 2 * t, 2 * t + 1, 2 * t + 2, 2 * t + 3), po[j & 1], z4);
      SBAR();
      if constexpr (j == 4) { asm volatile("" :: "v"(sm[0][3])); fin(IC<0>{}); }
    });
    fin(IC<1>{});
    asm volatile("" :: "v"(oa[0]), "v"(ob_[0]), "v"(oc[0]), "v"(oa[1]), "v"(ob_[1]));
  }
  const int kq = wid & 3, th = wid >> 2;
  u32 WB[4];
  { const int pos = 4 * g + q, s = (pos >> 1) & 3;
#pragma unroll
    for (int i = 0; i < 4; ++i) { WB[i] = lds0 + (kq + (th ? 20 : 0)) * 4096 + pos * 128 + (((i ^ (2 * (kq & 1))) ^ s) * 32) + p * 8; PIN(WB[i]); } }
  u32 myin = INB_OFF + (u32)(sg * 64 + mi) * 512u + lane * 8u, dst = INB_OFF + (u32)(sg * 64 + lane) * 512u + mi * 8u;
  PIN(myin); PIN(dst);
  const int T0 = 32 * sg + 4 * rg + 2 * th + (kq >> 1), TX = 32 * sg + 28 + rg;
  u32 l16 = lane * 16u; PIN(l16);
  u32 so_k, so_b[3];
  { int r = rot2 + (th ? 5 : 0); r = r >= 9 ? r - 9 : r; so_k = r * 1024u;
    so_b[0] = rg < 4 ? (u32)(TX * 36 + 9 * kq) * 1024u : 0xf0000000u;
    so_b[1] = (u32)((T0 ^ 1) * 36 + 9 * kq) * 1024u; so_b[2] = (u32)(T0 * 36 + 9 * kq) * 1024u;
    asm volatile("" : "+s"(so_k), "+s"(so_b[0]), "+s"(so_b[1]), "+s"(so_b[2])); }
  u32 RW[3];
#pragma unroll
  for (int j = 1; j < 4; ++j) { RW[j - 1] = lds0 + 32768 + ((th * 4 + (kq ^ j)) * 6 + (j - 1) * 2) * 1024 + lane * 16; PIN(RW[j - 1]); }
  wait_vm<0>();
  bar();
  if (wid == 7) __builtin_amdgcn_raw_buffer_store_b64((u32x2){vn, nonce}, rsS, dst, 0, AUX_SC1);
  *(LAS u32*)(uintptr_t)(lds0 + CNT + wid * 4) = 0u;
  u32x4 wf[9][4];
  sfor<0, 9>([&](auto KC) { constexpr int k = decltype(KC)::value;
    if constexpr (k == 4) {
      const u32 d = th ? (u32)(4 - 9) * 16384u : 4u * 16384u;
#pragma unroll
      for (int i = 0; i < 4; ++i) { WB[i] += d; PIN(WB[i]); }
    }
    if constexpr (k == 8) {
#pragma unroll
      for (int i = 0; i < 4; ++i) { WB[i] += 4u * 16384u; PIN(WB[i]); }
    }
#pragma unroll
    for (int i = 0; i < 4; ++i) wf[k][i] = trd2(WB[i] + (k & 3) * 16384, 2048); });
  SBAR();
  bool allok = true;
  if (wid == 7) {
    const int cl = (lane >> 2) & 3;
    const u64 need = __ballot(cl == (rg >> 1) || (rg < 4 && cl == 3));
    u64 got = 0;
#pragma unroll 1
    for (int spin = 0; spin < (1 << 20); ++spin) {
      const u32x2 v = __builtin_amdgcn_raw_buffer_load_b64(rsS, myin, 0, AUX_SC1);
      const bool ok = v[0] == nonce;
      if (ok && v[1] != vn) { vn = v[1]; __builtin_amdgcn_raw_buffer_store_b64((u32x2){vn, nonce}, rsS, dst, 0, AUX_SC1); }
      got = __ballot(ok);
      if ((got & need) == need) break;
      __builtin_amdgcn_s_sleep(1);
    }
    allok = got == ~0ull;
  }
  lgkm0();
  bar();
  if (rg < 7) {
    u32x4 of[OW + 1];
    const u32 so_k0 = so_k;
    auto ldo = [&](auto NC) { constexpr int n = decltype(NC)::value; constexpr int ph = ord_ph(n), k = ord_k(n);
      if constexpr (k == 0) so_k = so_k0;
      of[n % (OW + 1)] = __builtin_amdgcn_raw_buffer_load_b128(rsS, l16, (ph == 0 && k == 4 && th) ? 0xf0000000u : so_b[ph] + so_k, AUX_SC1);
      if constexpr (n + 1 < NL && ord_k(n + 1) == k + 1) so_k = (so_k == 8192u) ? 0u : so_k + 1024u; };
    const int L = 32 * (wid & 1) + (lane >> 1);
    float gm = 0.f, gx = 0.f;
    auto ldg = [&]() {
      gm = __uint_as_float(__builtin_amdgcn_raw_buffer_load_b32(rsS, c * 4u, GATE_OFF + (u32)T0 * 64u, AUX_SC1));
      gx = __uint_as_float(__builtin_amdgcn_raw_buffer_load_b32(rsS, (L & 15) * 4u, GATE_OFF + (u32)(TX & 127) * 64u, AUX_SC1)); };
    sfor<0, OW>([&](auto NC) { ldo(NC); SBAR(); });
    f32x4 ac[3][4], T[2];
    const __amdgpu_buffer_rsrc_t rsD = mkrs(out, 2048 * 1152);
    const int row = 2 * (128 * (T0 >> 4) + 16 * (T0 & 7) + c) + ((T0 >> 3) & 1);
    u32 po = (u32)(row * 576 + ct * 64 + (kq & 1) * 32 + (g & 1) * 16 + (g >> 1) * 8) * 2u;
    const int rowx = 2 * (128 * (TX >> 4) + 16 * (TX & 7) + (L & 15)) + ((TX >> 3) & 1);
    u32 px = (u32)(rowx * 576 + ct * 64 + (wid >> 1) * 16 + (L >> 4) * 4 + (lane & 1) * 2) * 2u;
    u32 ra = lds0 + 32768 + wid * 6144 + l16, rx = lds0 + wid * 512 + lane * 8, wx = (lds0 + wid * 4096 + l16) ^ (u32)((kq & 1) * 2048), wx2 = wx ^ 2048u;
    u32 fw = lds0 + CNT + wid * 4, fg = lds0 + CNT + th * 16, fp = fw ^ 4u, three = 3u;
    PIN(fp); PIN(three);
    PIN(po); PIN(px); PIN(ra); PIN(rx); PIN(wx); PIN(wx2); PIN(fw); PIN(fg);
    auto flag = [&](u32 v) { asm volatile("" ::: "memory"); *(volatile LAS u32*)(uintptr_t)fw = v; };
    float fm = 0.f;
    sfor<0, NL>([&](auto NC) {
      constexpr int n = decltype(NC)::value; constexpr int ph = ord_ph(n), k = ord_k(n);
      if constexpr (n + OW < NL) ldo(IC<n + OW>{});
      if constexpr (n == GL) ldg();
      SBAR();
#pragma unroll
      for (int ii = 0; ii < 4; ++ii) {
        const int i = ii ^ 2;
        if (ph != 0 || k == 0 || (rg < 4 && !(k == 4 && th))) ac[ph][i] = mfma(wf[k][i], of[n % (OW + 1)], k == 0 ? z4 : ac[ph][i]);
        if (n == NL - 1 && ii == 1) SBAR();
      }
      SBAR();
      if constexpr (n >= 10 && n < 14) lwr128(((n - 10) & 2 ? wx2 : wx) + ((n - 10) & 1) * 1024, ac[0][n - 10]);
      if constexpr (n >= 14 && n < 16) { lwr128(RW[n - 13], ac[1][2 * (n - 14)]); lwr128(RW[n - 13] + 1024, ac[1][2 * (n - 14) + 1]); }
      if constexpr (n == 15) flag(2u);
      if constexpr (n == MID) {
#pragma unroll 1
        for (;;) {
          const u32x4 f = *(volatile LAS u32x4*)(uintptr_t)fg;
          asm volatile("" ::: "memory");
#pragma unroll
          for (int t = 0; t < 2; ++t) T[t] = *(LAS f32x4*)(uintptr_t)(ra + 2048 + t * 1024) + *(LAS f32x4*)(uintptr_t)(ra + 4096 + t * 1024);
          asm volatile("" ::: "memory");
          if (__builtin_amdgcn_readfirstlane(f[0] & f[1] & f[2] & f[3]) & 2u) break;
        }
        fm = (kq >> 1) ? 1.f - gm : gm;
        T[0] *= 1.f - fm; T[1] *= 1.f - fm;
        PIN(T[0]); PIN(T[1]); PIN(fm);
      }
      SBAR();
    });
    lwr128(RW[0], ac[2][2]); lwr128(RW[0] + 1024, ac[2][3]);
    flag(three);
    SBAR();
    f32x4 D[2];
#pragma unroll
    for (int t = 0; t < 2; ++t) { D[t] = ac[2][t] * fm + T[t]; PIN(D[t]); }
    {
      f32x4 r[2];
#pragma unroll 1
      for (;;) {
        const u32 f = *(volatile LAS u32*)(uintptr_t)fp;
        asm volatile("" ::: "memory");
#pragma unroll
        for (int t = 0; t < 2; ++t) r[t] = *(LAS f32x4*)(uintptr_t)(ra + t * 1024);
        asm volatile("" ::: "memory");
        if (__builtin_amdgcn_readfirstlane(f) == 3u) break;
      }
      f32x4 v[2];
#pragma unroll
      for (int t = 0; t < 2; ++t) v[t] = r[t] * fm + D[t];
      auto s0_ = __builtin_amdgcn_permlane16_swap(pk(v[0][0], v[0][1]), pk(v[1][0], v[1][1]), false, false);
      auto s1_ = __builtin_amdgcn_permlane16_swap(pk(v[0][2], v[0][3]), pk(v[1][2], v[1][3]), false, false);
      __builtin_amdgcn_raw_buffer_store_b128((u32x4){s0_[0], s1_[0], s0_[1], s1_[1]}, rsD, po, 0, 0);
    }
    if (rg < 4 && th == 0) {
#pragma unroll
      for (int hf = 0; hf < 2; ++hf) {
        f32x2 e[8];
#pragma unroll 1
        for (;;) {
          const u32x4 f = *(volatile LAS u32x4*)(uintptr_t)(fg ^ 16u);
          asm volatile("" ::: "memory");
#pragma unroll
          for (int j = 0; j < 8; ++j) e[j] = *(LAS f32x2*)(uintptr_t)(rx + j * 4096 + hf * 2048);
          asm volatile("" ::: "memory");
          if (__builtin_amdgcn_readfirstlane(f[0] & f[1] & f[2] & f[3]) & 2u) break;
        }
        const f32x2 A = (e[0] + e[1]) + (e[4] + e[5]), Bv = (e[2] + e[3]) + (e[6] + e[7]);
        const f32x2 r = (A - Bv) * gx + Bv;
        __builtin_amdgcn_raw_buffer_store_b32(pk(r[0], r[1]), rsD, px + hf * 64, 0, 0);
      }
    }
  }
  if (!allok) {
#pragma unroll 1
    for (int spin = 0; spin < (1 << 20); ++spin) {
      const u32x2 v = __builtin_amdgcn_raw_buffer_load_b64(rsS, myin, 0, AUX_SC1);
      const bool ok = v[0] == nonce;
      if (ok && v[1] != vn) { vn = v[1]; __builtin_amdgcn_raw_buffer_store_b64((u32x2){vn, nonce}, rsS, dst, 0, AUX_SC1); }
      if (__ballot(ok) == ~0ull) break;
      __builtin_amdgcn_s_sleep(1);
    }
  }
  asm volatile(".Lgm_end:");
}

extern "C" void kernel_launch(void* const* d_in, const int* in_sizes, int n_in, void* d_out, int out_size, void* d_ws, size_t ws_size, hipStream_t stream) {
  const u16* x = (const u16*)d_in[0]; const u16* wqa = (const u16*)d_in[1]; const u16* woa = (const u16*)d_in[2];
  const u16* wqb = (const u16*)d_in[4]; const u16* wob = (const u16*)d_in[5]; const u16* gw = (const u16*)d_in[6];
  (void)hipFuncSetAttribute((const void*)fused, hipFuncAttributeMaxDynamicSharedMemorySize, LDS_BYTES);
  fused<<<256, 512, LDS_BYTES, stream>>>(x, wqa, woa, wqb, wob, gw, (u16*)d_out, (char*)d_ws);
}
```
